```python
import torch

def _softmax(x, axis=-1):
    return torch.softmax(x, dim=axis)
def _gelu(x):
    return torch.nn.functional.gelu(x, approximate="tanh")
def _silu(x):
    return torch.nn.functional.silu(x)
def _sigmoid(x):
    return torch.sigmoid(x)
def _layernorm(x, axis=-1, eps=1e-5):
    m = x.mean(dim=axis, keepdim=True)
    v = x.var(dim=axis, keepdim=True, unbiased=False)
    return (x - m) / torch.sqrt(v + eps)
def _scatter_add(tgt, idx, src, axis=0):
    return tgt.clone().index_add_(axis, idx, src)
def _segment_sum(src, seg, n):
    out = torch.zeros((n,) + src.shape[1:], dtype=src.dtype)
    return out.index_add_(0, seg, src)
def _pad(x, pw):
    flat = tuple(v for pair in reversed(pw) for v in pair)
    return torch.nn.functional.pad(x, flat)

kernel_name = 'gated_mix__softmax_full_mha__mixer_channel_mlp__prompt'

def setup_inputs():

    _w = (lambda a: (lambda t: t.to(torch.bfloat16) if t.is_floating_point() else t)(torch.as_tensor(a)))
    def _A_setup():
        B, S, D = 1, 2048, 576
        return {
            "x": _w((lambda *s: torch.randn(*s, dtype=torch.bfloat16) * 0.05)(B, S, D)),
            "wqkv": _w((lambda *s: torch.randn(*s, dtype=torch.bfloat16) * 0.8)(D, 3 * D)),
            "wo": _w((lambda *s: torch.randn(*s, dtype=torch.bfloat16) * 0.1)(D, D)),
        }
    _A_in = _A_setup()

    def _B_setup():
        B, S, D, F = 1, 2048, 576, 4608
        return {
            "x": _w((lambda *s: torch.randn(*s, dtype=torch.bfloat16) * 0.05)(B, S, D)),
            "w1": _w((lambda *s: torch.randn(*s, dtype=torch.bfloat16) * 1.6)(D, F) * (D ** -0.5)),
            "b1": _w((lambda *s: torch.randn(*s, dtype=torch.bfloat16) * 0.8)(F)),
            "w2": _w((lambda *s: torch.randn(*s, dtype=torch.bfloat16) * 0.05)(F, D) * (F ** -0.5)),
            "b2": _w((lambda *s: torch.randn(*s, dtype=torch.bfloat16) * 0.05)(D)),
        }
    _B_in = _B_setup()

    return {
        "A_x": _A_in["x"],
        "A_wqkv": _A_in["wqkv"],
        "A_wo": _A_in["wo"],
        "B_x": _B_in["x"],
        "B_w1": _B_in["w1"],
        "B_b1": _B_in["b1"],
        "B_w2": _B_in["w2"],
        "B_b2": _B_in["b2"],
        "gate_w": _w((lambda *s: torch.randn(*s, dtype=torch.bfloat16) * 0.05)(576) * 16.0),
    }

def reference(A_x, A_wqkv, A_wo, B_x, B_w1, B_b1, B_w2, B_b2, gate_w):
    def _A_ref(x, wqkv, wo):
        B, S, D = x.shape
        H = 32; Dh = D // H
        qkv = (lambda x, *d: x.permute(*d))((x @ wqkv).reshape(B, S, 3, H, Dh), 2, 0, 3, 1, 4)
        q, k, v = qkv[0], qkv[1], qkv[2]
        scale = Dh ** -0.5
        scores = torch.einsum("bhqd,bhkd->bhqk", q, k) * scale
        p = _softmax(scores, axis=-1)
        o = torch.einsum("bhqk,bhkd->bhqd", p, v)
        return (lambda x, *d: x.permute(*d))(o, 0, 2, 1, 3).reshape(B, S, D) @ wo

    def _B_ref(x, w1, b1, w2, b2):
        y = _layernorm(x, axis=-1)
        h = _gelu(torch.einsum("bsd,df->bsf", y, w1) + b1)
        return torch.einsum("bsf,fd->bsd", h, w2) + b2

    x = A_x
    g = _sigmoid((x * gate_w).sum(-1, keepdims=True))
    a = _A_ref(x, A_wqkv, A_wo)
    b = _B_ref(x, B_w1, B_b1, B_w2, B_b2)
    return g * a + (1 - g) * b
```

```cpp
#include <hip/hip_runtime.h>
#include <hip/hip_bf16.h>
#include <stdint.h>
#include <type_traits>

typedef short bf16x8 __attribute__((ext_vector_type(8)));
typedef short s16x4 __attribute__((ext_vector_type(4)));
typedef float f32x16 __attribute__((ext_vector_type(16)));
typedef float f32x4 __attribute__((ext_vector_type(4)));
typedef unsigned u32x4 __attribute__((ext_vector_type(4)));
typedef unsigned u32x2 __attribute__((ext_vector_type(2)));
typedef __bf16 bf16x2_t __attribute__((ext_vector_type(2)));
#define LAS __attribute__((address_space(3)))
#define GAS __attribute__((address_space(1)))
#define DI __device__ __forceinline__

constexpr int S = 2048, D = 576, H = 32, DH = 18, F = 4608, NQKV = 1728;

constexpr size_t WS_QF = 0;
constexpr size_t WS_Q2 = WS_QF + (size_t)H * 64 * 1024;
constexpr int    KV_GRP = 9472;
constexpr int    KV_HEAD = 16 * KV_GRP;
constexpr size_t WS_KV = WS_Q2 + (size_t)H * S * 4;
constexpr size_t WS_HT = WS_KV + (size_t)H * KV_HEAD;
constexpr size_t WS_OT = WS_HT + (size_t)S * F * 2;
constexpr size_t WS_BP = WS_OT + (size_t)S * D * 2;
constexpr size_t WS_G  = WS_BP + (size_t)2 * S * D * 4;
constexpr size_t WS_END = WS_G + (size_t)S * 4;

#define TS(kid, i)
DI unsigned cvtpk(float lo, float hi) {
  bf16x2_t p; p[0] = (__bf16)lo; p[1] = (__bf16)hi; return __builtin_bit_cast(unsigned, p);
}
DI float dot2(unsigned a, unsigned b, float c) {
  return __builtin_amdgcn_fdot2_f32_bf16(__builtin_bit_cast(bf16x2_t, a), __builtin_bit_cast(bf16x2_t, b), c, false);
}
DI void glds16(const void* g, char* lds_wave_base) {
  const unsigned lb = (unsigned)(uintptr_t)(LAS char*)lds_wave_base;
  unsigned sv;
  asm volatile("s_mov_b32 %0, m0\n\ts_mov_b32 m0, %2\n\ts_nop 0\n\tglobal_load_lds_dwordx4 %1, off\n\ts_mov_b32 m0, %0"
               : "=&s"(sv) : "v"(g), "s"(lb) : "memory");
}
DI s16x4 trread(const char* p) {
  return __builtin_bit_cast(s16x4, __builtin_amdgcn_ds_read_tr16_b64_v4i16((LAS s16x4*)p));
}
DI bf16x8 cat8(s16x4 a, s16x4 b) { return __builtin_shufflevector(a, b, 0, 1, 2, 3, 4, 5, 6, 7); }
#define MFMA32(a, b, c) __builtin_amdgcn_mfma_f32_32x32x16_bf16((a), (b), (c), 0, 0, 0)
#define MFMA16(a, b, c) __builtin_amdgcn_mfma_f32_16x16x32_bf16((a), (b), (c), 0, 0, 0)
#define WAITV(n) asm volatile("s_waitcnt vmcnt(" #n ") lgkmcnt(0)" ::: "memory")
#define BAR() __builtin_amdgcn_s_barrier()
#define WAIT_BAR(n) asm volatile("s_waitcnt vmcnt(" #n ") lgkmcnt(0)\n\ts_barrier" ::: "memory")
typedef const LAS char* lcp;
#define LDS_B128(p) (*(const LAS bf16x8*)(p))
#define SBAR() __builtin_amdgcn_sched_barrier(0)
#define PIN(x) asm volatile("" : "+v"(x))
#define EX2(v) __builtin_amdgcn_exp2f(v)
DI s16x4 trr(lcp p) { return __builtin_bit_cast(s16x4, __builtin_amdgcn_ds_read_tr16_b64_v4i16((LAS s16x4*)p)); }
DI float gelu_f(float x) {
  constexpr float c1 = -2.0f * 0.7978845608028654f * 1.4426950408889634f;
  constexpr float c2 = c1 * 0.044715f;
  float u = x * __builtin_fmaf(x * x, c2, c1);
  float e = __builtin_amdgcn_exp2f(u);
  return x * __builtin_amdgcn_rcpf(1.0f + e);
}

constexpr int K1_CH = 24576;
constexpr int K1_Y0 = 3 * K1_CH;
constexpr int K1_YH = 4 * 18 * 1088;
constexpr int K1_MISC = K1_Y0 + K1_YH;
constexpr int K1_LDS = K1_MISC + 512 + 1536;

template <bool ISG1>
DI void k1_body(const uint16_t* __restrict__ x, const uint16_t* __restrict__ W, const uint16_t* __restrict__ b1,
                const uint16_t* __restrict__ gate_w, char* __restrict__ ws, char* smem, int grp, int rb) {
  const int tid = threadIdx.x, lane = tid & 63;
  const int wid = __builtin_amdgcn_readfirstlane(tid >> 6);
  constexpr int ldw = ISG1 ? F : NQKV;
  const int step0 = ISG1 ? grp * 6 : grp * 7;
  const int nsteps = ISG1 ? 6 : ((grp == 3) ? 6 : 7);
  const int nch = nsteps * 3;
  const int wm = wid & 3, wn = wid >> 2;
  const int hh = lane >> 5, l31 = lane & 31;

  TS(0, 0);
  const int rr = lane >> 2, cc = lane & 3;
  const int Rloc = wid * 16 + rr;
  const uint16_t* xrow = x + (size_t)(rb * 128 + Rloc) * D + cc * 8;
  u32x4 xv[18];
#pragma unroll
  for (int i = 0; i < 18; ++i) xv[i] = *(const u32x4*)(xrow + i * 32);
  SBAR();

  const uint16_t* bsrc[3];
#pragma unroll
  for (int j = 0; j < 3; ++j) {
    int n = wid + 8 * j; int wn_ = n / 12, row = 16 * (n % 12) + (lane >> 2);
    bsrc[j] = W + (size_t)row * ldw + step0 * 64 + wn_ * 32 + (lane & 3) * 8;
  }
  auto issue_chunk = [&](int c) {
    int st = c / 3, kc = c - st * 3;
    size_t off = (size_t)kc * 192 * ldw + st * 64;
    char* dst = smem + (c & 3) * K1_CH + wid * 1024;
#pragma unroll
    for (int j = 0; j < 3; ++j) glds16(bsrc[j] + off, dst + j * 8192);
  };
  issue_chunk(0); issue_chunk(1); issue_chunk(2);

  float* rstd_l = (float*)(smem + K1_MISC);
  float* bias_l = rstd_l + 128;
  float rs = 1.0f;
  if (ISG1) {
    if (tid < 384) bias_l[tid] = __uint_as_float((unsigned)b1[step0 * 64 + tid] << 16);
    float xf[144];
    float s0 = 0.f, s1 = 0.f;
#pragma unroll
    for (int i = 0; i < 18; ++i)
#pragma unroll
      for (int e = 0; e < 4; ++e) {
        const float lo = __uint_as_float(xv[i][e] << 16), hi = __uint_as_float(xv[i][e] & 0xffff0000u);
        xf[(i * 4 + e) * 2] = lo; xf[(i * 4 + e) * 2 + 1] = hi;
        s0 += lo; s1 += hi;
      }
    float sum = s0 + s1;
    sum += __shfl_xor(sum, 1); sum += __shfl_xor(sum, 2);
    const float nm = -sum * (1.0f / D);
    if (grp == 0) {
      float gs = 0.f;
#pragma unroll
      for (int i = 0; i < 18; ++i) {
        u32x4 gw = *(const u32x4*)(gate_w + cc * 8 + i * 32);
#pragma unroll
        for (int e = 0; e < 4; ++e) gs = dot2(xv[i][e], gw[e], gs);
      }
      gs += __shfl_xor(gs, 1); gs += __shfl_xor(gs, 2);
      if (cc == 0) ((float*)(ws + WS_G))[rb * 128 + Rloc] = 1.0f / (1.0f + __expf(-gs));
    }
    float q0 = 0.f, q1 = 0.f;
#pragma unroll
    for (int i = 0; i < 18; ++i)
#pragma unroll
      for (int e = 0; e < 4; ++e) {
        const float lo = xf[(i * 4 + e) * 2] + nm, hi = xf[(i * 4 + e) * 2 + 1] + nm;
        q0 = __builtin_fmaf(lo, lo, q0); q1 = __builtin_fmaf(hi, hi, q1);
        xv[i][e] = cvtpk(lo, hi);
      }
    float ss = q0 + q1;
    ss += __shfl_xor(ss, 1); ss += __shfl_xor(ss, 2);
    if (cc == 0) rstd_l[Rloc] = __builtin_amdgcn_rsqf(ss * (1.0f / D) + 1e-5f);
  }
  TS(0, 1);
  bf16x8 xa[36];
  {
    char* ybase = smem + K1_Y0;
    char* wr = ybase + (Rloc >> 5) * (18 * 1088) + (cc >> 1) * 1088 + (cc & 1) * 544 + (Rloc & 31) * 16;
    const char* rd = ybase + wm * (18 * 1088) + hh * 544 + l31 * 16;
#pragma unroll
    for (int half = 0; half < 2; ++half) {
#pragma unroll
      for (int i = 0; i < 9; ++i) *(u32x4*)(wr + i * 2 * 1088) = xv[half * 9 + i];
      asm volatile("s_waitcnt lgkmcnt(0)\n\ts_barrier" ::: "memory");
#pragma unroll
      for (int ks = 0; ks < 18; ++ks) xa[half * 18 + ks] = *(const bf16x8*)(rd + ks * 1088);
      if (half == 0) asm volatile("s_waitcnt lgkmcnt(0)\n\ts_barrier" ::: "memory");
    }
  }
  if (ISG1) rs = rstd_l[wm * 32 + l31];
  TS(0, 2);

  const int i16 = lane & 15, G = lane >> 4;
  const lcp tb = (lcp)smem + wn * 12288 + (8 * (G >> 1) + (i16 >> 2)) * 64 + (16 * (G & 1) + 4 * (i16 & 3)) * 2;
  const int T = rb * 128 + wm * 32 + l31;
  const bool grpA = wn == 0;

  f32x16 acc, z;
  u32x4 pk;
  bf16x8 wf[3];
  int colP = 0;
  bool pendV = false;

  auto epi = [&](auto KC) {
    constexpr int kc = decltype(KC)::value;
    constexpr int p0 = kc * 3, p1 = kc == 2 ? 8 : p0 + 3;
    if (ISG1) {
      char* hb = ws + WS_HT + ((size_t)(T >> 5) * 288 + (colP >> 4)) * 1024 + lane * 16;
#pragma unroll
      for (int p = p0; p < p1; ++p) {
        pk[p & 3] = cvtpk(gelu_f(z[2 * p]), gelu_f(z[2 * p + 1]));
        if ((p & 3) == 3) *(u32x4*)(hb + (p >> 2) * 1024) = pk;
      }
    } else if (pendV) {
      if (kc < 2) {
        const int cv = colP + l31 - 1152, head = cv / DH, d = cv - head * DH;
        char* vb = ws + WS_KV + (size_t)head * KV_HEAD + rb * KV_GRP + 4608 + (wm * 4 + hh) * 304 + d * 16 + kc * 608;
        u32x4 pv = {cvtpk(z[8 * kc + 0], z[8 * kc + 1]), cvtpk(z[8 * kc + 2], z[8 * kc + 3]),
                    cvtpk(z[8 * kc + 4], z[8 * kc + 5]), cvtpk(z[8 * kc + 6], z[8 * kc + 7])};
        *(u32x4*)(vb) = pv;
        if (d == 17) { u32x4 one = {0x3f803f80u, 0x3f803f80u, 0x3f803f80u, 0x3f803f80u}; *(u32x4*)(vb + 16) = one; }
      }
    } else {
      const bool isK = colP >= 576;
      const int cbase = colP - (isK ? 576 : 0) + 4 * hh;
#pragma unroll
      for (int pp = p0; pp < p1; ++pp) {
        const int i = pp >> 1, u = pp & 1;
        const int c2 = cbase + 8 * i + 2 * u, head = c2 / DH, p = (c2 - head * DH) >> 1;
        const unsigned v = cvtpk(z[2 * pp], z[2 * pp + 1]);
        size_t off;
        if (isK) {
          off = WS_KV + (size_t)head * KV_HEAD + rb * KV_GRP +
                (p < 8 ? wm * 1024 + ((p >> 2) * 32 + l31) * 16 + (p & 3) * 4 : 4096 + l31 * 16 + wm * 4);
        } else {
          off = p < 8 ? WS_QF + ((size_t)(head * 64 + (T >> 5)) * 64 + (p >> 2) * 32 + l31) * 16 + (p & 3) * 4
                      : WS_Q2 + ((size_t)head * S + T) * 4;
        }
        *(unsigned*)(ws + off) = v;
      }
    }
  };
#define K1RD(dst, base, ks) dst = cat8(trr((base) + (ks) * 1024), trr((base) + (ks) * 1024 + 256))
  auto run = [&](auto KC, auto SW, int c) {
    constexpr int kc = decltype(KC)::value;
    constexpr bool sw = decltype(SW)::value;
    const lcp cur = tb + (c & 3) * K1_CH, nxt = tb + ((c + 1) & 3) * K1_CH;
#pragma unroll
    for (int ks = 0; ks < 12; ++ks) {
      if (ks + 2 < 12) { K1RD(wf[(ks + 2) % 3], cur, ks + 2); } else { K1RD(wf[(ks + 2) % 3], nxt, ks - 10); }
      if (kc == 0 && ks == 0) {
        const f32x16 zero16 = {};
        acc = sw ? MFMA32(xa[0], wf[0], zero16) : MFMA32(wf[0], xa[0], zero16);
      } else {
        acc = sw ? MFMA32(xa[kc * 12 + ks], wf[ks % 3], acc) : MFMA32(wf[ks % 3], xa[kc * 12 + ks], acc);
      }
      SBAR();
    }
  };
  std::integral_constant<bool, false> Fc; std::integral_constant<bool, true> Tc;

  WAIT_BAR(3);
  K1RD(wf[0], tb, 0); K1RD(wf[1], tb, 1);
#pragma unroll 1
  for (int st = 0; st < nsteps; ++st) {
    const int col0 = (step0 + st) * 64 + wn * 32;
    const bool isV = (!ISG1) && (col0 >= 1152);
    auto chunk = [&](auto KC) {
      constexpr int kc = decltype(KC)::value;
      const int c = st * 3 + kc;
      if (c > 0) { if (c + 2 < nch) WAIT_BAR(3); else WAIT_BAR(0); }
      if (c + 3 < nch) issue_chunk(c + 3);
      if (grpA && st > 0) epi(KC);
      SBAR();
      if (isV) run(KC, Tc, c); else run(KC, Fc, c);
      if (!grpA && st > 0) epi(KC);
    };
    chunk(std::integral_constant<int, 0>{});
    chunk(std::integral_constant<int, 1>{});
    chunk(std::integral_constant<int, 2>{});
    if (st < 2) TS(0, 3 + st);
    if (ISG1) {
      const float* bl = bias_l + st * 64 + wn * 32 + 4 * hh;
#pragma unroll
      for (int i = 0; i < 4; ++i) {
        const f32x4 bv = *(const f32x4*)(bl + 8 * i);
#pragma unroll
        for (int e = 0; e < 4; ++e) z[4 * i + e] = __builtin_fmaf(acc[4 * i + e], rs, bv[e]);
      }
    } else {
      const float sc = col0 >= 576 ? 1.0f : 0.23570226039551584f * 1.4426950408889634f;
#pragma unroll
      for (int r = 0; r < 16; ++r) z[r] = acc[r] * sc;
    }
    colP = col0; pendV = isV;
  }
  TS(0, 5);
  epi(std::integral_constant<int, 0>{});
  epi(std::integral_constant<int, 1>{});
  epi(std::integral_constant<int, 2>{});
  TS(0, 6);
}

__global__ __launch_bounds__(512, 2) void k1_kernel(const uint16_t* __restrict__ x, const uint16_t* __restrict__ wqkv,
                                                    const uint16_t* __restrict__ w1, const uint16_t* __restrict__ b1,
                                                    const uint16_t* __restrict__ gate_w, char* __restrict__ ws) {
  extern __shared__ __attribute__((aligned(16))) char smem[];
  const int b = blockIdx.x, xcd = b & 7, idx = b >> 3;
  const int cg = xcd & 3, rb = (xcd >> 2) * 8 + (idx & 7), sub = idx >> 3;
  if (sub < 3) k1_body<true>(x, w1, b1, gate_w, ws, smem, cg * 3 + sub, rb);
  else k1_body<false>(x, wqkv, b1, gate_w, ws, smem, cg, rb);
}

constexpr int AT_NS = 6;
constexpr int AT_LDS = AT_NS * KV_GRP + 16;
constexpr float AT_THR = 16.0f;
DI float swapmax32(float v) {
  auto rr = __builtin_amdgcn_permlane32_swap(__float_as_uint(v), __float_as_uint(v), false, false);
  return __builtin_fmaxf(__uint_as_float(rr[0]), __uint_as_float(rr[1]));
}

__global__ __launch_bounds__(512, 2) void attn_kernel(char* __restrict__ ws) {
  extern __shared__ __attribute__((aligned(16))) char smem[];
  const int tid = threadIdx.x, lane = tid & 63;
  const int wid = __builtin_amdgcn_readfirstlane(tid >> 6);
  const int b = blockIdx.x, head = (b & 7) * 4 + ((b >> 3) & 3), qc = b >> 5;
  const int hh = lane >> 5, l31 = lane & 31;
  const int qtile = qc * 8 + wid, T = qtile * 32 + l31;
  const char* kv = ws + WS_KV + (size_t)head * KV_HEAD;
  const char* srcA = kv + wid * 1024 + lane * 16;
  const char* srcB = kv + 8192 + wid * 160 + (lane < 10 ? lane : 0) * 16;
  auto issue_grp = [&](int g, int slot) {
    char* dst = smem + slot * KV_GRP;
    glds16(srcA + g * KV_GRP, dst + wid * 1024);
    if (lane < 10) glds16(srcB + g * KV_GRP, dst + 8192 + wid * 160);
  };
  TS(1, 0);
#pragma unroll
  for (int g = 0; g < AT_NS - 2; ++g) issue_grp(g, g);
  const bf16x8 q1 = *(const bf16x8*)(ws + WS_QF + ((size_t)(head * 64 + qtile) * 64 + lane) * 16);
  const unsigned q2v = *(const unsigned*)(ws + WS_Q2 + ((size_t)head * S + T) * 4);
  volatile LAS unsigned* flag = (volatile LAS unsigned*)((LAS char*)smem + AT_NS * KV_GRP);
  if (tid == 0) *flag = 0u;

  const int dmap = l31 < 19 ? l31 : ((l31 >= 20 && l31 <= 27) ? (l31 & 3) : 18);
  const lcp L0 = (lcp)smem;
  const int koff = lane * 16;
  const int k2off = hh ? (4608 + 18 * 16) : (4096 + l31 * 16);
  const int voff = 4608 + hh * 304 + dmap * 16;
  const f32x16 zero16 = {};
  f32x16 o = {};
  u32x4 q2[4];
#pragma unroll
  for (int t = 0; t < 4; ++t) { u32x4 z = {0u, 0u, 0u, 0u}; z[t] = hh ? 0u : q2v; q2[t] = z; }

  asm volatile("s_waitcnt vmcnt(6) lgkmcnt(0)\n\ts_barrier" ::: "memory");
  issue_grp(AT_NS - 2, AT_NS - 2);
  TS(1, 1);
  bf16x8 kf = LDS_B128(L0 + koff), k2f = LDS_B128(L0 + k2off);
  f32x16 sA, sB;
  {
    f32x16 s = MFMA32(kf, q1, zero16);
    s = MFMA32(k2f, __builtin_bit_cast(bf16x8, q2[0]), s);
    float mx = __builtin_fmaxf(__builtin_fmaxf(s[0], s[1]), s[2]);
#pragma unroll
    for (int r = 3; r < 15; r += 2) mx = __builtin_fmaxf(__builtin_fmaxf(mx, s[r]), s[r + 1]);
    mx = swapmax32(__builtin_fmaxf(mx, s[15]));
    const unsigned nm = cvtpk(-mx, 0.f);
    if (hh) {
#pragma unroll
      for (int t = 0; t < 4; ++t) q2[t][0] = nm;
    }
    sA = MFMA32(kf, q1, zero16);
    sA = MFMA32(k2f, __builtin_bit_cast(bf16x8, q2[0]), sA);
  }
  kf = LDS_B128(L0 + koff + 1024);
  u32x4 p0, p1 = {0u, 0u, 0u, 0u};
  bf16x8 v0, v1 = LDS_B128(L0 + voff);
  int gcur = 0;
  int gnxt = KV_GRP;
  int slot_free = AT_NS - 1;

#define PKT(lo, hi) cvtpk(lo, hi)
#define EXP4(SC, i, PW, j) do { SC[i] = EX2(SC[i]); SC[i + 1] = EX2(SC[i + 1]); SC[i + 2] = EX2(SC[i + 2]); SC[i + 3] = EX2(SC[i + 3]); \
    PW[j] = PKT(SC[i], SC[i + 1]); PW[j + 1] = PKT(SC[i + 2], SC[i + 3]); PIN(PW); } while (0)
#define STEP(SC, SN, TI) do { \
    const lcp gc_ = L0 + gcur, gn_ = L0 + gnxt; \
    o = MFMA32(v1, __builtin_bit_cast(bf16x8, p1), o); \
    v0 = LDS_B128(gc_ + voff + TI * 1216); \
    EXP4(SC, 0, p0, 0); SBAR(); \
    SN = MFMA32(kf, q1, zero16); \
    v1 = LDS_B128(gc_ + voff + TI * 1216 + 608); \
    EXP4(SC, 4, p0, 2); SBAR(); \
    SN = MFMA32(k2f, __builtin_bit_cast(bf16x8, q2[(TI + 1) & 3]), SN); \
    kf = LDS_B128((TI >= 2 ? gn_ : gc_) + koff + ((TI + 2) & 3) * 1024); \
    EXP4(SC, 8, p1, 0); SBAR(); \
    o = MFMA32(v0, __builtin_bit_cast(bf16x8, p0), o); \
    if (TI == 2) k2f = LDS_B128(gn_ + k2off); \
    EXP4(SC, 12, p1, 2); SBAR(); \
  } while (0)

#pragma unroll 1
  for (int j = 0; j < 16; ++j) {
    STEP(sA, sB, 0);
    STEP(sB, sA, 1);
    if (j < 15) {
      if (j + AT_NS - 2 < 16) asm volatile("s_waitcnt vmcnt(6) lgkmcnt(0)\n\ts_barrier" ::: "memory");
      else asm volatile("s_waitcnt vmcnt(0) lgkmcnt(0)\n\ts_barrier" ::: "memory");
      if (j + AT_NS - 1 < 16) issue_grp(j + AT_NS - 1, slot_free);
      slot_free = slot_free == AT_NS - 1 ? 0 : slot_free + 1;
    }
    STEP(sA, sB, 2);
    STEP(sB, sA, 3);
    gcur = gnxt;
    gnxt = gnxt == (AT_NS - 1) * KV_GRP ? 0 : gnxt + KV_GRP;
  }
  o = MFMA32(v1, __builtin_bit_cast(bf16x8, p1), o);
  TS(1, 2);
  {
    bool bad = !(o[10] < 1.8446744073709552e19f) && !hh;
#pragma unroll
    for (int r = 0; r < 10; ++r) bad = bad || !(__builtin_fabsf(o[r]) < 3.0e38f);
    if (__any(bad) && lane == 0) *flag = 1u;
    asm volatile("s_waitcnt vmcnt(0) lgkmcnt(0)\n\ts_barrier" ::: "memory");
    const unsigned fl = __builtin_amdgcn_readfirstlane(*flag);
    if (__builtin_expect(fl != 0u, 0)) {
      f32x16 negm = {};
      float m = 0.f;
      o = zero16;
#pragma unroll
      for (int t = 0; t < 4; ++t) { u32x4 z = {0u, 0u, 0u, 0u}; z[t] = hh ? 0u : q2v; q2[t] = z; }
#pragma unroll 1
      for (int g = 0; g < 16; ++g) {
        asm volatile("s_waitcnt lgkmcnt(0)\n\ts_barrier" ::: "memory");
        issue_grp(g, 0);
        asm volatile("s_waitcnt vmcnt(0)\n\ts_barrier" ::: "memory");
        const bf16x8 k2s = LDS_B128(L0 + k2off);
#pragma unroll
        for (int ti = 0; ti < 4; ++ti) {
          const bf16x8 ks = LDS_B128(L0 + koff + ti * 1024);
          f32x16 s = MFMA32(ks, q1, negm);
          s = MFMA32(k2s, __builtin_bit_cast(bf16x8, q2[ti]), s);
          float mx = __builtin_fmaxf(__builtin_fmaxf(s[0], s[1]), s[2]);
#pragma unroll
          for (int r = 3; r < 15; r += 2) mx = __builtin_fmaxf(__builtin_fmaxf(mx, s[r]), s[r + 1]);
          mx = __builtin_fmaxf(mx, s[15]);
          const bool first = (g == 0 && ti == 0);
          if (first || __any(mx > AT_THR)) {
            float dl = swapmax32(mx);
            if (!first) dl = __builtin_fmaxf(dl, 0.f);
            m += dl;
            const float f = EX2(-dl);
#pragma unroll
            for (int r = 0; r < 16; ++r) { s[r] -= dl; negm[r] = -m; }
            if (!first) {
#pragma unroll
              for (int r = 0; r < 16; ++r) o[r] *= f;
            }
          }
          u32x4 pa, pb;
#pragma unroll
          for (int r = 0; r < 4; ++r) {
            pa[r] = cvtpk(EX2(s[2 * r]), EX2(s[2 * r + 1]));
            pb[r] = cvtpk(EX2(s[8 + 2 * r]), EX2(s[8 + 2 * r + 1]));
          }
          const bf16x8 va = LDS_B128(L0 + voff + ti * 1216);
          const bf16x8 vb = LDS_B128(L0 + voff + ti * 1216 + 608);
          o = MFMA32(va, __builtin_bit_cast(bf16x8, pa), o);
          o = MFMA32(vb, __builtin_bit_cast(bf16x8, pb), o);
        }
      }
    }
  }
  const float l = __shfl(o[10], l31);
  const float rl = 1.0f / l;
  char* ob = ws + WS_OT + (size_t)(T >> 5) * (36 * 1024) + l31 * 16;
#pragma unroll
  for (int u = 0; u < 5; ++u) {
    const int reg = 2 * u, d = (reg & 3) + 8 * (reg >> 2) + 4 * hh;
    if (d < 18) {
      const int k = head * DH + d, kk = k & 15, j = (kk & 3) + 4 * (kk >> 3);
      *(unsigned*)(ob + (k >> 4) * 1024 + ((kk >> 2) & 1) * 512 + j * 2) = cvtpk(o[reg] * rl, o[reg + 1] * rl);
    }
  }
  TS(1, 3);
}

constexpr int G2_RING = 24576;
constexpr int G2_LDS = 4 * G2_RING;
#define GLOAD(dst, p) asm volatile("global_load_dwordx4 %0, %1, off" : "=v"(dst) : "v"(p) : "memory")

struct G2Plan { int tile, blk0, nblk, part, gfirst, gsize; };
DI G2Plan g2_plan(int wg, int w) {
  const int quad = wg >> 2, wq = (wg & 3) * 4 + w;
  const int tl = wq < 5 ? 0 : (wq < 11 ? 1 : 2);
  const int pos = wq - (tl == 0 ? 0 : (tl == 1 ? 5 : 11));
  G2Plan p;
  p.tile = quad * 3 + tl;
  if (tl == 1) { p.blk0 = pos * 8; p.nblk = 8; }
  else { p.blk0 = pos < 3 ? pos * 10 : 30 + (pos - 3) * 9; p.nblk = pos < 3 ? 10 : 9; }
  const int sub = wg & 3;
  if (sub == 0 || sub == 3) { p.gfirst = 0; p.gsize = 4; p.part = sub == 0 ? 0 : 1; }
  else if (sub == 1) { if (w == 0) { p.gfirst = 0; p.gsize = 1; p.part = 1; } else { p.gfirst = 1; p.gsize = 3; p.part = 0; } }
  else { if (w < 3) { p.gfirst = 0; p.gsize = 3; p.part = 1; } else { p.gfirst = 3; p.gsize = 1; p.part = 0; } }
  return p;
}

__global__ __launch_bounds__(256, 2) void g2_kernel(const uint16_t* __restrict__ w2, char* __restrict__ ws) {
  extern __shared__ __attribute__((aligned(16))) char smem[];
  const int tid = threadIdx.x, lane = tid & 63;
  const int wid = __builtin_amdgcn_readfirstlane(tid >> 6);
  const int b = blockIdx.x;
  const G2Plan pl = g2_plan((b & 7) * 32 + (b >> 3), wid);
  const int mb = pl.tile / 6, nb = pl.tile - mb * 6;
  const int s0 = pl.blk0 * 6;
  TS(2, 0);
  char* ring = smem + wid * G2_RING;
  const char* asrc = ws + WS_HT + ((size_t)(mb * 2) * 288 + s0) * 1024 + lane * 16;
  const char* bsrc[3];
#pragma unroll
  for (int j = 0; j < 3; ++j) {
    const int o = j * 1024 + lane * 16, row = o / 192, cb = o - row * 192;
    bsrc[j] = (const char*)w2 + (size_t)(s0 * 16 + row) * (D * 2) + nb * 192 + cb;
  }
  const int slast = pl.nblk * 6 - 1;
  u32x4 hf[6][2];
  auto issue = [&](int i, int r) {
    const int rc = r < slast ? r : slast;
    GLOAD(hf[i][0], asrc + (size_t)rc * 1024);
    GLOAD(hf[i][1], asrc + (size_t)rc * 1024 + 288 * 1024);
    char* dst = ring + (r & 7) * 3072;
#pragma unroll
    for (int j = 0; j < 3; ++j) glds16(bsrc[j] + (size_t)rc * (16 * D * 2), dst + j * 1024);
  };
#pragma unroll
  for (int i = 0; i < 6; ++i) issue(i, i);
  const int i16 = lane & 15, G = lane >> 4;
  const lcp tb = (lcp)ring + (4 * (G >> 1) + (i16 >> 2)) * 192 + (16 * (G & 1) + 4 * (i16 & 3)) * 2;
  f32x16 acc[2][3];
#pragma unroll
  for (int i = 0; i < 2; ++i)
#pragma unroll
    for (int j = 0; j < 3; ++j) acc[i][j] = f32x16{};
#pragma unroll 1
  for (int blk = 0; blk < pl.nblk; ++blk) {
#pragma unroll
    for (int i = 0; i < 6; ++i) {
      const int r = blk * 6 + i;
      asm volatile("s_waitcnt vmcnt(25)" : "+v"(hf[i][0]), "+v"(hf[i][1]) :: "memory");
      const lcp sb = tb + (r & 7) * 3072;
      bf16x8 wf[3];
#pragma unroll
      for (int j = 0; j < 3; ++j) wf[j] = cat8(trr(sb + j * 64), trr(sb + j * 64 + 8 * 192));
      const bf16x8 a0 = __builtin_bit_cast(bf16x8, hf[i][0]), a1 = __builtin_bit_cast(bf16x8, hf[i][1]);
#pragma unroll
      for (int j = 0; j < 3; ++j) {
        acc[0][j] = MFMA32(wf[j], a0, acc[0][j]);
        acc[1][j] = MFMA32(wf[j], a1, acc[1][j]);
      }
      SBAR();
      issue(i, r + 6);
      SBAR();
    }
  }
  asm volatile("s_waitcnt vmcnt(0) lgkmcnt(0)"
               : "+v"(hf[0][0]), "+v"(hf[0][1]), "+v"(hf[1][0]), "+v"(hf[1][1]), "+v"(hf[2][0]), "+v"(hf[2][1]),
                 "+v"(hf[3][0]), "+v"(hf[3][1]), "+v"(hf[4][0]), "+v"(hf[4][1]), "+v"(hf[5][0]), "+v"(hf[5][1]) :: "memory");
  TS(2, 1);
  {
    f32x4* red = (f32x4*)ring + lane;
#pragma unroll
    for (int i = 0; i < 2; ++i)
#pragma unroll
      for (int j = 0; j < 3; ++j)
#pragma unroll
        for (int q = 0; q < 4; ++q) {
          f32x4 v = {acc[i][j][4 * q], acc[i][j][4 * q + 1], acc[i][j][4 * q + 2], acc[i][j][4 * q + 3]};
          red[((i * 3 + j) * 4 + q) * 64] = v;
        }
  }
  asm volatile("s_waitcnt lgkmcnt(0)\n\ts_barrier" ::: "memory");
  {
    f32x4* bp = (f32x4*)(ws + WS_BP) + ((size_t)pl.part * 192 + pl.tile) * (24 * 64) + lane;
    const f32x4* red = (const f32x4*)(smem + pl.gfirst * G2_RING) + lane;
    for (int u = wid - pl.gfirst; u < 24; u += pl.gsize) {
      f32x4 v = red[u * 64];
      for (int m = 1; m < pl.gsize; ++m) v += red[m * (G2_RING / 16) + u * 64];
      bp[u * 64] = v;
    }
  }
  TS(2, 2);
}

constexpr int K3_LDS = 576 * 192;

__global__ __launch_bounds__(384, 2) void k3_kernel(const uint16_t* __restrict__ wo, const uint16_t* __restrict__ b2,
                                                    char* __restrict__ ws, uint16_t* __restrict__ out) {
  extern __shared__ __attribute__((aligned(16))) char smem[];
  const int tid = threadIdx.x, lane = tid & 63;
  const int wid = __builtin_amdgcn_readfirstlane(tid >> 6);
  const int b = blockIdx.x, tile = (b & 7) * 24 + (b >> 3), mb = tile / 6, nb = tile - mb * 6;
  const int mt = wid / 3, nt = wid - mt * 3;
  const int hh = lane >> 5, l31 = lane & 31;
  TS(3, 0);
#pragma unroll
  for (int j = 0; j < 18; ++j) {
    const int n = j * 6 + wid;
    const int o = n * 1024 + lane * 16, row = o / 192, cb = o - row * 192;
    glds16((const char*)wo + (size_t)row * (D * 2) + nb * 192 + cb, smem + n * 1024);
  }
  const int T = mb * 64 + mt * 32 + l31;
  bf16x8 oa[36];
  const char* op = ws + WS_OT + (size_t)(mb * 2 + mt) * (36 * 1024) + lane * 16;
#pragma unroll
  for (int ks = 0; ks < 36; ++ks) oa[ks] = *(const bf16x8*)(op + ks * 1024);
  const float gt = ((const float*)(ws + WS_G))[T];
  const f32x4* bp = (const f32x4*)(ws + WS_BP) + ((size_t)tile * 24 + wid * 4) * 64 + lane;
  f32x4 pb[4];
  u32x2 bb2[4];
#pragma unroll
  for (int q = 0; q < 4; ++q) {
    pb[q] = bp[q * 64] + bp[(size_t)192 * 24 * 64 + q * 64];
    bb2[q] = *(const u32x2*)(b2 + nb * 96 + nt * 32 + 8 * q + 4 * hh);
  }
  WAIT_BAR(0);
  TS(3, 1);
  const int i16 = lane & 15, G = lane >> 4;
  const lcp tb = (lcp)smem + (4 * (G >> 1) + (i16 >> 2)) * 192 + (nt * 32 + 16 * (G & 1) + 4 * (i16 & 3)) * 2;
  f32x16 acc = {};
#pragma unroll
  for (int ks = 0; ks < 36; ++ks) {
    const bf16x8 wf = cat8(trr(tb + ks * 3072), trr(tb + ks * 3072 + 8 * 192));
    acc = MFMA32(wf, oa[ks], acc);
  }
#pragma unroll
  for (int q = 0; q < 4; ++q) {
    float r[4];
#pragma unroll
    for (int e = 0; e < 4; ++e) {
      const unsigned w = bb2[q][e >> 1];
      const float b2v = __uint_as_float((e & 1) ? (w & 0xffff0000u) : (w << 16));
      r[e] = gt * acc[4 * q + e] + (1.0f - gt) * (pb[q][e] + b2v);
    }
    u32x2 o2 = {cvtpk(r[0], r[1]), cvtpk(r[2], r[3])};
    *(u32x2*)(out + (size_t)T * D + nb * 96 + nt * 32 + 8 * q + 4 * hh) = o2;
  }
  TS(3, 2);
}

extern "C" void kernel_launch(void* const* d_in, const int* in_sizes, int n_in,
                              void* d_out, int out_size, void* d_ws, size_t ws_size,
                              hipStream_t stream) {
  const uint16_t* x = (const uint16_t*)d_in[0];
  const uint16_t* wqkv = (const uint16_t*)d_in[1];
  const uint16_t* wo = (const uint16_t*)d_in[2];
  const uint16_t* w1 = (const uint16_t*)d_in[4];
  const uint16_t* b1 = (const uint16_t*)d_in[5];
  const uint16_t* w2 = (const uint16_t*)d_in[6];
  const uint16_t* b2 = (const uint16_t*)d_in[7];
  const uint16_t* gw = (const uint16_t*)d_in[8];
  char* ws = (char*)d_ws;
  (void)hipFuncSetAttribute((const void*)k1_kernel, hipFuncAttributeMaxDynamicSharedMemorySize, K1_LDS);
  (void)hipFuncSetAttribute((const void*)attn_kernel, hipFuncAttributeMaxDynamicSharedMemorySize, AT_LDS);
  (void)hipFuncSetAttribute((const void*)g2_kernel, hipFuncAttributeMaxDynamicSharedMemorySize, G2_LDS);
  (void)hipFuncSetAttribute((const void*)k3_kernel, hipFuncAttributeMaxDynamicSharedMemorySize, K3_LDS);
  k1_kernel<<<256, 512, K1_LDS, stream>>>(x, wqkv, w1, b1, gw, ws);
  attn_kernel<<<256, 512, AT_LDS, stream>>>(ws);
  g2_kernel<<<256, 256, G2_LDS, stream>>>(w2, ws);
  k3_kernel<<<192, 384, K3_LDS, stream>>>(wo, b2, ws, (uint16_t*)d_out);
}
```

```cpp
#include <hip/hip_runtime.h>
#include <hip/hip_bf16.h>
#include <stdint.h>
#include <type_traits>

typedef short bf16x8 __attribute__((ext_vector_type(8)));
typedef short s16x4 __attribute__((ext_vector_type(4)));
typedef float f32x16 __attribute__((ext_vector_type(16)));
typedef float f32x4 __attribute__((ext_vector_type(4)));
typedef float f32x2 __attribute__((ext_vector_type(2)));
typedef unsigned u32x4 __attribute__((ext_vector_type(4)));
typedef unsigned u32x2 __attribute__((ext_vector_type(2)));
typedef __bf16 bf16x2_t __attribute__((ext_vector_type(2)));
#define LAS __attribute__((address_space(3)))
#define GAS __attribute__((address_space(1)))
#define DI __device__ __forceinline__

constexpr int S = 2048, D = 576, H = 32, DH = 18, F = 4608, NQKV = 1728;

constexpr size_t WS_QF = 0;
constexpr size_t WS_Q2 = WS_QF + (size_t)H * 64 * 1024;
constexpr int    KV_T = 2496;
constexpr int    KV_K2 = 2368;
constexpr int    KV_GRP = 4 * KV_T;
constexpr int    KV_HEAD = 16 * KV_GRP;
constexpr size_t WS_KV = WS_Q2 + (size_t)H * S * 4;
#define HT_PAD 0
constexpr int    HT_ST = 288 * 1024 + HT_PAD;
constexpr size_t WS_HT = WS_KV + (size_t)H * KV_HEAD;
constexpr size_t WS_OT = WS_HT + (size_t)64 * HT_ST;
constexpr size_t WS_BP = WS_OT + (size_t)S * D * 2;
constexpr size_t WS_G  = WS_BP + (size_t)4 * 64 * 8 * 5120;
constexpr size_t WS_SY = WS_G + (size_t)S * 8;
constexpr int    SY_B = 64 * 128;
constexpr int    SY_F = SY_B + 128 * 128;
constexpr unsigned SY_ALL = 0x04040404u;
constexpr size_t WS_END = WS_SY + SY_F + 2048;

#define G2_DELAY 44
#define G2_DELAYC 44
DI int lane_id() {
  int l;
  asm volatile("v_mbcnt_lo_u32_b32 %0, -1, 0\n\tv_mbcnt_hi_u32_b32 %0, -1, %0" : "=v"(l));
  return l;
}
#define PAD() asm volatile("77:\n\ts_cbranch_execz 77f")
#define SEG(k) asm volatile("77:\n\ts_cbranch_execz .Lpf_end\n.Lpf_s" #k ":\n\ts_cbranch_execz 77f")
#define TS(kid, i)
#define LTS_DECL
#define LTS(i)
#define LTS_OUT(kid)
DI unsigned cvtpk(float lo, float hi) {
  bf16x2_t p; p[0] = (__bf16)lo; p[1] = (__bf16)hi; return __builtin_bit_cast(unsigned, p);
}
DI float dot2(unsigned a, unsigned b, float c) {
  return __builtin_amdgcn_fdot2_f32_bf16(__builtin_bit_cast(bf16x2_t, a), __builtin_bit_cast(bf16x2_t, b), c, false);
}
DI void glds16(const void* g, char* lds_wave_base) {
  const unsigned lb = (unsigned)(uintptr_t)(LAS char*)lds_wave_base;
  unsigned sv;
  asm volatile("s_mov_b32 %0, m0\n\ts_mov_b32 m0, %2\n\ts_nop 0\n\tglobal_load_lds_dwordx4 %1, off\n\ts_mov_b32 m0, %0"
               : "=&s"(sv) : "v"(g), "s"(lb) : "memory");
}
DI s16x4 trread(const char* p) {
  return __builtin_bit_cast(s16x4, __builtin_amdgcn_ds_read_tr16_b64_v4i16((LAS s16x4*)p));
}
DI bf16x8 cat8(s16x4 a, s16x4 b) { return __builtin_shufflevector(a, b, 0, 1, 2, 3, 4, 5, 6, 7); }
#define MFMA32(a, b, c) __builtin_amdgcn_mfma_f32_32x32x16_bf16((a), (b), (c), 0, 0, 0)
#define MFMA16(a, b, c) __builtin_amdgcn_mfma_f32_16x16x32_bf16((a), (b), (c), 0, 0, 0)
#define WAITV(n) asm volatile("s_waitcnt vmcnt(" #n ") lgkmcnt(0)" ::: "memory")
#define BAR() __builtin_amdgcn_s_barrier()
#define WAIT_BAR(n) asm volatile("s_waitcnt vmcnt(" #n ") lgkmcnt(0)\n\ts_barrier" ::: "memory")
typedef const LAS char* lcp;
#define LDS_B128(p) (*(const LAS bf16x8*)(p))
#define SBAR() __builtin_amdgcn_sched_barrier(0)
#define PIN(x) asm volatile("" : "+v"(x))
#define BC8(x) __builtin_bit_cast(bf16x8, (x))
#define EX2(v) __builtin_amdgcn_exp2f(v)
DI s16x4 trr(lcp p) { return __builtin_bit_cast(s16x4, __builtin_amdgcn_ds_read_tr16_b64_v4i16((LAS s16x4*)p)); }
DI float gelu_f(float x) {
  constexpr float c1 = -2.0f * 0.7978845608028654f * 1.4426950408889634f;
  constexpr float c2 = c1 * 0.044715f;
  float u = x * __builtin_fmaf(x * x, c2, c1);
  float e = __builtin_amdgcn_exp2f(u);
  return x * __builtin_amdgcn_rcpf(1.0f + e);
}

constexpr int K1_CH = 24576;
constexpr int K1_BIAS = 6 * K1_CH;
constexpr int K1_CS = K1_BIAS + 2048;
constexpr int K1_ST = K1_CS + 1024;
constexpr int K1_TAB = K1_ST + 2048;
constexpr int K1_LDS = K1_TAB + 2048;
#define K1_NPRE 4
typedef unsigned u32x8 __attribute__((ext_vector_type(8)));
typedef unsigned u32x16 __attribute__((ext_vector_type(16)));
typedef float f32x8 __attribute__((ext_vector_type(8)));
DI void glds16s(const void* sbase, unsigned voff, unsigned ldsaddr) {
  asm volatile("s_mov_b32 m0, %2\n\ts_nop 0\n\tglobal_load_lds_dwordx4 %0, %1" :: "v"(voff), "s"(sbase), "s"(ldsaddr) : "memory", "m0");
}
DI float addx32(float v) {
  auto rr = __builtin_amdgcn_permlane32_swap(__float_as_uint(v), __float_as_uint(v), false, false);
  return __uint_as_float(rr[0]) + __uint_as_float(rr[1]);
}
#define VCL10(p) "v" #p "0", "v" #p "1", "v" #p "2", "v" #p "3", "v" #p "4", "v" #p "5", "v" #p "6", "v" #p "7", "v" #p "8", "v" #p "9"
#define K1_CLOB VCL10(), VCL10(1), VCL10(2), VCL10(3), VCL10(4), VCL10(5), VCL10(6), VCL10(7), VCL10(8), VCL10(9), VCL10(10), VCL10(11), \
                VCL10(12), VCL10(13), VCL10(14), VCL10(15), "v176", "v177", "v178", "v179", VCL10(18), VCL10(19), \
                "s46", "s47", "s49", "s54", "scc", "m0", "memory"
#define K1_TSOUT

template <bool ISG1>
DI void k1_body(const uint16_t* __restrict__ x, const uint16_t* __restrict__ W, const uint16_t* __restrict__ b1,
                const uint16_t* __restrict__ gate_w, char* __restrict__ ws, char* smem, int grp, int rb, int rot) {
  int tid = threadIdx.x;
  PIN(tid);
  const int lane = tid & 63;
  const int wid = __builtin_amdgcn_readfirstlane(tid >> 6);
  constexpr int ldw = ISG1 ? F : NQKV;
  const int step0 = ISG1 ? grp * 6 : grp * 7;
  const int wm = wid & 3, wn = wid >> 2;
  const int hh = lane >> 5, l31 = lane & 31;
  const unsigned lds0 = (unsigned)(uintptr_t)(LAS char*)smem;

  const unsigned wvo = (unsigned)(((lane >> 3) + wid * 8) * ldw * 2 + (((lane & 7) ^ ((lane >> 2) & 4)) * 16));
  const char* wb = (const char*)W + step0 * 128;
  const int t0 = rot, t1 = rot == 2 ? 0 : rot + 1, t2 = rot == 0 ? 2 : rot - 1;
  u32x4 wvk = {0u, wvo + t0 * (192 * ldw * 2), wvo + t1 * (192 * ldw * 2), wvo + t2 * (192 * ldw * 2)};
  auto issue_chunk = [&](int kc, int slot) {
#pragma unroll
    for (int j = 0; j < 3; ++j) glds16s(wb + (size_t)(j * 64) * (ldw * 2), wvk[1 + kc], lds0 + slot * K1_CH + (wid + 8 * j) * 1024);
  };
  const char* xb = (const char*)x + (size_t)rb * 128 * (D * 2);
  const unsigned xvo0 = (unsigned)((16 * wid + (lane >> 3)) * (D * 2) + (((lane & 7) ^ (lane >> 4)) * 16));
  const unsigned xvo1 = (unsigned)((16 * wid + 8 + (lane >> 3)) * (D * 2) + (((lane & 7) ^ (4 + (lane >> 4))) * 16));
  auto issue_x = [&](int t, int slot, int n) {
#pragma unroll
    for (int k = 0; k < n; ++k)
      glds16s(xb + t * 384 + (k >> 1) * 128, (k & 1) ? xvo1 : xvo0, lds0 + slot * K1_CH + (k >> 1) * 16384 + wid * 2048 + (k & 1) * 1024);
  };
  issue_x(t0, 0, 6);
  issue_chunk(0, 2);
  unsigned b1v;
  if (ISG1) asm volatile("global_load_ushort %0, %1, %2" : "=v"(b1v) : "v"((tid < 384 ? tid : 383) * 2), "s"(b1 + step0 * 64) : "memory");
  else asm volatile("global_load_dword %0, %1, %2" : "=v"(b1v) : "v"((tid + rot * 96 - (tid + rot * 96 < 288 ? 0 : tid + rot * 96 < 576 ? 288 : 576)) * 4), "s"(gate_w) : "memory");
  asm volatile("s_barrier" ::: "memory");
  issue_x(t1, 3, K1_NPRE);
  const char* xc = xb + t2 * 384;
  xb += t1 * 384;

  const int sro = ISG1 ? wm + 2 * wn : 2 * wn;
  const int i16 = lane & 15, G = lane >> 4;
  const unsigned tb = lds0 + (8 * (G >> 1) + (i16 >> 2)) * 128 +
                      (((wn * 4 + 2 * (G & 1) + ((i16 & 3) >> 1)) ^ ((i16 >> 1) & 4)) * 16) + 8 * (i16 & 1);
  const unsigned xrow = lds0 + (wm * 32 + l31) * 128;
  const int xg = hh ^ ((l31 >> 1) & 7);
  u32x16 ad;
  u32x8 vs;
#pragma unroll
  for (int j = 0; j < 4; ++j) {
    const int seg = (j + sro) & 3;
#pragma unroll
    for (int r = 0; r < 3; ++r) {
      const int ks = 3 * seg + r;
      ad[3 * j + r] = xrow + (((2 * (ks & 3)) ^ xg) * 16) + (ks >> 2) * 16384;
    }
    ad[12 + j] = tb + seg * 6144 + 2 * K1_CH;
    vs[j] = tb + seg * 6144 + 3 * K1_CH;
  }
  vs[4] = wvo; vs[5] = xvo0; vs[6] = xvo1; vs[7] = lane * 16;
  const char* pfa;
  asm volatile("s_getpc_b64 s[38:39]\n78:\n\ts_add_u32 s38, s38, .Lpf_s0-78b\n\ts_addc_u32 s39, s39, 0" : "={s[38:39]}"(pfa) :: "scc");
  pfa += wid * 3328;
  unsigned sex = lds0 + wid * 2048, smp = lds0 + wid * 1024 + 3 * K1_CH, smo = lds0 + wid * 1024;
  int sdl = -3 * K1_CH;
  if constexpr (ISG1) {
    constexpr float c1 = -2.0f * 0.7978845608028654f * 1.4426950408889634f;
    constexpr float c2 = c1 * 0.044715f;
    const unsigned sv = ((i16 & 3) == 0 && ((i16 >> 2) & 1) == (G & 1)) ? 0x3f803f80u : 0u;
    const u32x4 sel = {sv, sv, sv, sv};
    u32x4 on = {0x3f803f80u, 0x3f803f80u, lds0 + K1_ST + (wm * 32 + l31) * 16, 0u};
    u32x4 ms = {0u, __float_as_uint(c1), lds0 + K1_BIAS + (wn * 32 + 4 * hh) * 4, lds0 + K1_CS + (wn * 32 + l31) * 16};
    const char* hb = ws + WS_HT + (size_t)(rb * 4 + wm) * HT_ST + (step0 * 4 + wn * 2) * 1024;
    asm volatile("s_waitcnt vmcnt(%1)" : "+v"(b1v) : "n"(K1_NPRE) : "memory");
    *(LAS unsigned*)((lcp)smem + K1_BIAS + tid * 4) = b1v << 16;
    asm volatile(
    "s_waitcnt lgkmcnt(0)\n\t"
    "s_barrier\n\t"
    "ds_read_b128 v[0:3], v160\n\t"
    "ds_read_b64_tr_b16 v[176:177], v172 offset:0\n\t"
    "ds_read_b64_tr_b16 v[178:179], v172 offset:512\n\t"
    "ds_read_b128 v[4:7], v161\n\t"
    "ds_read_b64_tr_b16 v[180:181], v172 offset:2048\n\t"
    "ds_read_b64_tr_b16 v[182:183], v172 offset:2560\n\t"
    "ds_read_b128 v[8:11], v162\n\t"
    "ds_read_b64_tr_b16 v[184:185], v172 offset:4096\n\t"
    "ds_read_b64_tr_b16 v[186:187], v172 offset:4608\n\t"
    "s_waitcnt lgkmcnt(6)\n\t"
    "v_mfma_f32_16x16x32_bf16 v[212:215], v[208:211], v[176:179], 0\n\t"
    "v_mfma_f32_32x32x16_bf16 v[144:159], v[176:179], v[0:3], 0\n\t"
    "ds_read_b64_tr_b16 v[188:189], v173 offset:0\n\t"
    "ds_read_b64_tr_b16 v[190:191], v173 offset:512\n\t"
    "ds_read_b128 v[12:15], v163\n\t"
    "ds_read_b128 v[16:19], v164\n\t"
    "ds_read_b128 v[20:23], v165\n\t"
    "s_add_u32 m0, s48, 106496\n\t"
    "s_add_u32 s46, s42, 256\n\t"
    "s_addc_u32 s47, s43, 0\n\t"
    "global_load_lds_dwordx4 v205, s[46:47]\n\t"
    "s_waitcnt lgkmcnt(8)\n\t"
    "v_mfma_f32_16x16x32_bf16 v[212:215], v[208:211], v[180:183], v[212:215]\n\t"
    "v_mfma_f32_32x32x16_bf16 v[144:159], v[180:183], v[4:7], v[144:159]\n\t"
    "ds_read_b64_tr_b16 v[176:177], v173 offset:2048\n\t"
    "ds_read_b64_tr_b16 v[178:179], v173 offset:2560\n\t"
    "ds_read_b128 v[24:27], v166\n\t"
    "ds_read_b128 v[28:31], v167\n\t"
    "ds_read_b128 v[32:35], v168\n\t"
    "s_add_u32 m0, s48, 107520\n\t"
    "s_add_u32 s46, s42, 256\n\t"
    "s_addc_u32 s47, s43, 0\n\t"
    "global_load_lds_dwordx4 v206, s[46:47]\n\t"
    "s_waitcnt lgkmcnt(10)\n\t"
    "v_mfma_f32_16x16x32_bf16 v[212:215], v[208:211], v[184:187], v[212:215]\n\t"
    "v_mfma_f32_32x32x16_bf16 v[144:159], v[184:187], v[8:11], v[144:159]\n\t"
    "ds_read_b64_tr_b16 v[180:181], v173 offset:4096\n\t"
    "ds_read_b64_tr_b16 v[182:183], v173 offset:4608\n\t"
    "ds_read_b128 v[36:39], v169\n\t"
    "ds_read_b128 v[40:43], v170\n\t"
    "ds_read_b128 v[44:47], v171\n\t"
    "s_add_u32 m0, s50, 49152\n\t"
    "s_add_u32 s46, s40, 0\n\t"
    "s_addc_u32 s47, s41, 0\n\t"
    "global_load_lds_dwordx4 v250, s[46:47]\n\t"
    "s_waitcnt lgkmcnt(12)\n\t"
    "v_mfma_f32_32x32x16_bf16 v[144:159], v[188:191], v[12:15], v[144:159]\n\t"
    "ds_read_b64_tr_b16 v[184:185], v174 offset:0\n\t"
    "ds_read_b64_tr_b16 v[186:187], v174 offset:512\n\t"
    "s_add_u32 m0, s50, 57344\n\t"
    "s_add_u32 s46, s40, 589824\n\t"
    "s_addc_u32 s47, s41, 0\n\t"
    "global_load_lds_dwordx4 v250, s[46:47]\n\t"
    "s_waitcnt lgkmcnt(10)\n\t"
    "v_mfma_f32_32x32x16_bf16 v[144:159], v[176:179], v[16:19], v[144:159]\n\t"
    "ds_read_b64_tr_b16 v[188:189], v174 offset:2048\n\t"
    "ds_read_b64_tr_b16 v[190:191], v174 offset:2560\n\t"
    "s_add_u32 m0, s50, 65536\n\t"
    "s_add_u32 s46, s40, 1179648\n\t"
    "s_addc_u32 s47, s41, 0\n\t"
    "global_load_lds_dwordx4 v250, s[46:47]\n\t"
    "s_waitcnt lgkmcnt(7)\n\t"
    "v_mfma_f32_32x32x16_bf16 v[144:159], v[180:183], v[20:23], v[144:159]\n\t"
    "ds_read_b64_tr_b16 v[176:177], v174 offset:4096\n\t"
    "ds_read_b64_tr_b16 v[178:179], v174 offset:4608\n\t"
    "s_waitcnt lgkmcnt(6)\n\t"
    "s_barrier\n\t"
    "s_waitcnt lgkmcnt(4)\n\t"
    "v_mfma_f32_32x32x16_bf16 v[144:159], v[184:187], v[24:27], v[144:159]\n\t"
    "ds_read_b64_tr_b16 v[180:181], v175 offset:0\n\t"
    "ds_read_b64_tr_b16 v[182:183], v175 offset:512\n\t"
    "s_add_u32 m0, s48, 0\n\t"
    "s_add_u32 s46, s36, 0\n\t"
    "s_addc_u32 s47, s37, 0\n\t"
    "global_load_lds_dwordx4 v205, s[46:47]\n\t"
    "v_mfma_f32_4x4x4_16b_bf16 v[220:223], v[228:229], v[0:1], 0\n\t"
    "v_mfma_f32_4x4x4_16b_bf16 v[224:227], v[0:1], v[0:1], 0\n\t"
    "s_nop 0\n\t"
    "v_mfma_f32_4x4x4_16b_bf16 v[220:223], v[228:229], v[2:3], v[220:223]\n\t"
    "v_mfma_f32_4x4x4_16b_bf16 v[224:227], v[2:3], v[2:3], v[224:227]\n\t"
    "s_nop 0\n\t"
    "s_waitcnt lgkmcnt(4)\n\t"
    "v_mfma_f32_32x32x16_bf16 v[144:159], v[188:191], v[28:31], v[144:159]\n\t"
    "ds_read_b64_tr_b16 v[184:185], v175 offset:2048\n\t"
    "ds_read_b64_tr_b16 v[186:187], v175 offset:2560\n\t"
    "s_add_u32 m0, s48, 1024\n\t"
    "s_add_u32 s46, s36, 0\n\t"
    "s_addc_u32 s47, s37, 0\n\t"
    "global_load_lds_dwordx4 v206, s[46:47]\n\t"
    "v_mfma_f32_4x4x4_16b_bf16 v[220:223], v[228:229], v[4:5], v[220:223]\n\t"
    "v_mfma_f32_4x4x4_16b_bf16 v[224:227], v[4:5], v[4:5], v[224:227]\n\t"
    "s_nop 0\n\t"
    "v_mfma_f32_4x4x4_16b_bf16 v[220:223], v[228:229], v[6:7], v[220:223]\n\t"
    "v_mfma_f32_4x4x4_16b_bf16 v[224:227], v[6:7], v[6:7], v[224:227]\n\t"
    "s_nop 0\n\t"
    "s_waitcnt lgkmcnt(4)\n\t"
    "v_mfma_f32_32x32x16_bf16 v[144:159], v[176:179], v[32:35], v[144:159]\n\t"
    "ds_read_b64_tr_b16 v[188:189], v175 offset:4096\n\t"
    "ds_read_b64_tr_b16 v[190:191], v175 offset:4608\n\t"
    "s_add_u32 m0, s48, 16384\n\t"
    "s_add_u32 s46, s36, 128\n\t"
    "s_addc_u32 s47, s37, 0\n\t"
    "global_load_lds_dwordx4 v205, s[46:47]\n\t"
    "v_subrev_u32_e32 v160, s52, v160\n\t"
    "v_subrev_u32_e32 v161, s52, v161\n\t"
    "v_subrev_u32_e32 v162, s52, v162\n\t"
    "v_mfma_f32_4x4x4_16b_bf16 v[220:223], v[228:229], v[8:9], v[220:223]\n\t"
    "v_mfma_f32_4x4x4_16b_bf16 v[224:227], v[8:9], v[8:9], v[224:227]\n\t"
    "s_nop 0\n\t"
    "v_mfma_f32_4x4x4_16b_bf16 v[220:223], v[228:229], v[10:11], v[220:223]\n\t"
    "v_mfma_f32_4x4x4_16b_bf16 v[224:227], v[10:11], v[10:11], v[224:227]\n\t"
    "s_nop 0\n\t"
    "s_waitcnt lgkmcnt(4)\n\t"
    "v_mfma_f32_32x32x16_bf16 v[144:159], v[180:183], v[36:39], v[144:159]\n\t"
    "s_add_u32 m0, s48, 17408\n\t"
    "s_add_u32 s46, s36, 128\n\t"
    "s_addc_u32 s47, s37, 0\n\t"
    "global_load_lds_dwordx4 v206, s[46:47]\n\t"
    "v_subrev_u32_e32 v163, s52, v163\n\t"
    "v_subrev_u32_e32 v164, s52, v164\n\t"
    "v_subrev_u32_e32 v165, s52, v165\n\t"
    "v_mfma_f32_4x4x4_16b_bf16 v[220:223], v[228:229], v[12:13], v[220:223]\n\t"
    "v_mfma_f32_4x4x4_16b_bf16 v[224:227], v[12:13], v[12:13], v[224:227]\n\t"
    "s_nop 0\n\t"
    "v_mfma_f32_4x4x4_16b_bf16 v[220:223], v[228:229], v[14:15], v[220:223]\n\t"
    "v_mfma_f32_4x4x4_16b_bf16 v[224:227], v[14:15], v[14:15], v[224:227]\n\t"
    "s_nop 0\n\t"
    "s_waitcnt lgkmcnt(2)\n\t"
    "v_mfma_f32_32x32x16_bf16 v[144:159], v[184:187], v[40:43], v[144:159]\n\t"
    "v_subrev_u32_e32 v166, s52, v166\n\t"
    "v_subrev_u32_e32 v167, s52, v167\n\t"
    "v_subrev_u32_e32 v168, s52, v168\n\t"
    "v_mfma_f32_4x4x4_16b_bf16 v[220:223], v[228:229], v[16:17], v[220:223]\n\t"
    "v_mfma_f32_4x4x4_16b_bf16 v[224:227], v[16:17], v[16:17], v[224:227]\n\t"
    "s_nop 0\n\t"
    "v_mfma_f32_4x4x4_16b_bf16 v[220:223], v[228:229], v[18:19], v[220:223]\n\t"
    "v_mfma_f32_4x4x4_16b_bf16 v[224:227], v[18:19], v[18:19], v[224:227]\n\t"
    "s_nop 0\n\t"
    "s_waitcnt lgkmcnt(0)\n\t"
    "v_mfma_f32_32x32x16_bf16 v[144:159], v[188:191], v[44:47], v[144:159]\n\t"
    "v_subrev_u32_e32 v169, s52, v169\n\t"
    "v_subrev_u32_e32 v170, s52, v170\n\t"
    "v_subrev_u32_e32 v171, s52, v171\n\t"
    "v_mfma_f32_4x4x4_16b_bf16 v[220:223], v[228:229], v[20:21], v[220:223]\n\t"
    "v_mfma_f32_4x4x4_16b_bf16 v[224:227], v[20:21], v[20:21], v[224:227]\n\t"
    "s_nop 0\n\t"
    "v_mfma_f32_4x4x4_16b_bf16 v[220:223], v[228:229], v[22:23], v[220:223]\n\t"
    "v_mfma_f32_4x4x4_16b_bf16 v[224:227], v[22:23], v[22:23], v[224:227]\n\t"
    "s_nop 0\n\t"
    "s_waitcnt vmcnt(4)\n\t"
    "s_barrier\n\t"
    "ds_read_b128 v[48:51], v160\n\t"
    "ds_read_b64_tr_b16 v[176:177], v200 offset:49152\n\t"
    "ds_read_b64_tr_b16 v[178:179], v200 offset:49664\n\t"
    "ds_read_b128 v[52:55], v161\n\t"
    "ds_read_b64_tr_b16 v[180:181], v200 offset:51200\n\t"
    "ds_read_b64_tr_b16 v[182:183], v200 offset:51712\n\t"
    "ds_read_b128 v[56:59], v162\n\t"
    "ds_read_b64_tr_b16 v[184:185], v200 offset:53248\n\t"
    "ds_read_b64_tr_b16 v[186:187], v200 offset:53760\n\t"
    "s_waitcnt lgkmcnt(6)\n\t"
    "v_mfma_f32_16x16x32_bf16 v[212:215], v[208:211], v[176:179], v[212:215]\n\t"
    "v_mfma_f32_32x32x16_bf16 v[144:159], v[176:179], v[48:51], v[144:159]\n\t"
    "ds_read_b64_tr_b16 v[188:189], v201 offset:49152\n\t"
    "ds_read_b64_tr_b16 v[190:191], v201 offset:49664\n\t"
    "ds_read_b128 v[60:63], v163\n\t"
    "ds_read_b128 v[64:67], v164\n\t"
    "ds_read_b128 v[68:71], v165\n\t"
    "s_add_u32 m0, s48, 32768\n\t"
    "s_add_u32 s46, s36, 256\n\t"
    "s_addc_u32 s47, s37, 0\n\t"
    "global_load_lds_dwordx4 v205, s[46:47]\n\t"
    "s_waitcnt lgkmcnt(8)\n\t"
    "v_mfma_f32_16x16x32_bf16 v[212:215], v[208:211], v[180:183], v[212:215]\n\t"
    "v_mfma_f32_32x32x16_bf16 v[144:159], v[180:183], v[52:55], v[144:159]\n\t"
    "ds_read_b64_tr_b16 v[176:177], v201 offset:51200\n\t"
    "ds_read_b64_tr_b16 v[178:179], v201 offset:51712\n\t"
    "ds_read_b128 v[72:75], v166\n\t"
    "ds_read_b128 v[76:79], v167\n\t"
    "ds_read_b128 v[80:83], v168\n\t"
    "s_add_u32 m0, s48, 33792\n\t"
    "s_add_u32 s46, s36, 256\n\t"
    "s_addc_u32 s47, s37, 0\n\t"
    "global_load_lds_dwordx4 v206, s[46:47]\n\t"
    "s_waitcnt lgkmcnt(10)\n\t"
    "v_mfma_f32_16x16x32_bf16 v[212:215], v[208:211], v[184:187], v[212:215]\n\t"
    "v_mfma_f32_32x32x16_bf16 v[144:159], v[184:187], v[56:59], v[144:159]\n\t"
    "ds_read_b64_tr_b16 v[180:181], v201 offset:53248\n\t"
    "ds_read_b64_tr_b16 v[182:183], v201 offset:53760\n\t"
    "ds_read_b128 v[84:87], v169\n\t"
    "ds_read_b128 v[88:91], v170\n\t"
    "ds_read_b128 v[92:95], v171\n\t"
    "s_add_u32 m0, s51, 49152\n\t"
    "s_add_u32 s46, s40, 0\n\t"
    "s_addc_u32 s47, s41, 0\n\t"
    "global_load_lds_dwordx4 v251, s[46:47]\n\t"
    "s_waitcnt lgkmcnt(12)\n\t"
    "v_mfma_f32_32x32x16_bf16 v[144:159], v[188:191], v[60:63], v[144:159]\n\t"
    "ds_read_b64_tr_b16 v[184:185], v202 offset:49152\n\t"
    "ds_read_b64_tr_b16 v[186:187], v202 offset:49664\n\t"
    "s_add_u32 m0, s51, 57344\n\t"
    "s_add_u32 s46, s40, 589824\n\t"
    "s_addc_u32 s47, s41, 0\n\t"
    "global_load_lds_dwordx4 v251, s[46:47]\n\t"
    "s_waitcnt lgkmcnt(10)\n\t"
    "v_mfma_f32_32x32x16_bf16 v[144:159], v[176:179], v[64:67], v[144:159]\n\t"
    "ds_read_b64_tr_b16 v[188:189], v202 offset:51200\n\t"
    "ds_read_b64_tr_b16 v[190:191], v202 offset:51712\n\t"
    "s_add_u32 m0, s51, 65536\n\t"
    "s_add_u32 s46, s40, 1179648\n\t"
    "s_addc_u32 s47, s41, 0\n\t"
    "global_load_lds_dwordx4 v251, s[46:47]\n\t"
    "s_waitcnt lgkmcnt(7)\n\t"
    "v_mfma_f32_32x32x16_bf16 v[144:159], v[180:183], v[68:71], v[144:159]\n\t"
    "ds_read_b64_tr_b16 v[176:177], v202 offset:53248\n\t"
    "ds_read_b64_tr_b16 v[178:179], v202 offset:53760\n\t"
    "s_waitcnt lgkmcnt(6)\n\t"
    "s_barrier\n\t"
    "s_waitcnt lgkmcnt(4)\n\t"
    "v_mfma_f32_32x32x16_bf16 v[144:159], v[184:187], v[72:75], v[144:159]\n\t"
    "ds_read_b64_tr_b16 v[180:181], v203 offset:49152\n\t"
    "ds_read_b64_tr_b16 v[182:183], v203 offset:49664\n\t"
    "s_add_u32 m0, s50, 0\n\t"
    "s_add_u32 s46, s40, 128\n\t"
    "s_addc_u32 s47, s41, 0\n\t"
    "global_load_lds_dwordx4 v249, s[46:47]\n\t"
    "v_mfma_f32_4x4x4_16b_bf16 v[220:223], v[228:229], v[48:49], v[220:223]\n\t"
    "v_mfma_f32_4x4x4_16b_bf16 v[224:227], v[48:49], v[48:49], v[224:227]\n\t"
    "s_nop 0\n\t"
    "v_mfma_f32_4x4x4_16b_bf16 v[220:223], v[228:229], v[50:51], v[220:223]\n\t"
    "v_mfma_f32_4x4x4_16b_bf16 v[224:227], v[50:51], v[50:51], v[224:227]\n\t"
    "s_nop 0\n\t"
    "s_waitcnt lgkmcnt(4)\n\t"
    "v_mfma_f32_32x32x16_bf16 v[144:159], v[188:191], v[76:79], v[144:159]\n\t"
    "ds_read_b64_tr_b16 v[184:185], v203 offset:51200\n\t"
    "ds_read_b64_tr_b16 v[186:187], v203 offset:51712\n\t"
    "s_add_u32 m0, s50, 8192\n\t"
    "s_add_u32 s46, s40, 589952\n\t"
    "s_addc_u32 s47, s41, 0\n\t"
    "global_load_lds_dwordx4 v249, s[46:47]\n\t"
    "v_mfma_f32_4x4x4_16b_bf16 v[220:223], v[228:229], v[52:53], v[220:223]\n\t"
    "v_mfma_f32_4x4x4_16b_bf16 v[224:227], v[52:53], v[52:53], v[224:227]\n\t"
    "s_nop 0\n\t"
    "v_mfma_f32_4x4x4_16b_bf16 v[220:223], v[228:229], v[54:55], v[220:223]\n\t"
    "v_mfma_f32_4x4x4_16b_bf16 v[224:227], v[54:55], v[54:55], v[224:227]\n\t"
    "s_nop 0\n\t"
    "s_waitcnt lgkmcnt(4)\n\t"
    "v_mfma_f32_32x32x16_bf16 v[144:159], v[176:179], v[80:83], v[144:159]\n\t"
    "ds_read_b64_tr_b16 v[188:189], v203 offset:53248\n\t"
    "ds_read_b64_tr_b16 v[190:191], v203 offset:53760\n\t"
    "s_add_u32 m0, s50, 16384\n\t"
    "s_add_u32 s46, s40, 1179776\n\t"
    "s_addc_u32 s47, s41, 0\n\t"
    "global_load_lds_dwordx4 v249, s[46:47]\n\t"
    "v_add_u32_e32 v160, s52, v160\n\t"
    "v_add_u32_e32 v161, s52, v161\n\t"
    "v_add_u32_e32 v162, s52, v162\n\t"
    "v_mfma_f32_4x4x4_16b_bf16 v[220:223], v[228:229], v[56:57], v[220:223]\n\t"
    "v_mfma_f32_4x4x4_16b_bf16 v[224:227], v[56:57], v[56:57], v[224:227]\n\t"
    "s_nop 0\n\t"
    "v_mfma_f32_4x4x4_16b_bf16 v[220:223], v[228:229], v[58:59], v[220:223]\n\t"
    "v_mfma_f32_4x4x4_16b_bf16 v[224:227], v[58:59], v[58:59], v[224:227]\n\t"
    "s_nop 0\n\t"
    "s_waitcnt lgkmcnt(4)\n\t"
    "v_mfma_f32_32x32x16_bf16 v[144:159], v[180:183], v[84:87], v[144:159]\n\t"
    "s_add_u32 m0, s50, 24576\n\t"
    "s_add_u32 s46, s40, 128\n\t"
    "s_addc_u32 s47, s41, 0\n\t"
    "global_load_lds_dwordx4 v250, s[46:47]\n\t"
    "v_add_u32_e32 v163, s52, v163\n\t"
    "v_add_u32_e32 v164, s52, v164\n\t"
    "v_add_u32_e32 v165, s52, v165\n\t"
    "v_mfma_f32_4x4x4_16b_bf16 v[220:223], v[228:229], v[60:61], v[220:223]\n\t"
    "v_mfma_f32_4x4x4_16b_bf16 v[224:227], v[60:61], v[60:61], v[224:227]\n\t"
    "s_nop 0\n\t"
    "v_mfma_f32_4x4x4_16b_bf16 v[220:223], v[228:229], v[62:63], v[220:223]\n\t"
    "v_mfma_f32_4x4x4_16b_bf16 v[224:227], v[62:63], v[62:63], v[224:227]\n\t"
    "s_nop 0\n\t"
    "s_waitcnt lgkmcnt(2)\n\t"
    "v_mfma_f32_32x32x16_bf16 v[144:159], v[184:187], v[88:91], v[144:159]\n\t"
    "v_add_u32_e32 v166, s52, v166\n\t"
    "v_add_u32_e32 v167, s52, v167\n\t"
    "v_add_u32_e32 v168, s52, v168\n\t"
    "v_mfma_f32_4x4x4_16b_bf16 v[220:223], v[228:229], v[64:65], v[220:223]\n\t"
    "v_mfma_f32_4x4x4_16b_bf16 v[224:227], v[64:65], v[64:65], v[224:227]\n\t"
    "s_nop 0\n\t"
    "v_mfma_f32_4x4x4_16b_bf16 v[220:223], v[228:229], v[66:67], v[220:223]\n\t"
    "v_mfma_f32_4x4x4_16b_bf16 v[224:227], v[66:67], v[66:67], v[224:227]\n\t"
    "s_nop 0\n\t"
    "s_waitcnt lgkmcnt(0)\n\t"
    "v_mfma_f32_32x32x16_bf16 v[144:159], v[188:191], v[92:95], v[144:159]\n\t"
    "v_add_u32_e32 v169, s52, v169\n\t"
    "v_add_u32_e32 v170, s52, v170\n\t"
    "v_add_u32_e32 v171, s52, v171\n\t"
    "v_mfma_f32_4x4x4_16b_bf16 v[220:223], v[228:229], v[68:69], v[220:223]\n\t"
    "v_mfma_f32_4x4x4_16b_bf16 v[224:227], v[68:69], v[68:69], v[224:227]\n\t"
    "s_nop 0\n\t"
    "v_mfma_f32_4x4x4_16b_bf16 v[220:223], v[228:229], v[70:71], v[220:223]\n\t"
    "v_mfma_f32_4x4x4_16b_bf16 v[224:227], v[70:71], v[70:71], v[224:227]\n\t"
    "s_nop 0\n\t"
    "s_waitcnt vmcnt(4)\n\t"
    "s_barrier\n\t"
    "ds_read_b128 v[96:99], v160\n\t"
    "ds_read_b64_tr_b16 v[176:177], v172 offset:0\n\t"
    "ds_read_b64_tr_b16 v[178:179], v172 offset:512\n\t"
    "ds_read_b128 v[100:103], v161\n\t"
    "ds_read_b64_tr_b16 v[180:181], v172 offset:2048\n\t"
    "ds_read_b64_tr_b16 v[182:183], v172 offset:2560\n\t"
    "ds_read_b128 v[104:107], v162\n\t"
    "ds_read_b64_tr_b16 v[184:185], v172 offset:4096\n\t"
    "ds_read_b64_tr_b16 v[186:187], v172 offset:4608\n\t"
    "s_waitcnt lgkmcnt(6)\n\t"
    "v_mfma_f32_16x16x32_bf16 v[212:215], v[208:211], v[176:179], v[212:215]\n\t"
    "v_mfma_f32_32x32x16_bf16 v[144:159], v[176:179], v[96:99], v[144:159]\n\t"
    "ds_read_b64_tr_b16 v[188:189], v173 offset:0\n\t"
    "ds_read_b64_tr_b16 v[190:191], v173 offset:512\n\t"
    "ds_read_b128 v[108:111], v163\n\t"
    "ds_read_b128 v[112:115], v164\n\t"
    "ds_read_b128 v[116:119], v165\n\t"
    "s_add_u32 m0, s50, 32768\n\t"
    "s_add_u32 s46, s40, 589952\n\t"
    "s_addc_u32 s47, s41, 0\n\t"
    "global_load_lds_dwordx4 v250, s[46:47]\n\t"
    "v_mfma_f32_4x4x4_16b_bf16 v[220:223], v[228:229], v[96:97], v[220:223]\n\t"
    "v_mfma_f32_4x4x4_16b_bf16 v[224:227], v[96:97], v[96:97], v[224:227]\n\t"
    "s_nop 0\n\t"
    "v_mfma_f32_4x4x4_16b_bf16 v[220:223], v[228:229], v[98:99], v[220:223]\n\t"
    "v_mfma_f32_4x4x4_16b_bf16 v[224:227], v[98:99], v[98:99], v[224:227]\n\t"
    "s_nop 0\n\t"
    "s_waitcnt lgkmcnt(10)\n\t"
    "v_mfma_f32_4x4x4_16b_bf16 v[220:223], v[228:229], v[100:101], v[220:223]\n\t"
    "v_mfma_f32_4x4x4_16b_bf16 v[224:227], v[100:101], v[100:101], v[224:227]\n\t"
    "s_nop 0\n\t"
    "v_mfma_f32_4x4x4_16b_bf16 v[220:223], v[228:229], v[102:103], v[220:223]\n\t"
    "v_mfma_f32_4x4x4_16b_bf16 v[224:227], v[102:103], v[102:103], v[224:227]\n\t"
    "s_nop 0\n\t"
    "s_waitcnt lgkmcnt(8)\n\t"
    "v_mfma_f32_16x16x32_bf16 v[212:215], v[208:211], v[180:183], v[212:215]\n\t"
    "v_mfma_f32_32x32x16_bf16 v[144:159], v[180:183], v[100:103], v[144:159]\n\t"
    "ds_read_b64_tr_b16 v[176:177], v173 offset:2048\n\t"
    "ds_read_b64_tr_b16 v[178:179], v173 offset:2560\n\t"
    "ds_read_b128 v[120:123], v166\n\t"
    "ds_read_b128 v[124:127], v167\n\t"
    "ds_read_b128 v[128:131], v168\n\t"
    "s_add_u32 m0, s50, 40960\n\t"
    "s_add_u32 s46, s40, 1179776\n\t"
    "s_addc_u32 s47, s41, 0\n\t"
    "global_load_lds_dwordx4 v250, s[46:47]\n\t"
    "s_waitcnt lgkmcnt(12)\n\t"
    "v_mfma_f32_4x4x4_16b_bf16 v[220:223], v[228:229], v[104:105], v[220:223]\n\t"
    "v_mfma_f32_4x4x4_16b_bf16 v[224:227], v[104:105], v[104:105], v[224:227]\n\t"
    "s_nop 0\n\t"
    "v_mfma_f32_4x4x4_16b_bf16 v[220:223], v[228:229], v[106:107], v[220:223]\n\t"
    "v_mfma_f32_4x4x4_16b_bf16 v[224:227], v[106:107], v[106:107], v[224:227]\n\t"
    "s_nop 0\n\t"
    "s_waitcnt lgkmcnt(7)\n\t"
    "v_mfma_f32_4x4x4_16b_bf16 v[220:223], v[228:229], v[108:109], v[220:223]\n\t"
    "v_mfma_f32_4x4x4_16b_bf16 v[224:227], v[108:109], v[108:109], v[224:227]\n\t"
    "s_nop 0\n\t"
    "v_mfma_f32_4x4x4_16b_bf16 v[220:223], v[228:229], v[110:111], v[220:223]\n\t"
    "v_mfma_f32_4x4x4_16b_bf16 v[224:227], v[110:111], v[110:111], v[224:227]\n\t"
    "s_nop 0\n\t"
    "v_mfma_f32_16x16x32_bf16 v[212:215], v[208:211], v[184:187], v[212:215]\n\t"
    "v_mfma_f32_32x32x16_bf16 v[144:159], v[184:187], v[104:107], v[144:159]\n\t"
    "ds_read_b64_tr_b16 v[180:181], v173 offset:4096\n\t"
    "ds_read_b64_tr_b16 v[182:183], v173 offset:4608\n\t"
    "ds_read_b128 v[132:135], v169\n\t"
    "ds_read_b128 v[136:139], v170\n\t"
    "ds_read_b128 v[140:143], v171\n\t"
    "s_add_u32 m0, s50, 49152\n\t"
    "s_add_u32 s46, s40, 128\n\t"
    "s_addc_u32 s47, s41, 0\n\t"
    "global_load_lds_dwordx4 v251, s[46:47]\n\t"
    "s_waitcnt lgkmcnt(11)\n\t"
    "v_mfma_f32_4x4x4_16b_bf16 v[220:223], v[228:229], v[112:113], v[220:223]\n\t"
    "v_mfma_f32_4x4x4_16b_bf16 v[224:227], v[112:113], v[112:113], v[224:227]\n\t"
    "s_nop 0\n\t"
    "v_mfma_f32_4x4x4_16b_bf16 v[220:223], v[228:229], v[114:115], v[220:223]\n\t"
    "v_mfma_f32_4x4x4_16b_bf16 v[224:227], v[114:115], v[114:115], v[224:227]\n\t"
    "s_nop 0\n\t"
    "s_waitcnt lgkmcnt(10)\n\t"
    "v_mfma_f32_4x4x4_16b_bf16 v[220:223], v[228:229], v[116:117], v[220:223]\n\t"
    "v_mfma_f32_4x4x4_16b_bf16 v[224:227], v[116:117], v[116:117], v[224:227]\n\t"
    "s_nop 0\n\t"
    "v_mfma_f32_4x4x4_16b_bf16 v[220:223], v[228:229], v[118:119], v[220:223]\n\t"
    "v_mfma_f32_4x4x4_16b_bf16 v[224:227], v[118:119], v[118:119], v[224:227]\n\t"
    "s_nop 0\n\t"
    "v_mfma_f32_32x32x16_bf16 v[144:159], v[188:191], v[108:111], v[144:159]\n\t"
    "ds_read_b64_tr_b16 v[184:185], v174 offset:0\n\t"
    "ds_read_b64_tr_b16 v[186:187], v174 offset:512\n\t"
    "s_add_u32 m0, s50, 57344\n\t"
    "s_add_u32 s46, s40, 589952\n\t"
    "s_addc_u32 s47, s41, 0\n\t"
    "global_load_lds_dwordx4 v251, s[46:47]\n\t"
    "s_waitcnt lgkmcnt(10)\n\t"
    "v_mfma_f32_32x32x16_bf16 v[144:159], v[176:179], v[112:115], v[144:159]\n\t"
    "ds_read_b64_tr_b16 v[188:189], v174 offset:2048\n\t"
    "ds_read_b64_tr_b16 v[190:191], v174 offset:2560\n\t"
    "s_add_u32 m0, s50, 65536\n\t"
    "s_add_u32 s46, s40, 1179776\n\t"
    "s_addc_u32 s47, s41, 0\n\t"
    "global_load_lds_dwordx4 v251, s[46:47]\n\t"
    "v_cvt_pk_bf16_f32 v213, v212, v212\n\t"
    "v_lshlrev_b32_e32 v213, 16, v213\n\t"
    "v_sub_f32_e32 v213, v212, v213\n\t"
    "v_cvt_pk_bf16_f32 v213, v212, v213\n\t"
    "v_add_u32_e32 v214, s55, v243\n\t"
    "ds_write_b32 v214, v213\n\t"
    "s_waitcnt lgkmcnt(8)\n\t"
    "v_mfma_f32_32x32x16_bf16 v[144:159], v[180:183], v[116:119], v[144:159]\n\t"
    "ds_read_b64_tr_b16 v[176:177], v174 offset:4096\n\t"
    "ds_read_b64_tr_b16 v[178:179], v174 offset:4608\n\t"
    "s_mov_b32 s48, 0x22222222\n\t"
    "s_mov_b32 s49, 0x22222222\n\t"
    "v_cndmask_b32_e64 v224, v224, v225, s[48:49]\n\t"
    "s_lshl_b64 s[48:49], s[48:49], 1\n\t"
    "v_cndmask_b32_e64 v224, v224, v226, s[48:49]\n\t"
    "s_lshl_b64 s[48:49], s[48:49], 1\n\t"
    "v_cndmask_b32_e64 v224, v224, v227, s[48:49]\n\t"
    "v_mov_b32_e32 v225, v224\n\t"
    "s_nop 1\n\t"
    "v_permlane32_swap_b32_e32 v220, v221\n\t"
    "v_permlane32_swap_b32_e32 v224, v225\n\t"
    "s_nop 0\n\t"
    "v_add_f32_e32 v220, v220, v221\n\t"
    "v_add_f32_e32 v221, v224, v225\n\t"
    "v_add_u32_e32 v222, s56, v230\n\t"
    "ds_write_b64 v222, v[220:221]\n\t"
    "s_waitcnt lgkmcnt(6)\n\t"
    "v_mfma_f32_32x32x16_bf16 v[144:159], v[184:187], v[120:123], v[144:159]\n\t"
    "ds_read_b64_tr_b16 v[180:181], v175 offset:0\n\t"
    "ds_read_b64_tr_b16 v[182:183], v175 offset:512\n\t"
    "s_waitcnt lgkmcnt(6)\n\t"
    "v_mfma_f32_32x32x16_bf16 v[144:159], v[188:191], v[124:127], v[144:159]\n\t"
    "s_waitcnt lgkmcnt(2)\n\t"
    "s_waitcnt vmcnt(6)\n\t"
    "s_barrier\n\t"
    "ds_read_b128 v[244:247], v243\n\t"
    "ds_read_b128 v[232:235], v230\n\t"
    "ds_read_b64_tr_b16 v[184:185], v175 offset:2048\n\t"
    "ds_read_b64_tr_b16 v[186:187], v175 offset:2560\n\t"
    "v_mfma_f32_32x32x16_bf16 v[144:159], v[176:179], v[128:131], v[144:159]\n\t"
    "ds_read_b64_tr_b16 v[188:189], v175 offset:4096\n\t"
    "ds_read_b64_tr_b16 v[190:191], v175 offset:4608\n\t"
    "ds_read_b128 v[224:227], v242 offset:0\n\t"
    "s_waitcnt lgkmcnt(7)\n\t"
    "v_mfma_f32_32x32x16_bf16 v[144:159], v[180:183], v[132:135], v[144:159]\n\t"
    "ds_read_b64_tr_b16 v[176:177], v200 offset:0\n\t"
    "ds_read_b64_tr_b16 v[178:179], v200 offset:512\n\t"
    "s_waitcnt lgkmcnt(7)\n\t"
    "v_add_f32_e32 v236, v232, v234\n\t"
    "v_add_f32_e32 v237, v233, v235\n\t"
    "v_mul_f32_e32 v236, 0xbae38e39, v236\n\t"
    "v_mul_f32_e32 v238, v236, v236\n\t"
    "v_mul_f32_e32 v237, 0x3ae38e39, v237\n\t"
    "v_sub_f32_e32 v237, v237, v238\n\t"
    "v_max_f32_e32 v237, 0, v237\n\t"
    "v_add_f32_e32 v237, 0x3727c5ac, v237\n\t"
    "v_rsq_f32_e32 v240, v237\n\t"
    "v_cvt_pk_bf16_f32 v238, v236, v236\n\t"
    "v_lshlrev_b32_e32 v239, 16, v238\n\t"
    "v_sub_f32_e32 v239, v236, v239\n\t"
    "v_cvt_pk_bf16_f32 v239, v239, v239\n\t"
    "s_mov_b32 s48, 0\n\t"
    "s_mov_b32 s49, -1\n\t"
    "v_cndmask_b32_e64 v216, v238, v239, s[48:49]\n\t"
    "v_cndmask_b32_e64 v217, v238, v239, s[48:49]\n\t"
    "v_cndmask_b32_e64 v218, v238, v239, s[48:49]\n\t"
    "v_cndmask_b32_e64 v219, v238, v239, s[48:49]\n\t"
    "s_waitcnt lgkmcnt(5)\n\t"
    "v_mfma_f32_32x32x16_bf16 v[144:159], v[184:187], v[136:139], v[144:159]\n\t"
    "ds_read_b64_tr_b16 v[180:181], v200 offset:2048\n\t"
    "ds_read_b64_tr_b16 v[182:183], v200 offset:2560\n\t"
    "s_waitcnt lgkmcnt(5)\n\t"
    "v_mfma_f32_32x32x16_bf16 v[144:159], v[244:247], v[216:219], v[144:159]\n\t"
    "v_mfma_f32_32x32x16_bf16 v[160:175], v[188:191], v[140:143], v[144:159]\n\t"
    "ds_read_b64_tr_b16 v[184:185], v200 offset:4096\n\t"
    "ds_read_b64_tr_b16 v[186:187], v200 offset:4608\n\t"
    "s_add_u32 s40, s40, 128\n\t"
    "s_addc_u32 s41, s41, 0\n\t"
    "s_waitcnt lgkmcnt(4)\n\t"
    "v_mfma_f32_16x16x32_bf16 v[212:215], v[208:211], v[176:179], 0\n\t"
    "v_mfma_f32_32x32x16_bf16 v[144:159], v[176:179], v[0:3], 0\n\t"
    "ds_read_b64_tr_b16 v[188:189], v201 offset:0\n\t"
    "ds_read_b64_tr_b16 v[190:191], v201 offset:512\n\t"
    "s_waitcnt lgkmcnt(4)\n\t"
    "v_mfma_f32_16x16x32_bf16 v[212:215], v[208:211], v[180:183], v[212:215]\n\t"
    "v_mfma_f32_32x32x16_bf16 v[144:159], v[180:183], v[4:7], v[144:159]\n\t"
    "ds_read_b64_tr_b16 v[176:177], v201 offset:2048\n\t"
    "ds_read_b64_tr_b16 v[178:179], v201 offset:2560\n\t"
    "s_add_u32 m0, s51, 0\n\t"
    "s_add_u32 s46, s40, 128\n\t"
    "s_addc_u32 s47, s41, 0\n\t"
    "global_load_lds_dwordx4 v249, s[46:47]\n\t"
    "s_waitcnt lgkmcnt(4)\n\t"
    "v_mfma_f32_16x16x32_bf16 v[212:215], v[208:211], v[184:187], v[212:215]\n\t"
    "v_mfma_f32_32x32x16_bf16 v[144:159], v[184:187], v[8:11], v[144:159]\n\t"
    "ds_read_b64_tr_b16 v[180:181], v201 offset:4096\n\t"
    "ds_read_b64_tr_b16 v[182:183], v201 offset:4608\n\t"
    "v_fma_f32 v232, v160, v240, v224\n\t"
    "v_fma_f32 v233, v161, v240, v225\n\t"
    "s_waitcnt lgkmcnt(4)\n\t"
    "v_mfma_f32_32x32x16_bf16 v[144:159], v[188:191], v[12:15], v[144:159]\n\t"
    "ds_read_b64_tr_b16 v[184:185], v202 offset:0\n\t"
    "ds_read_b64_tr_b16 v[186:187], v202 offset:512\n\t"
    "s_add_u32 m0, s51, 8192\n\t"
    "s_add_u32 s46, s40, 589952\n\t"
    "s_addc_u32 s47, s41, 0\n\t"
    "global_load_lds_dwordx4 v249, s[46:47]\n\t"
    "v_mul_f32_e32 v236, v232, v232\n\t"
    "v_mul_f32_e32 v237, v233, v233\n\t"
    "v_fma_f32 v236, v236, s53, v241\n\t"
    "v_fma_f32 v237, v237, s53, v241\n\t"
    "v_mul_f32_e32 v236, v232, v236\n\t"
    "v_mul_f32_e32 v237, v233, v237\n\t"
    "s_waitcnt lgkmcnt(4)\n\t"
    "v_mfma_f32_32x32x16_bf16 v[144:159], v[176:179], v[16:19], v[144:159]\n\t"
    "ds_read_b64_tr_b16 v[188:189], v202 offset:2048\n\t"
    "ds_read_b64_tr_b16 v[190:191], v202 offset:2560\n\t"
    "v_exp_f32_e32 v236, v236\n\t"
    "v_exp_f32_e32 v237, v237\n\t"
    "s_waitcnt lgkmcnt(4)\n\t"
    "v_mfma_f32_32x32x16_bf16 v[144:159], v[180:183], v[20:23], v[144:159]\n\t"
    "ds_read_b64_tr_b16 v[176:177], v202 offset:4096\n\t"
    "ds_read_b64_tr_b16 v[178:179], v202 offset:4608\n\t"
    "s_add_u32 m0, s51, 16384\n\t"
    "s_add_u32 s46, s40, 1179776\n\t"
    "s_addc_u32 s47, s41, 0\n\t"
    "global_load_lds_dwordx4 v249, s[46:47]\n\t"
    "ds_read_b128 v[228:231], v242 offset:32\n\t"
    "v_add_f32_e32 v238, 1.0, v236\n\t"
    "v_add_f32_e32 v239, 1.0, v237\n\t"
    "v_rcp_f32_e32 v238, v238\n\t"
    "v_rcp_f32_e32 v239, v239\n\t"
    "s_waitcnt lgkmcnt(5)\n\t"
    "v_mfma_f32_32x32x16_bf16 v[144:159], v[184:187], v[24:27], v[144:159]\n\t"
    "ds_read_b64_tr_b16 v[180:181], v203 offset:0\n\t"
    "ds_read_b64_tr_b16 v[182:183], v203 offset:512\n\t"
    "s_waitcnt vmcnt(6)\n\t"
    "s_barrier\n\t"
    "v_mul_f32_e32 v238, v232, v238\n\t"
    "v_mul_f32_e32 v239, v233, v239\n\t"
    "v_cvt_pk_bf16_f32 v220, v238, v239\n\t"
    "v_fma_f32 v234, v162, v240, v226\n\t"
    "v_fma_f32 v235, v163, v240, v227\n\t"
    "s_waitcnt lgkmcnt(5)\n\t"
    "v_mfma_f32_32x32x16_bf16 v[144:159], v[188:191], v[28:31], v[144:159]\n\t"
    "ds_read_b64_tr_b16 v[184:185], v203 offset:2048\n\t"
    "ds_read_b64_tr_b16 v[186:187], v203 offset:2560\n\t"
    "s_add_u32 m0, s51, 24576\n\t"
    "s_add_u32 s46, s40, 128\n\t"
    "s_addc_u32 s47, s41, 0\n\t"
    "global_load_lds_dwordx4 v250, s[46:47]\n\t"
    "v_mul_f32_e32 v236, v234, v234\n\t"
    "v_mul_f32_e32 v237, v235, v235\n\t"
    "v_fma_f32 v236, v236, s53, v241\n\t"
    "v_fma_f32 v237, v237, s53, v241\n\t"
    "v_mul_f32_e32 v236, v234, v236\n\t"
    "v_mul_f32_e32 v237, v235, v237\n\t"
    "s_waitcnt lgkmcnt(5)\n\t"
    "v_mfma_f32_32x32x16_bf16 v[144:159], v[176:179], v[32:35], v[144:159]\n\t"
    "ds_read_b64_tr_b16 v[188:189], v203 offset:4096\n\t"
    "ds_read_b64_tr_b16 v[190:191], v203 offset:4608\n\t"
    "v_exp_f32_e32 v236, v236\n\t"
    "v_exp_f32_e32 v237, v237\n\t"
    "s_waitcnt lgkmcnt(4)\n\t"
    "v_mfma_f32_32x32x16_bf16 v[144:159], v[180:183], v[36:39], v[144:159]\n\t"
    "ds_read_b64_tr_b16 v[176:177], v200 offset:24576\n\t"
    "ds_read_b64_tr_b16 v[178:179], v200 offset:25088\n\t"
    "s_add_u32 m0, s51, 32768\n\t"
    "s_add_u32 s46, s40, 589952\n\t"
    "s_addc_u32 s47, s41, 0\n\t"
    "global_load_lds_dwordx4 v250, s[46:47]\n\t"
    "v_add_f32_e32 v238, 1.0, v236\n\t"
    "v_add_f32_e32 v239, 1.0, v237\n\t"
    "v_rcp_f32_e32 v238, v238\n\t"
    "v_rcp_f32_e32 v239, v239\n\t"
    "s_waitcnt lgkmcnt(4)\n\t"
    "v_mfma_f32_32x32x16_bf16 v[144:159], v[184:187], v[40:43], v[144:159]\n\t"
    "ds_read_b64_tr_b16 v[180:181], v200 offset:26624\n\t"
    "ds_read_b64_tr_b16 v[182:183], v200 offset:27136\n\t"
    "v_mul_f32_e32 v238, v234, v238\n\t"
    "v_mul_f32_e32 v239, v235, v239\n\t"
    "v_cvt_pk_bf16_f32 v221, v238, v239\n\t"
    "v_fma_f32 v232, v164, v240, v228\n\t"
    "v_fma_f32 v233, v165, v240, v229\n\t"
    "s_waitcnt lgkmcnt(4)\n\t"
    "v_mfma_f32_32x32x16_bf16 v[144:159], v[188:191], v[44:47], v[144:159]\n\t"
    "ds_read_b64_tr_b16 v[184:185], v200 offset:28672\n\t"
    "ds_read_b64_tr_b16 v[186:187], v200 offset:29184\n\t"
    "s_add_u32 m0, s51, 40960\n\t"
    "s_add_u32 s46, s40, 1179776\n\t"
    "s_addc_u32 s47, s41, 0\n\t"
    "global_load_lds_dwordx4 v250, s[46:47]\n\t"
    "v_mul_f32_e32 v236, v232, v232\n\t"
    "v_mul_f32_e32 v237, v233, v233\n\t"
    "v_fma_f32 v236, v236, s53, v241\n\t"
    "v_fma_f32 v237, v237, s53, v241\n\t"
    "v_mul_f32_e32 v236, v232, v236\n\t"
    "v_mul_f32_e32 v237, v233, v237\n\t"
    "s_waitcnt lgkmcnt(4)\n\t"
    "v_mfma_f32_16x16x32_bf16 v[212:215], v[208:211], v[176:179], v[212:215]\n\t"
    "v_mfma_f32_32x32x16_bf16 v[144:159], v[176:179], v[48:51], v[144:159]\n\t"
    "ds_read_b64_tr_b16 v[188:189], v201 offset:24576\n\t"
    "ds_read_b64_tr_b16 v[190:191], v201 offset:25088\n\t"
    "v_exp_f32_e32 v236, v236\n\t"
    "v_exp_f32_e32 v237, v237\n\t"
    "s_waitcnt lgkmcnt(4)\n\t"
    "v_mfma_f32_16x16x32_bf16 v[212:215], v[208:211], v[180:183], v[212:215]\n\t"
    "v_mfma_f32_32x32x16_bf16 v[144:159], v[180:183], v[52:55], v[144:159]\n\t"
    "ds_read_b64_tr_b16 v[176:177], v201 offset:26624\n\t"
    "ds_read_b64_tr_b16 v[178:179], v201 offset:27136\n\t"
    "s_add_u32 m0, s51, 49152\n\t"
    "s_add_u32 s46, s40, 128\n\t"
    "s_addc_u32 s47, s41, 0\n\t"
    "global_load_lds_dwordx4 v251, s[46:47]\n\t"
    "ds_read_b128 v[224:227], v242 offset:64\n\t"
    "v_add_f32_e32 v238, 1.0, v236\n\t"
    "v_add_f32_e32 v239, 1.0, v237\n\t"
    "v_rcp_f32_e32 v238, v238\n\t"
    "v_rcp_f32_e32 v239, v239\n\t"
    "s_waitcnt lgkmcnt(5)\n\t"
    "v_mfma_f32_16x16x32_bf16 v[212:215], v[208:211], v[184:187], v[212:215]\n\t"
    "v_mfma_f32_32x32x16_bf16 v[144:159], v[184:187], v[56:59], v[144:159]\n\t"
    "ds_read_b64_tr_b16 v[180:181], v201 offset:28672\n\t"
    "ds_read_b64_tr_b16 v[182:183], v201 offset:29184\n\t"
    "v_mul_f32_e32 v238, v232, v238\n\t"
    "v_mul_f32_e32 v239, v233, v239\n\t"
    "v_cvt_pk_bf16_f32 v222, v238, v239\n\t"
    "v_fma_f32 v234, v166, v240, v230\n\t"
    "v_fma_f32 v235, v167, v240, v231\n\t"
    "s_waitcnt lgkmcnt(5)\n\t"
    "v_mfma_f32_32x32x16_bf16 v[144:159], v[188:191], v[60:63], v[144:159]\n\t"
    "ds_read_b64_tr_b16 v[184:185], v202 offset:24576\n\t"
    "ds_read_b64_tr_b16 v[186:187], v202 offset:25088\n\t"
    "s_add_u32 m0, s51, 57344\n\t"
    "s_add_u32 s46, s40, 589952\n\t"
    "s_addc_u32 s47, s41, 0\n\t"
    "global_load_lds_dwordx4 v251, s[46:47]\n\t"
    "v_mul_f32_e32 v236, v234, v234\n\t"
    "v_mul_f32_e32 v237, v235, v235\n\t"
    "v_fma_f32 v236, v236, s53, v241\n\t"
    "v_fma_f32 v237, v237, s53, v241\n\t"
    "v_mul_f32_e32 v236, v234, v236\n\t"
    "v_mul_f32_e32 v237, v235, v237\n\t"
    "s_waitcnt lgkmcnt(5)\n\t"
    "v_mfma_f32_32x32x16_bf16 v[144:159], v[176:179], v[64:67], v[144:159]\n\t"
    "ds_read_b64_tr_b16 v[188:189], v202 offset:26624\n\t"
    "ds_read_b64_tr_b16 v[190:191], v202 offset:27136\n\t"
    "v_exp_f32_e32 v236, v236\n\t"
    "v_exp_f32_e32 v237, v237\n\t"
    "s_waitcnt lgkmcnt(4)\n\t"
    "v_mfma_f32_32x32x16_bf16 v[144:159], v[180:183], v[68:71], v[144:159]\n\t"
    "ds_read_b64_tr_b16 v[176:177], v202 offset:28672\n\t"
    "ds_read_b64_tr_b16 v[178:179], v202 offset:29184\n\t"
    "s_add_u32 m0, s51, 65536\n\t"
    "s_add_u32 s46, s40, 1179776\n\t"
    "s_addc_u32 s47, s41, 0\n\t"
    "global_load_lds_dwordx4 v251, s[46:47]\n\t"
    "v_add_f32_e32 v238, 1.0, v236\n\t"
    "v_add_f32_e32 v239, 1.0, v237\n\t"
    "v_rcp_f32_e32 v238, v238\n\t"
    "v_rcp_f32_e32 v239, v239\n\t"
    "s_waitcnt lgkmcnt(4)\n\t"
    "v_mfma_f32_32x32x16_bf16 v[144:159], v[184:187], v[72:75], v[144:159]\n\t"
    "ds_read_b64_tr_b16 v[180:181], v203 offset:24576\n\t"
    "ds_read_b64_tr_b16 v[182:183], v203 offset:25088\n\t"
    "s_waitcnt vmcnt(9)\n\t"
    "s_barrier\n\t"
    "v_mul_f32_e32 v238, v234, v238\n\t"
    "v_mul_f32_e32 v239, v235, v239\n\t"
    "v_cvt_pk_bf16_f32 v223, v238, v239\n\t"
    "global_store_dwordx4 v207, v[220:223], s[44:45] offset:0 sc1\n\t"
    "v_fma_f32 v232, v168, v240, v224\n\t"
    "v_fma_f32 v233, v169, v240, v225\n\t"
    "s_waitcnt lgkmcnt(4)\n\t"
    "v_mfma_f32_32x32x16_bf16 v[144:159], v[188:191], v[76:79], v[144:159]\n\t"
    "ds_read_b64_tr_b16 v[184:185], v203 offset:26624\n\t"
    "ds_read_b64_tr_b16 v[186:187], v203 offset:27136\n\t"
    "s_add_u32 m0, s50, 0\n\t"
    "s_add_u32 s46, s40, 256\n\t"
    "s_addc_u32 s47, s41, 0\n\t"
    "global_load_lds_dwordx4 v249, s[46:47]\n\t"
    "v_mul_f32_e32 v236, v232, v232\n\t"
    "v_mul_f32_e32 v237, v233, v233\n\t"
    "v_fma_f32 v236, v236, s53, v241\n\t"
    "v_fma_f32 v237, v237, s53, v241\n\t"
    "v_mul_f32_e32 v236, v232, v236\n\t"
    "v_mul_f32_e32 v237, v233, v237\n\t"
    "s_waitcnt lgkmcnt(4)\n\t"
    "v_mfma_f32_32x32x16_bf16 v[144:159], v[176:179], v[80:83], v[144:159]\n\t"
    "ds_read_b64_tr_b16 v[188:189], v203 offset:28672\n\t"
    "ds_read_b64_tr_b16 v[190:191], v203 offset:29184\n\t"
    "v_exp_f32_e32 v236, v236\n\t"
    "v_exp_f32_e32 v237, v237\n\t"
    "s_waitcnt lgkmcnt(4)\n\t"
    "v_mfma_f32_32x32x16_bf16 v[144:159], v[180:183], v[84:87], v[144:159]\n\t"
    "ds_read_b64_tr_b16 v[176:177], v200 offset:49152\n\t"
    "ds_read_b64_tr_b16 v[178:179], v200 offset:49664\n\t"
    "s_add_u32 m0, s50, 8192\n\t"
    "s_add_u32 s46, s40, 590080\n\t"
    "s_addc_u32 s47, s41, 0\n\t"
    "global_load_lds_dwordx4 v249, s[46:47]\n\t"
    "ds_read_b128 v[228:231], v242 offset:96\n\t"
    "v_add_f32_e32 v238, 1.0, v236\n\t"
    "v_add_f32_e32 v239, 1.0, v237\n\t"
    "v_rcp_f32_e32 v238, v238\n\t"
    "v_rcp_f32_e32 v239, v239\n\t"
    "s_waitcnt lgkmcnt(5)\n\t"
    "v_mfma_f32_32x32x16_bf16 v[144:159], v[184:187], v[88:91], v[144:159]\n\t"
    "ds_read_b64_tr_b16 v[180:181], v200 offset:51200\n\t"
    "ds_read_b64_tr_b16 v[182:183], v200 offset:51712\n\t"
    "v_mul_f32_e32 v238, v232, v238\n\t"
    "v_mul_f32_e32 v239, v233, v239\n\t"
    "v_cvt_pk_bf16_f32 v220, v238, v239\n\t"
    "v_fma_f32 v234, v170, v240, v226\n\t"
    "v_fma_f32 v235, v171, v240, v227\n\t"
    "s_waitcnt lgkmcnt(5)\n\t"
    "v_mfma_f32_32x32x16_bf16 v[144:159], v[188:191], v[92:95], v[144:159]\n\t"
    "ds_read_b64_tr_b16 v[184:185], v200 offset:53248\n\t"
    "ds_read_b64_tr_b16 v[186:187], v200 offset:53760\n\t"
    "s_add_u32 m0, s50, 16384\n\t"
    "s_add_u32 s46, s40, 1179904\n\t"
    "s_addc_u32 s47, s41, 0\n\t"
    "global_load_lds_dwordx4 v249, s[46:47]\n\t"
    "v_mul_f32_e32 v236, v234, v234\n\t"
    "v_mul_f32_e32 v237, v235, v235\n\t"
    "v_fma_f32 v236, v236, s53, v241\n\t"
    "v_fma_f32 v237, v237, s53, v241\n\t"
    "v_mul_f32_e32 v236, v234, v236\n\t"
    "v_mul_f32_e32 v237, v235, v237\n\t"
    "s_waitcnt lgkmcnt(5)\n\t"
    "v_mfma_f32_16x16x32_bf16 v[212:215], v[208:211], v[176:179], v[212:215]\n\t"
    "v_mfma_f32_32x32x16_bf16 v[144:159], v[176:179], v[96:99], v[144:159]\n\t"
    "ds_read_b64_tr_b16 v[188:189], v201 offset:49152\n\t"
    "ds_read_b64_tr_b16 v[190:191], v201 offset:49664\n\t"
    "v_add_u32_e32 v200, s52, v200\n\t"
    "v_exp_f32_e32 v236, v236\n\t"
    "v_exp_f32_e32 v237, v237\n\t"
    "s_waitcnt lgkmcnt(4)\n\t"
    "v_mfma_f32_16x16x32_bf16 v[212:215], v[208:211], v[180:183], v[212:215]\n\t"
    "v_mfma_f32_32x32x16_bf16 v[144:159], v[180:183], v[100:103], v[144:159]\n\t"
    "ds_read_b64_tr_b16 v[176:177], v201 offset:51200\n\t"
    "ds_read_b64_tr_b16 v[178:179], v201 offset:51712\n\t"
    "v_add_u32_e32 v242, 0x100, v242\n\t"
    "v_add_f32_e32 v238, 1.0, v236\n\t"
    "v_add_f32_e32 v239, 1.0, v237\n\t"
    "v_rcp_f32_e32 v238, v238\n\t"
    "v_rcp_f32_e32 v239, v239\n\t"
    "s_waitcnt lgkmcnt(4)\n\t"
    "v_mfma_f32_16x16x32_bf16 v[212:215], v[208:211], v[184:187], v[212:215]\n\t"
    "v_mfma_f32_32x32x16_bf16 v[144:159], v[184:187], v[104:107], v[144:159]\n\t"
    "ds_read_b64_tr_b16 v[180:181], v201 offset:53248\n\t"
    "ds_read_b64_tr_b16 v[182:183], v201 offset:53760\n\t"
    "v_mul_f32_e32 v238, v234, v238\n\t"
    "v_mul_f32_e32 v239, v235, v239\n\t"
    "v_cvt_pk_bf16_f32 v221, v238, v239\n\t"
    "v_fma_f32 v232, v172, v240, v228\n\t"
    "v_fma_f32 v233, v173, v240, v229\n\t"
    "s_waitcnt lgkmcnt(4)\n\t"
    "v_mfma_f32_32x32x16_bf16 v[144:159], v[188:191], v[108:111], v[144:159]\n\t"
    "ds_read_b64_tr_b16 v[184:185], v202 offset:49152\n\t"
    "ds_read_b64_tr_b16 v[186:187], v202 offset:49664\n\t"
    "v_add_u32_e32 v201, s52, v201\n\t"
    "v_mul_f32_e32 v236, v232, v232\n\t"
    "v_mul_f32_e32 v237, v233, v233\n\t"
    "v_fma_f32 v236, v236, s53, v241\n\t"
    "v_fma_f32 v237, v237, s53, v241\n\t"
    "v_mul_f32_e32 v236, v232, v236\n\t"
    "v_mul_f32_e32 v237, v233, v237\n\t"
    "s_waitcnt lgkmcnt(4)\n\t"
    "v_mfma_f32_32x32x16_bf16 v[144:159], v[176:179], v[112:115], v[144:159]\n\t"
    "ds_read_b64_tr_b16 v[188:189], v202 offset:51200\n\t"
    "ds_read_b64_tr_b16 v[190:191], v202 offset:51712\n\t"
    "v_cvt_pk_bf16_f32 v213, v212, v212\n\t"
    "v_lshlrev_b32_e32 v213, 16, v213\n\t"
    "v_sub_f32_e32 v213, v212, v213\n\t"
    "v_cvt_pk_bf16_f32 v213, v212, v213\n\t"
    "v_add_u32_e32 v214, s55, v243\n\t"
    "ds_write_b32 v214, v213\n\t"
    "v_exp_f32_e32 v236, v236\n\t"
    "v_exp_f32_e32 v237, v237\n\t"
    "s_waitcnt lgkmcnt(5)\n\t"
    "v_mfma_f32_32x32x16_bf16 v[144:159], v[180:183], v[116:119], v[144:159]\n\t"
    "ds_read_b64_tr_b16 v[176:177], v202 offset:53248\n\t"
    "ds_read_b64_tr_b16 v[178:179], v202 offset:53760\n\t"
    "v_add_f32_e32 v238, 1.0, v236\n\t"
    "v_add_f32_e32 v239, 1.0, v237\n\t"
    "v_rcp_f32_e32 v238, v238\n\t"
    "v_rcp_f32_e32 v239, v239\n\t"
    "s_waitcnt lgkmcnt(5)\n\t"
    "v_mfma_f32_32x32x16_bf16 v[144:159], v[184:187], v[120:123], v[144:159]\n\t"
    "ds_read_b64_tr_b16 v[180:181], v203 offset:49152\n\t"
    "ds_read_b64_tr_b16 v[182:183], v203 offset:49664\n\t"
    "s_waitcnt lgkmcnt(4)\n\t"
    "s_waitcnt vmcnt(10)\n\t"
    "s_barrier\n\t"
    "ds_read_b128 v[244:247], v243\n\t"
    "v_add_u32_e32 v202, s52, v202\n\t"
    "v_mul_f32_e32 v238, v232, v238\n\t"
    "v_mul_f32_e32 v239, v233, v239\n\t"
    "v_cvt_pk_bf16_f32 v222, v238, v239\n\t"
    "v_fma_f32 v234, v174, v240, v230\n\t"
    "v_fma_f32 v235, v175, v240, v231\n\t"
    "v_mfma_f32_32x32x16_bf16 v[144:159], v[188:191], v[124:127], v[144:159]\n\t"
    "ds_read_b64_tr_b16 v[184:185], v203 offset:51200\n\t"
    "ds_read_b64_tr_b16 v[186:187], v203 offset:51712\n\t"
    "s_add_u32 m0, s50, 24576\n\t"
    "s_add_u32 s46, s40, 256\n\t"
    "s_addc_u32 s47, s41, 0\n\t"
    "global_load_lds_dwordx4 v250, s[46:47]\n\t"
    "v_mul_f32_e32 v236, v234, v234\n\t"
    "v_mul_f32_e32 v237, v235, v235\n\t"
    "v_fma_f32 v236, v236, s53, v241\n\t"
    "v_fma_f32 v237, v237, s53, v241\n\t"
    "v_mul_f32_e32 v236, v234, v236\n\t"
    "v_mul_f32_e32 v237, v235, v237\n\t"
    "s_waitcnt lgkmcnt(5)\n\t"
    "v_mfma_f32_32x32x16_bf16 v[144:159], v[176:179], v[128:131], v[144:159]\n\t"
    "ds_read_b64_tr_b16 v[188:189], v203 offset:53248\n\t"
    "ds_read_b64_tr_b16 v[190:191], v203 offset:53760\n\t"
    "ds_read_b128 v[224:227], v242 offset:0\n\t"
    "v_exp_f32_e32 v236, v236\n\t"
    "v_exp_f32_e32 v237, v237\n\t"
    "s_waitcnt lgkmcnt(6)\n\t"
    "v_mfma_f32_32x32x16_bf16 v[144:159], v[180:183], v[132:135], v[144:159]\n\t"
    "ds_read_b64_tr_b16 v[176:177], v200 offset:0\n\t"
    "ds_read_b64_tr_b16 v[178:179], v200 offset:512\n\t"
    "s_add_u32 m0, s50, 32768\n\t"
    "s_add_u32 s46, s40, 590080\n\t"
    "s_addc_u32 s47, s41, 0\n\t"
    "global_load_lds_dwordx4 v250, s[46:47]\n\t"
    "v_add_u32_e32 v203, s52, v203\n\t"
    "v_add_f32_e32 v238, 1.0, v236\n\t"
    "v_add_f32_e32 v239, 1.0, v237\n\t"
    "v_rcp_f32_e32 v238, v238\n\t"
    "v_rcp_f32_e32 v239, v239\n\t"
    "s_waitcnt lgkmcnt(5)\n\t"
    "v_mfma_f32_32x32x16_bf16 v[144:159], v[184:187], v[136:139], v[144:159]\n\t"
    "ds_read_b64_tr_b16 v[180:181], v200 offset:2048\n\t"
    "ds_read_b64_tr_b16 v[182:183], v200 offset:2560\n\t"
    "v_mul_f32_e32 v238, v234, v238\n\t"
    "v_mul_f32_e32 v239, v235, v239\n\t"
    "v_cvt_pk_bf16_f32 v223, v238, v239\n\t"
    "global_store_dwordx4 v207, v[220:223], s[44:45] offset:1024 sc1\n\t"
    "s_waitcnt lgkmcnt(5)\n\t"
    "v_mfma_f32_32x32x16_bf16 v[144:159], v[244:247], v[216:219], v[144:159]\n\t"
    "v_mfma_f32_32x32x16_bf16 v[160:175], v[188:191], v[140:143], v[144:159]\n\t"
    "ds_read_b64_tr_b16 v[184:185], v200 offset:4096\n\t"
    "ds_read_b64_tr_b16 v[186:187], v200 offset:4608\n\t"
    "s_add_u32 m0, s50, 40960\n\t"
    "s_add_u32 s46, s40, 1179904\n\t"
    "s_addc_u32 s47, s41, 0\n\t"
    "global_load_lds_dwordx4 v250, s[46:47]\n\t"
    "s_sub_u32 s52, 0, s52\n\t"
    "s_mov_b32 s46, s50\n\t"
    "s_mov_b32 s50, s51\n\t"
    "s_mov_b32 s51, s46\n\t"
    "s_add_u32 s40, s40, 128\n\t"
    "s_addc_u32 s41, s41, 0\n\t"
    "s_add_u32 s44, s44, 4096\n\t"
    "s_addc_u32 s45, s45, 0\n\t"
    "s_mov_b32 s54, 2\n\t"
    ".p2align 4\n\t"
    ".Lk1g%=:\n\t"
    "s_waitcnt lgkmcnt(4)\n\t"
    "v_mfma_f32_16x16x32_bf16 v[212:215], v[208:211], v[176:179], 0\n\t"
    "v_mfma_f32_32x32x16_bf16 v[144:159], v[176:179], v[0:3], 0\n\t"
    "ds_read_b64_tr_b16 v[188:189], v201 offset:0\n\t"
    "ds_read_b64_tr_b16 v[190:191], v201 offset:512\n\t"
    "s_waitcnt lgkmcnt(4)\n\t"
    "v_mfma_f32_16x16x32_bf16 v[212:215], v[208:211], v[180:183], v[212:215]\n\t"
    "v_mfma_f32_32x32x16_bf16 v[144:159], v[180:183], v[4:7], v[144:159]\n\t"
    "ds_read_b64_tr_b16 v[176:177], v201 offset:2048\n\t"
    "ds_read_b64_tr_b16 v[178:179], v201 offset:2560\n\t"
    "s_waitcnt lgkmcnt(4)\n\t"
    "v_mfma_f32_16x16x32_bf16 v[212:215], v[208:211], v[184:187], v[212:215]\n\t"
    "v_mfma_f32_32x32x16_bf16 v[144:159], v[184:187], v[8:11], v[144:159]\n\t"
    "ds_read_b64_tr_b16 v[180:181], v201 offset:4096\n\t"
    "ds_read_b64_tr_b16 v[182:183], v201 offset:4608\n\t"
    "v_fma_f32 v232, v160, v240, v224\n\t"
    "v_fma_f32 v233, v161, v240, v225\n\t"
    "s_waitcnt lgkmcnt(4)\n\t"
    "v_mfma_f32_32x32x16_bf16 v[144:159], v[188:191], v[12:15], v[144:159]\n\t"
    "ds_read_b64_tr_b16 v[184:185], v202 offset:0\n\t"
    "ds_read_b64_tr_b16 v[186:187], v202 offset:512\n\t"
    "v_mul_f32_e32 v236, v232, v232\n\t"
    "v_mul_f32_e32 v237, v233, v233\n\t"
    "v_fma_f32 v236, v236, s53, v241\n\t"
    "v_fma_f32 v237, v237, s53, v241\n\t"
    "v_mul_f32_e32 v236, v232, v236\n\t"
    "v_mul_f32_e32 v237, v233, v237\n\t"
    "s_waitcnt lgkmcnt(4)\n\t"
    "v_mfma_f32_32x32x16_bf16 v[144:159], v[176:179], v[16:19], v[144:159]\n\t"
    "ds_read_b64_tr_b16 v[188:189], v202 offset:2048\n\t"
    "ds_read_b64_tr_b16 v[190:191], v202 offset:2560\n\t"
    "v_exp_f32_e32 v236, v236\n\t"
    "v_exp_f32_e32 v237, v237\n\t"
    "s_waitcnt lgkmcnt(4)\n\t"
    "v_mfma_f32_32x32x16_bf16 v[144:159], v[180:183], v[20:23], v[144:159]\n\t"
    "ds_read_b64_tr_b16 v[176:177], v202 offset:4096\n\t"
    "ds_read_b64_tr_b16 v[178:179], v202 offset:4608\n\t"
    "ds_read_b128 v[228:231], v242 offset:32\n\t"
    "v_add_f32_e32 v238, 1.0, v236\n\t"
    "v_add_f32_e32 v239, 1.0, v237\n\t"
    "v_rcp_f32_e32 v238, v238\n\t"
    "v_rcp_f32_e32 v239, v239\n\t"
    "s_waitcnt lgkmcnt(5)\n\t"
    "v_mfma_f32_32x32x16_bf16 v[144:159], v[184:187], v[24:27], v[144:159]\n\t"
    "ds_read_b64_tr_b16 v[180:181], v203 offset:0\n\t"
    "ds_read_b64_tr_b16 v[182:183], v203 offset:512\n\t"
    "s_waitcnt vmcnt(11)\n\t"
    "s_barrier\n\t"
    "v_mul_f32_e32 v238, v232, v238\n\t"
    "v_mul_f32_e32 v239, v233, v239\n\t"
    "v_cvt_pk_bf16_f32 v220, v238, v239\n\t"
    "v_fma_f32 v234, v162, v240, v226\n\t"
    "v_fma_f32 v235, v163, v240, v227\n\t"
    "s_waitcnt lgkmcnt(5)\n\t"
    "v_mfma_f32_32x32x16_bf16 v[144:159], v[188:191], v[28:31], v[144:159]\n\t"
    "ds_read_b64_tr_b16 v[184:185], v203 offset:2048\n\t"
    "ds_read_b64_tr_b16 v[186:187], v203 offset:2560\n\t"
    "s_add_u32 m0, s51, 49152\n\t"
    "s_add_u32 s46, s40, 128\n\t"
    "s_addc_u32 s47, s41, 0\n\t"
    "global_load_lds_dwordx4 v251, s[46:47]\n\t"
    "v_mul_f32_e32 v236, v234, v234\n\t"
    "v_mul_f32_e32 v237, v235, v235\n\t"
    "v_fma_f32 v236, v236, s53, v241\n\t"
    "v_fma_f32 v237, v237, s53, v241\n\t"
    "v_mul_f32_e32 v236, v234, v236\n\t"
    "v_mul_f32_e32 v237, v235, v237\n\t"
    "s_waitcnt lgkmcnt(5)\n\t"
    "v_mfma_f32_32x32x16_bf16 v[144:159], v[176:179], v[32:35], v[144:159]\n\t"
    "ds_read_b64_tr_b16 v[188:189], v203 offset:4096\n\t"
    "ds_read_b64_tr_b16 v[190:191], v203 offset:4608\n\t"
    "v_exp_f32_e32 v236, v236\n\t"
    "v_exp_f32_e32 v237, v237\n\t"
    "s_waitcnt lgkmcnt(4)\n\t"
    "v_mfma_f32_32x32x16_bf16 v[144:159], v[180:183], v[36:39], v[144:159]\n\t"
    "ds_read_b64_tr_b16 v[176:177], v200 offset:24576\n\t"
    "ds_read_b64_tr_b16 v[178:179], v200 offset:25088\n\t"
    "s_add_u32 m0, s51, 57344\n\t"
    "s_add_u32 s46, s40, 589952\n\t"
    "s_addc_u32 s47, s41, 0\n\t"
    "global_load_lds_dwordx4 v251, s[46:47]\n\t"
    "v_add_f32_e32 v238, 1.0, v236\n\t"
    "v_add_f32_e32 v239, 1.0, v237\n\t"
    "v_rcp_f32_e32 v238, v238\n\t"
    "v_rcp_f32_e32 v239, v239\n\t"
    "s_waitcnt lgkmcnt(4)\n\t"
    "v_mfma_f32_32x32x16_bf16 v[144:159], v[184:187], v[40:43], v[144:159]\n\t"
    "ds_read_b64_tr_b16 v[180:181], v200 offset:26624\n\t"
    "ds_read_b64_tr_b16 v[182:183], v200 offset:27136\n\t"
    "v_mul_f32_e32 v238, v234, v238\n\t"
    "v_mul_f32_e32 v239, v235, v239\n\t"
    "v_cvt_pk_bf16_f32 v221, v238, v239\n\t"
    "v_fma_f32 v232, v164, v240, v228\n\t"
    "v_fma_f32 v233, v165, v240, v229\n\t"
    "s_waitcnt lgkmcnt(4)\n\t"
    "v_mfma_f32_32x32x16_bf16 v[144:159], v[188:191], v[44:47], v[144:159]\n\t"
    "ds_read_b64_tr_b16 v[184:185], v200 offset:28672\n\t"
    "ds_read_b64_tr_b16 v[186:187], v200 offset:29184\n\t"
    "s_add_u32 m0, s51, 65536\n\t"
    "s_add_u32 s46, s40, 1179776\n\t"
    "s_addc_u32 s47, s41, 0\n\t"
    "global_load_lds_dwordx4 v251, s[46:47]\n\t"
    "v_mul_f32_e32 v236, v232, v232\n\t"
    "v_mul_f32_e32 v237, v233, v233\n\t"
    "v_fma_f32 v236, v236, s53, v241\n\t"
    "v_fma_f32 v237, v237, s53, v241\n\t"
    "v_mul_f32_e32 v236, v232, v236\n\t"
    "v_mul_f32_e32 v237, v233, v237\n\t"
    "s_waitcnt lgkmcnt(4)\n\t"
    "v_mfma_f32_16x16x32_bf16 v[212:215], v[208:211], v[176:179], v[212:215]\n\t"
    "v_mfma_f32_32x32x16_bf16 v[144:159], v[176:179], v[48:51], v[144:159]\n\t"
    "ds_read_b64_tr_b16 v[188:189], v201 offset:24576\n\t"
    "ds_read_b64_tr_b16 v[190:191], v201 offset:25088\n\t"
    "v_exp_f32_e32 v236, v236\n\t"
    "v_exp_f32_e32 v237, v237\n\t"
    "s_waitcnt lgkmcnt(4)\n\t"
    "v_mfma_f32_16x16x32_bf16 v[212:215], v[208:211], v[180:183], v[212:215]\n\t"
    "v_mfma_f32_32x32x16_bf16 v[144:159], v[180:183], v[52:55], v[144:159]\n\t"
    "ds_read_b64_tr_b16 v[176:177], v201 offset:26624\n\t"
    "ds_read_b64_tr_b16 v[178:179], v201 offset:27136\n\t"
    "ds_read_b128 v[224:227], v242 offset:64\n\t"
    "v_add_f32_e32 v238, 1.0, v236\n\t"
    "v_add_f32_e32 v239, 1.0, v237\n\t"
    "v_rcp_f32_e32 v238, v238\n\t"
    "v_rcp_f32_e32 v239, v239\n\t"
    "s_waitcnt lgkmcnt(5)\n\t"
    "v_mfma_f32_16x16x32_bf16 v[212:215], v[208:211], v[184:187], v[212:215]\n\t"
    "v_mfma_f32_32x32x16_bf16 v[144:159], v[184:187], v[56:59], v[144:159]\n\t"
    "ds_read_b64_tr_b16 v[180:181], v201 offset:28672\n\t"
    "ds_read_b64_tr_b16 v[182:183], v201 offset:29184\n\t"
    "v_mul_f32_e32 v238, v232, v238\n\t"
    "v_mul_f32_e32 v239, v233, v239\n\t"
    "v_cvt_pk_bf16_f32 v222, v238, v239\n\t"
    "v_fma_f32 v234, v166, v240, v230\n\t"
    "v_fma_f32 v235, v167, v240, v231\n\t"
    "s_waitcnt lgkmcnt(5)\n\t"
    "v_mfma_f32_32x32x16_bf16 v[144:159], v[188:191], v[60:63], v[144:159]\n\t"
    "ds_read_b64_tr_b16 v[184:185], v202 offset:24576\n\t"
    "ds_read_b64_tr_b16 v[186:187], v202 offset:25088\n\t"
    "v_mul_f32_e32 v236, v234, v234\n\t"
    "v_mul_f32_e32 v237, v235, v235\n\t"
    "v_fma_f32 v236, v236, s53, v241\n\t"
    "v_fma_f32 v237, v237, s53, v241\n\t"
    "v_mul_f32_e32 v236, v234, v236\n\t"
    "v_mul_f32_e32 v237, v235, v237\n\t"
    "s_waitcnt lgkmcnt(5)\n\t"
    "v_mfma_f32_32x32x16_bf16 v[144:159], v[176:179], v[64:67], v[144:159]\n\t"
    "ds_read_b64_tr_b16 v[188:189], v202 offset:26624\n\t"
    "ds_read_b64_tr_b16 v[190:191], v202 offset:27136\n\t"
    "v_exp_f32_e32 v236, v236\n\t"
    "v_exp_f32_e32 v237, v237\n\t"
    "s_waitcnt lgkmcnt(4)\n\t"
    "v_mfma_f32_32x32x16_bf16 v[144:159], v[180:183], v[68:71], v[144:159]\n\t"
    "ds_read_b64_tr_b16 v[176:177], v202 offset:28672\n\t"
    "ds_read_b64_tr_b16 v[178:179], v202 offset:29184\n\t"
    "v_add_f32_e32 v238, 1.0, v236\n\t"
    "v_add_f32_e32 v239, 1.0, v237\n\t"
    "v_rcp_f32_e32 v238, v238\n\t"
    "v_rcp_f32_e32 v239, v239\n\t"
    "s_waitcnt lgkmcnt(4)\n\t"
    "v_mfma_f32_32x32x16_bf16 v[144:159], v[184:187], v[72:75], v[144:159]\n\t"
    "ds_read_b64_tr_b16 v[180:181], v203 offset:24576\n\t"
    "ds_read_b64_tr_b16 v[182:183], v203 offset:25088\n\t"
    "s_waitcnt vmcnt(11)\n\t"
    "s_barrier\n\t"
    "v_mul_f32_e32 v238, v234, v238\n\t"
    "v_mul_f32_e32 v239, v235, v239\n\t"
    "v_cvt_pk_bf16_f32 v223, v238, v239\n\t"
    "global_store_dwordx4 v207, v[220:223], s[44:45] offset:0 sc1\n\t"
    "v_fma_f32 v232, v168, v240, v224\n\t"
    "v_fma_f32 v233, v169, v240, v225\n\t"
    "s_waitcnt lgkmcnt(4)\n\t"
    "v_mfma_f32_32x32x16_bf16 v[144:159], v[188:191], v[76:79], v[144:159]\n\t"
    "ds_read_b64_tr_b16 v[184:185], v203 offset:26624\n\t"
    "ds_read_b64_tr_b16 v[186:187], v203 offset:27136\n\t"
    "s_add_u32 m0, s50, 0\n\t"
    "s_add_u32 s46, s40, 256\n\t"
    "s_addc_u32 s47, s41, 0\n\t"
    "global_load_lds_dwordx4 v249, s[46:47]\n\t"
    "v_mul_f32_e32 v236, v232, v232\n\t"
    "v_mul_f32_e32 v237, v233, v233\n\t"
    "v_fma_f32 v236, v236, s53, v241\n\t"
    "v_fma_f32 v237, v237, s53, v241\n\t"
    "v_mul_f32_e32 v236, v232, v236\n\t"
    "v_mul_f32_e32 v237, v233, v237\n\t"
    "s_waitcnt lgkmcnt(4)\n\t"
    "v_mfma_f32_32x32x16_bf16 v[144:159], v[176:179], v[80:83], v[144:159]\n\t"
    "ds_read_b64_tr_b16 v[188:189], v203 offset:28672\n\t"
    "ds_read_b64_tr_b16 v[190:191], v203 offset:29184\n\t"
    "v_exp_f32_e32 v236, v236\n\t"
    "v_exp_f32_e32 v237, v237\n\t"
    "s_waitcnt lgkmcnt(4)\n\t"
    "v_mfma_f32_32x32x16_bf16 v[144:159], v[180:183], v[84:87], v[144:159]\n\t"
    "ds_read_b64_tr_b16 v[176:177], v200 offset:49152\n\t"
    "ds_read_b64_tr_b16 v[178:179], v200 offset:49664\n\t"
    "s_add_u32 m0, s50, 8192\n\t"
    "s_add_u32 s46, s40, 590080\n\t"
    "s_addc_u32 s47, s41, 0\n\t"
    "global_load_lds_dwordx4 v249, s[46:47]\n\t"
    "ds_read_b128 v[228:231], v242 offset:96\n\t"
    "v_add_f32_e32 v238, 1.0, v236\n\t"
    "v_add_f32_e32 v239, 1.0, v237\n\t"
    "v_rcp_f32_e32 v238, v238\n\t"
    "v_rcp_f32_e32 v239, v239\n\t"
    "s_waitcnt lgkmcnt(5)\n\t"
    "v_mfma_f32_32x32x16_bf16 v[144:159], v[184:187], v[88:91], v[144:159]\n\t"
    "ds_read_b64_tr_b16 v[180:181], v200 offset:51200\n\t"
    "ds_read_b64_tr_b16 v[182:183], v200 offset:51712\n\t"
    "v_mul_f32_e32 v238, v232, v238\n\t"
    "v_mul_f32_e32 v239, v233, v239\n\t"
    "v_cvt_pk_bf16_f32 v220, v238, v239\n\t"
    "v_fma_f32 v234, v170, v240, v226\n\t"
    "v_fma_f32 v235, v171, v240, v227\n\t"
    "s_waitcnt lgkmcnt(5)\n\t"
    "v_mfma_f32_32x32x16_bf16 v[144:159], v[188:191], v[92:95], v[144:159]\n\t"
    "ds_read_b64_tr_b16 v[184:185], v200 offset:53248\n\t"
    "ds_read_b64_tr_b16 v[186:187], v200 offset:53760\n\t"
    "s_add_u32 m0, s50, 16384\n\t"
    "s_add_u32 s46, s40, 1179904\n\t"
    "s_addc_u32 s47, s41, 0\n\t"
    "global_load_lds_dwordx4 v249, s[46:47]\n\t"
    "v_mul_f32_e32 v236, v234, v234\n\t"
    "v_mul_f32_e32 v237, v235, v235\n\t"
    "v_fma_f32 v236, v236, s53, v241\n\t"
    "v_fma_f32 v237, v237, s53, v241\n\t"
    "v_mul_f32_e32 v236, v234, v236\n\t"
    "v_mul_f32_e32 v237, v235, v237\n\t"
    "s_waitcnt lgkmcnt(5)\n\t"
    "v_mfma_f32_16x16x32_bf16 v[212:215], v[208:211], v[176:179], v[212:215]\n\t"
    "v_mfma_f32_32x32x16_bf16 v[144:159], v[176:179], v[96:99], v[144:159]\n\t"
    "ds_read_b64_tr_b16 v[188:189], v201 offset:49152\n\t"
    "ds_read_b64_tr_b16 v[190:191], v201 offset:49664\n\t"
    "v_add_u32_e32 v200, s52, v200\n\t"
    "v_exp_f32_e32 v236, v236\n\t"
    "v_exp_f32_e32 v237, v237\n\t"
    "s_waitcnt lgkmcnt(4)\n\t"
    "v_mfma_f32_16x16x32_bf16 v[212:215], v[208:211], v[180:183], v[212:215]\n\t"
    "v_mfma_f32_32x32x16_bf16 v[144:159], v[180:183], v[100:103], v[144:159]\n\t"
    "ds_read_b64_tr_b16 v[176:177], v201 offset:51200\n\t"
    "ds_read_b64_tr_b16 v[178:179], v201 offset:51712\n\t"
    "v_add_u32_e32 v242, 0x100, v242\n\t"
    "v_add_f32_e32 v238, 1.0, v236\n\t"
    "v_add_f32_e32 v239, 1.0, v237\n\t"
    "v_rcp_f32_e32 v238, v238\n\t"
    "v_rcp_f32_e32 v239, v239\n\t"
    "s_waitcnt lgkmcnt(4)\n\t"
    "v_mfma_f32_16x16x32_bf16 v[212:215], v[208:211], v[184:187], v[212:215]\n\t"
    "v_mfma_f32_32x32x16_bf16 v[144:159], v[184:187], v[104:107], v[144:159]\n\t"
    "ds_read_b64_tr_b16 v[180:181], v201 offset:53248\n\t"
    "ds_read_b64_tr_b16 v[182:183], v201 offset:53760\n\t"
    "v_mul_f32_e32 v238, v234, v238\n\t"
    "v_mul_f32_e32 v239, v235, v239\n\t"
    "v_cvt_pk_bf16_f32 v221, v238, v239\n\t"
    "v_fma_f32 v232, v172, v240, v228\n\t"
    "v_fma_f32 v233, v173, v240, v229\n\t"
    "s_waitcnt lgkmcnt(4)\n\t"
    "v_mfma_f32_32x32x16_bf16 v[144:159], v[188:191], v[108:111], v[144:159]\n\t"
    "ds_read_b64_tr_b16 v[184:185], v202 offset:49152\n\t"
    "ds_read_b64_tr_b16 v[186:187], v202 offset:49664\n\t"
    "v_add_u32_e32 v201, s52, v201\n\t"
    "v_mul_f32_e32 v236, v232, v232\n\t"
    "v_mul_f32_e32 v237, v233, v233\n\t"
    "v_fma_f32 v236, v236, s53, v241\n\t"
    "v_fma_f32 v237, v237, s53, v241\n\t"
    "v_mul_f32_e32 v236, v232, v236\n\t"
    "v_mul_f32_e32 v237, v233, v237\n\t"
    "s_waitcnt lgkmcnt(4)\n\t"
    "v_mfma_f32_32x32x16_bf16 v[144:159], v[176:179], v[112:115], v[144:159]\n\t"
    "ds_read_b64_tr_b16 v[188:189], v202 offset:51200\n\t"
    "ds_read_b64_tr_b16 v[190:191], v202 offset:51712\n\t"
    "v_cvt_pk_bf16_f32 v213, v212, v212\n\t"
    "v_lshlrev_b32_e32 v213, 16, v213\n\t"
    "v_sub_f32_e32 v213, v212, v213\n\t"
    "v_cvt_pk_bf16_f32 v213, v212, v213\n\t"
    "v_add_u32_e32 v214, s55, v243\n\t"
    "ds_write_b32 v214, v213\n\t"
    "v_exp_f32_e32 v236, v236\n\t"
    "v_exp_f32_e32 v237, v237\n\t"
    "s_waitcnt lgkmcnt(5)\n\t"
    "v_mfma_f32_32x32x16_bf16 v[144:159], v[180:183], v[116:119], v[144:159]\n\t"
    "ds_read_b64_tr_b16 v[176:177], v202 offset:53248\n\t"
    "ds_read_b64_tr_b16 v[178:179], v202 offset:53760\n\t"
    "v_add_f32_e32 v238, 1.0, v236\n\t"
    "v_add_f32_e32 v239, 1.0, v237\n\t"
    "v_rcp_f32_e32 v238, v238\n\t"
    "v_rcp_f32_e32 v239, v239\n\t"
    "s_waitcnt lgkmcnt(5)\n\t"
    "v_mfma_f32_32x32x16_bf16 v[144:159], v[184:187], v[120:123], v[144:159]\n\t"
    "ds_read_b64_tr_b16 v[180:181], v203 offset:49152\n\t"
    "ds_read_b64_tr_b16 v[182:183], v203 offset:49664\n\t"
    "s_waitcnt lgkmcnt(4)\n\t"
    "s_waitcnt vmcnt(11)\n\t"
    "s_barrier\n\t"
    "ds_read_b128 v[244:247], v243\n\t"
    "v_add_u32_e32 v202, s52, v202\n\t"
    "v_mul_f32_e32 v238, v232, v238\n\t"
    "v_mul_f32_e32 v239, v233, v239\n\t"
    "v_cvt_pk_bf16_f32 v222, v238, v239\n\t"
    "v_fma_f32 v234, v174, v240, v230\n\t"
    "v_fma_f32 v235, v175, v240, v231\n\t"
    "v_mfma_f32_32x32x16_bf16 v[144:159], v[188:191], v[124:127], v[144:159]\n\t"
    "ds_read_b64_tr_b16 v[184:185], v203 offset:51200\n\t"
    "ds_read_b64_tr_b16 v[186:187], v203 offset:51712\n\t"
    "s_add_u32 m0, s50, 24576\n\t"
    "s_add_u32 s46, s40, 256\n\t"
    "s_addc_u32 s47, s41, 0\n\t"
    "global_load_lds_dwordx4 v250, s[46:47]\n\t"
    "v_mul_f32_e32 v236, v234, v234\n\t"
    "v_mul_f32_e32 v237, v235, v235\n\t"
    "v_fma_f32 v236, v236, s53, v241\n\t"
    "v_fma_f32 v237, v237, s53, v241\n\t"
    "v_mul_f32_e32 v236, v234, v236\n\t"
    "v_mul_f32_e32 v237, v235, v237\n\t"
    "s_waitcnt lgkmcnt(5)\n\t"
    "v_mfma_f32_32x32x16_bf16 v[144:159], v[176:179], v[128:131], v[144:159]\n\t"
    "ds_read_b64_tr_b16 v[188:189], v203 offset:53248\n\t"
    "ds_read_b64_tr_b16 v[190:191], v203 offset:53760\n\t"
    "ds_read_b128 v[224:227], v242 offset:0\n\t"
    "v_exp_f32_e32 v236, v236\n\t"
    "v_exp_f32_e32 v237, v237\n\t"
    "s_waitcnt lgkmcnt(6)\n\t"
    "v_mfma_f32_32x32x16_bf16 v[144:159], v[180:183], v[132:135], v[144:159]\n\t"
    "ds_read_b64_tr_b16 v[176:177], v200 offset:0\n\t"
    "ds_read_b64_tr_b16 v[178:179], v200 offset:512\n\t"
    "s_add_u32 m0, s50, 32768\n\t"
    "s_add_u32 s46, s40, 590080\n\t"
    "s_addc_u32 s47, s41, 0\n\t"
    "global_load_lds_dwordx4 v250, s[46:47]\n\t"
    "v_add_u32_e32 v203, s52, v203\n\t"
    "v_add_f32_e32 v238, 1.0, v236\n\t"
    "v_add_f32_e32 v239, 1.0, v237\n\t"
    "v_rcp_f32_e32 v238, v238\n\t"
    "v_rcp_f32_e32 v239, v239\n\t"
    "s_waitcnt lgkmcnt(5)\n\t"
    "v_mfma_f32_32x32x16_bf16 v[144:159], v[184:187], v[136:139], v[144:159]\n\t"
    "ds_read_b64_tr_b16 v[180:181], v200 offset:2048\n\t"
    "ds_read_b64_tr_b16 v[182:183], v200 offset:2560\n\t"
    "v_mul_f32_e32 v238, v234, v238\n\t"
    "v_mul_f32_e32 v239, v235, v239\n\t"
    "v_cvt_pk_bf16_f32 v223, v238, v239\n\t"
    "global_store_dwordx4 v207, v[220:223], s[44:45] offset:1024 sc1\n\t"
    "s_waitcnt lgkmcnt(5)\n\t"
    "v_mfma_f32_32x32x16_bf16 v[144:159], v[244:247], v[216:219], v[144:159]\n\t"
    "v_mfma_f32_32x32x16_bf16 v[160:175], v[188:191], v[140:143], v[144:159]\n\t"
    "ds_read_b64_tr_b16 v[184:185], v200 offset:4096\n\t"
    "ds_read_b64_tr_b16 v[186:187], v200 offset:4608\n\t"
    "s_add_u32 m0, s50, 40960\n\t"
    "s_add_u32 s46, s40, 1179904\n\t"
    "s_addc_u32 s47, s41, 0\n\t"
    "global_load_lds_dwordx4 v250, s[46:47]\n\t"
    "s_sub_u32 s52, 0, s52\n\t"
    "s_mov_b32 s46, s50\n\t"
    "s_mov_b32 s50, s51\n\t"
    "s_mov_b32 s51, s46\n\t"
    "s_add_u32 s40, s40, 128\n\t"
    "s_addc_u32 s41, s41, 0\n\t"
    "s_add_u32 s44, s44, 4096\n\t"
    "s_addc_u32 s45, s45, 0\n\t"
    "s_sub_u32 s54, s54, 1\n\t"
    "s_cmp_lg_u32 s54, 0\n\t"
    "s_cbranch_scc1 .Lk1g%=\n\t"
    "s_waitcnt lgkmcnt(4)\n\t"
    "v_mfma_f32_16x16x32_bf16 v[212:215], v[208:211], v[176:179], 0\n\t"
    "v_mfma_f32_32x32x16_bf16 v[144:159], v[176:179], v[0:3], 0\n\t"
    "ds_read_b64_tr_b16 v[188:189], v201 offset:0\n\t"
    "ds_read_b64_tr_b16 v[190:191], v201 offset:512\n\t"
    "s_waitcnt lgkmcnt(4)\n\t"
    "v_mfma_f32_16x16x32_bf16 v[212:215], v[208:211], v[180:183], v[212:215]\n\t"
    "v_mfma_f32_32x32x16_bf16 v[144:159], v[180:183], v[4:7], v[144:159]\n\t"
    "ds_read_b64_tr_b16 v[176:177], v201 offset:2048\n\t"
    "ds_read_b64_tr_b16 v[178:179], v201 offset:2560\n\t"
    "s_waitcnt lgkmcnt(4)\n\t"
    "v_mfma_f32_16x16x32_bf16 v[212:215], v[208:211], v[184:187], v[212:215]\n\t"
    "v_mfma_f32_32x32x16_bf16 v[144:159], v[184:187], v[8:11], v[144:159]\n\t"
    "ds_read_b64_tr_b16 v[180:181], v201 offset:4096\n\t"
    "ds_read_b64_tr_b16 v[182:183], v201 offset:4608\n\t"
    "v_fma_f32 v232, v160, v240, v224\n\t"
    "v_fma_f32 v233, v161, v240, v225\n\t"
    "s_waitcnt lgkmcnt(4)\n\t"
    "v_mfma_f32_32x32x16_bf16 v[144:159], v[188:191], v[12:15], v[144:159]\n\t"
    "ds_read_b64_tr_b16 v[184:185], v202 offset:0\n\t"
    "ds_read_b64_tr_b16 v[186:187], v202 offset:512\n\t"
    "v_mul_f32_e32 v236, v232, v232\n\t"
    "v_mul_f32_e32 v237, v233, v233\n\t"
    "v_fma_f32 v236, v236, s53, v241\n\t"
    "v_fma_f32 v237, v237, s53, v241\n\t"
    "v_mul_f32_e32 v236, v232, v236\n\t"
    "v_mul_f32_e32 v237, v233, v237\n\t"
    "s_waitcnt lgkmcnt(4)\n\t"
    "v_mfma_f32_32x32x16_bf16 v[144:159], v[176:179], v[16:19], v[144:159]\n\t"
    "ds_read_b64_tr_b16 v[188:189], v202 offset:2048\n\t"
    "ds_read_b64_tr_b16 v[190:191], v202 offset:2560\n\t"
    "v_exp_f32_e32 v236, v236\n\t"
    "v_exp_f32_e32 v237, v237\n\t"
    "s_waitcnt lgkmcnt(4)\n\t"
    "v_mfma_f32_32x32x16_bf16 v[144:159], v[180:183], v[20:23], v[144:159]\n\t"
    "ds_read_b64_tr_b16 v[176:177], v202 offset:4096\n\t"
    "ds_read_b64_tr_b16 v[178:179], v202 offset:4608\n\t"
    "ds_read_b128 v[228:231], v242 offset:32\n\t"
    "v_add_f32_e32 v238, 1.0, v236\n\t"
    "v_add_f32_e32 v239, 1.0, v237\n\t"
    "v_rcp_f32_e32 v238, v238\n\t"
    "v_rcp_f32_e32 v239, v239\n\t"
    "s_waitcnt lgkmcnt(5)\n\t"
    "v_mfma_f32_32x32x16_bf16 v[144:159], v[184:187], v[24:27], v[144:159]\n\t"
    "ds_read_b64_tr_b16 v[180:181], v203 offset:0\n\t"
    "ds_read_b64_tr_b16 v[182:183], v203 offset:512\n\t"
    "s_waitcnt vmcnt(11)\n\t"
    "s_barrier\n\t"
    "v_mul_f32_e32 v238, v232, v238\n\t"
    "v_mul_f32_e32 v239, v233, v239\n\t"
    "v_cvt_pk_bf16_f32 v220, v238, v239\n\t"
    "v_fma_f32 v234, v162, v240, v226\n\t"
    "v_fma_f32 v235, v163, v240, v227\n\t"
    "s_waitcnt lgkmcnt(5)\n\t"
    "v_mfma_f32_32x32x16_bf16 v[144:159], v[188:191], v[28:31], v[144:159]\n\t"
    "ds_read_b64_tr_b16 v[184:185], v203 offset:2048\n\t"
    "ds_read_b64_tr_b16 v[186:187], v203 offset:2560\n\t"
    "s_add_u32 m0, s51, 49152\n\t"
    "s_add_u32 s46, s40, 128\n\t"
    "s_addc_u32 s47, s41, 0\n\t"
    "global_load_lds_dwordx4 v251, s[46:47]\n\t"
    "v_mul_f32_e32 v236, v234, v234\n\t"
    "v_mul_f32_e32 v237, v235, v235\n\t"
    "v_fma_f32 v236, v236, s53, v241\n\t"
    "v_fma_f32 v237, v237, s53, v241\n\t"
    "v_mul_f32_e32 v236, v234, v236\n\t"
    "v_mul_f32_e32 v237, v235, v237\n\t"
    "s_waitcnt lgkmcnt(5)\n\t"
    "v_mfma_f32_32x32x16_bf16 v[144:159], v[176:179], v[32:35], v[144:159]\n\t"
    "ds_read_b64_tr_b16 v[188:189], v203 offset:4096\n\t"
    "ds_read_b64_tr_b16 v[190:191], v203 offset:4608\n\t"
    "v_exp_f32_e32 v236, v236\n\t"
    "v_exp_f32_e32 v237, v237\n\t"
    "s_waitcnt lgkmcnt(4)\n\t"
    "v_mfma_f32_32x32x16_bf16 v[144:159], v[180:183], v[36:39], v[144:159]\n\t"
    "ds_read_b64_tr_b16 v[176:177], v200 offset:24576\n\t"
    "ds_read_b64_tr_b16 v[178:179], v200 offset:25088\n\t"
    "s_add_u32 m0, s51, 57344\n\t"
    "s_add_u32 s46, s40, 589952\n\t"
    "s_addc_u32 s47, s41, 0\n\t"
    "global_load_lds_dwordx4 v251, s[46:47]\n\t"
    "v_add_f32_e32 v238, 1.0, v236\n\t"
    "v_add_f32_e32 v239, 1.0, v237\n\t"
    "v_rcp_f32_e32 v238, v238\n\t"
    "v_rcp_f32_e32 v239, v239\n\t"
    "s_waitcnt lgkmcnt(4)\n\t"
    "v_mfma_f32_32x32x16_bf16 v[144:159], v[184:187], v[40:43], v[144:159]\n\t"
    "ds_read_b64_tr_b16 v[180:181], v200 offset:26624\n\t"
    "ds_read_b64_tr_b16 v[182:183], v200 offset:27136\n\t"
    "v_mul_f32_e32 v238, v234, v238\n\t"
    "v_mul_f32_e32 v239, v235, v239\n\t"
    "v_cvt_pk_bf16_f32 v221, v238, v239\n\t"
    "v_fma_f32 v232, v164, v240, v228\n\t"
    "v_fma_f32 v233, v165, v240, v229\n\t"
    "s_waitcnt lgkmcnt(4)\n\t"
    "v_mfma_f32_32x32x16_bf16 v[144:159], v[188:191], v[44:47], v[144:159]\n\t"
    "ds_read_b64_tr_b16 v[184:185], v200 offset:28672\n\t"
    "ds_read_b64_tr_b16 v[186:187], v200 offset:29184\n\t"
    "s_add_u32 m0, s51, 65536\n\t"
    "s_add_u32 s46, s40, 1179776\n\t"
    "s_addc_u32 s47, s41, 0\n\t"
    "global_load_lds_dwordx4 v251, s[46:47]\n\t"
    "v_mul_f32_e32 v236, v232, v232\n\t"
    "v_mul_f32_e32 v237, v233, v233\n\t"
    "v_fma_f32 v236, v236, s53, v241\n\t"
    "v_fma_f32 v237, v237, s53, v241\n\t"
    "v_mul_f32_e32 v236, v232, v236\n\t"
    "v_mul_f32_e32 v237, v233, v237\n\t"
    "s_waitcnt lgkmcnt(4)\n\t"
    "v_mfma_f32_16x16x32_bf16 v[212:215], v[208:211], v[176:179], v[212:215]\n\t"
    "v_mfma_f32_32x32x16_bf16 v[144:159], v[176:179], v[48:51], v[144:159]\n\t"
    "ds_read_b64_tr_b16 v[188:189], v201 offset:24576\n\t"
    "ds_read_b64_tr_b16 v[190:191], v201 offset:25088\n\t"
    "v_exp_f32_e32 v236, v236\n\t"
    "v_exp_f32_e32 v237, v237\n\t"
    "s_waitcnt lgkmcnt(4)\n\t"
    "v_mfma_f32_16x16x32_bf16 v[212:215], v[208:211], v[180:183], v[212:215]\n\t"
    "v_mfma_f32_32x32x16_bf16 v[144:159], v[180:183], v[52:55], v[144:159]\n\t"
    "ds_read_b64_tr_b16 v[176:177], v201 offset:26624\n\t"
    "ds_read_b64_tr_b16 v[178:179], v201 offset:27136\n\t"
    "ds_read_b128 v[224:227], v242 offset:64\n\t"
    "v_add_f32_e32 v238, 1.0, v236\n\t"
    "v_add_f32_e32 v239, 1.0, v237\n\t"
    "v_rcp_f32_e32 v238, v238\n\t"
    "v_rcp_f32_e32 v239, v239\n\t"
    "s_waitcnt lgkmcnt(5)\n\t"
    "v_mfma_f32_16x16x32_bf16 v[212:215], v[208:211], v[184:187], v[212:215]\n\t"
    "v_mfma_f32_32x32x16_bf16 v[144:159], v[184:187], v[56:59], v[144:159]\n\t"
    "ds_read_b64_tr_b16 v[180:181], v201 offset:28672\n\t"
    "ds_read_b64_tr_b16 v[182:183], v201 offset:29184\n\t"
    "v_mul_f32_e32 v238, v232, v238\n\t"
    "v_mul_f32_e32 v239, v233, v239\n\t"
    "v_cvt_pk_bf16_f32 v222, v238, v239\n\t"
    "v_fma_f32 v234, v166, v240, v230\n\t"
    "v_fma_f32 v235, v167, v240, v231\n\t"
    "s_waitcnt lgkmcnt(5)\n\t"
    "v_mfma_f32_32x32x16_bf16 v[144:159], v[188:191], v[60:63], v[144:159]\n\t"
    "ds_read_b64_tr_b16 v[184:185], v202 offset:24576\n\t"
    "ds_read_b64_tr_b16 v[186:187], v202 offset:25088\n\t"
    "v_mul_f32_e32 v236, v234, v234\n\t"
    "v_mul_f32_e32 v237, v235, v235\n\t"
    "v_fma_f32 v236, v236, s53, v241\n\t"
    "v_fma_f32 v237, v237, s53, v241\n\t"
    "v_mul_f32_e32 v236, v234, v236\n\t"
    "v_mul_f32_e32 v237, v235, v237\n\t"
    "s_waitcnt lgkmcnt(5)\n\t"
    "v_mfma_f32_32x32x16_bf16 v[144:159], v[176:179], v[64:67], v[144:159]\n\t"
    "ds_read_b64_tr_b16 v[188:189], v202 offset:26624\n\t"
    "ds_read_b64_tr_b16 v[190:191], v202 offset:27136\n\t"
    "v_exp_f32_e32 v236, v236\n\t"
    "v_exp_f32_e32 v237, v237\n\t"
    "s_waitcnt lgkmcnt(4)\n\t"
    "v_mfma_f32_32x32x16_bf16 v[144:159], v[180:183], v[68:71], v[144:159]\n\t"
    "ds_read_b64_tr_b16 v[176:177], v202 offset:28672\n\t"
    "ds_read_b64_tr_b16 v[178:179], v202 offset:29184\n\t"
    "v_add_f32_e32 v238, 1.0, v236\n\t"
    "v_add_f32_e32 v239, 1.0, v237\n\t"
    "v_rcp_f32_e32 v238, v238\n\t"
    "v_rcp_f32_e32 v239, v239\n\t"
    "s_waitcnt lgkmcnt(4)\n\t"
    "v_mfma_f32_32x32x16_bf16 v[144:159], v[184:187], v[72:75], v[144:159]\n\t"
    "ds_read_b64_tr_b16 v[180:181], v203 offset:24576\n\t"
    "ds_read_b64_tr_b16 v[182:183], v203 offset:25088\n\t"
    "s_waitcnt vmcnt(11)\n\t"
    "s_barrier\n\t"
    "v_mul_f32_e32 v238, v234, v238\n\t"
    "v_mul_f32_e32 v239, v235, v239\n\t"
    "v_cvt_pk_bf16_f32 v223, v238, v239\n\t"
    "global_store_dwordx4 v207, v[220:223], s[44:45] offset:0 sc1\n\t"
    "v_fma_f32 v232, v168, v240, v224\n\t"
    "v_fma_f32 v233, v169, v240, v225\n\t"
    "s_waitcnt lgkmcnt(4)\n\t"
    "v_mfma_f32_32x32x16_bf16 v[144:159], v[188:191], v[76:79], v[144:159]\n\t"
    "ds_read_b64_tr_b16 v[184:185], v203 offset:26624\n\t"
    "ds_read_b64_tr_b16 v[186:187], v203 offset:27136\n\t"
    "v_mul_f32_e32 v236, v232, v232\n\t"
    "v_mul_f32_e32 v237, v233, v233\n\t"
    "v_fma_f32 v236, v236, s53, v241\n\t"
    "v_fma_f32 v237, v237, s53, v241\n\t"
    "v_mul_f32_e32 v236, v232, v236\n\t"
    "v_mul_f32_e32 v237, v233, v237\n\t"
    "s_waitcnt lgkmcnt(4)\n\t"
    "v_mfma_f32_32x32x16_bf16 v[144:159], v[176:179], v[80:83], v[144:159]\n\t"
    "ds_read_b64_tr_b16 v[188:189], v203 offset:28672\n\t"
    "ds_read_b64_tr_b16 v[190:191], v203 offset:29184\n\t"
    "v_exp_f32_e32 v236, v236\n\t"
    "v_exp_f32_e32 v237, v237\n\t"
    "s_waitcnt lgkmcnt(4)\n\t"
    "v_mfma_f32_32x32x16_bf16 v[144:159], v[180:183], v[84:87], v[144:159]\n\t"
    "ds_read_b64_tr_b16 v[176:177], v200 offset:49152\n\t"
    "ds_read_b64_tr_b16 v[178:179], v200 offset:49664\n\t"
    "ds_read_b128 v[228:231], v242 offset:96\n\t"
    "v_add_f32_e32 v238, 1.0, v236\n\t"
    "v_add_f32_e32 v239, 1.0, v237\n\t"
    "v_rcp_f32_e32 v238, v238\n\t"
    "v_rcp_f32_e32 v239, v239\n\t"
    "s_waitcnt lgkmcnt(5)\n\t"
    "v_mfma_f32_32x32x16_bf16 v[144:159], v[184:187], v[88:91], v[144:159]\n\t"
    "ds_read_b64_tr_b16 v[180:181], v200 offset:51200\n\t"
    "ds_read_b64_tr_b16 v[182:183], v200 offset:51712\n\t"
    "v_mul_f32_e32 v238, v232, v238\n\t"
    "v_mul_f32_e32 v239, v233, v239\n\t"
    "v_cvt_pk_bf16_f32 v220, v238, v239\n\t"
    "v_fma_f32 v234, v170, v240, v226\n\t"
    "v_fma_f32 v235, v171, v240, v227\n\t"
    "s_waitcnt lgkmcnt(5)\n\t"
    "v_mfma_f32_32x32x16_bf16 v[144:159], v[188:191], v[92:95], v[144:159]\n\t"
    "ds_read_b64_tr_b16 v[184:185], v200 offset:53248\n\t"
    "ds_read_b64_tr_b16 v[186:187], v200 offset:53760\n\t"
    "v_mul_f32_e32 v236, v234, v234\n\t"
    "v_mul_f32_e32 v237, v235, v235\n\t"
    "v_fma_f32 v236, v236, s53, v241\n\t"
    "v_fma_f32 v237, v237, s53, v241\n\t"
    "v_mul_f32_e32 v236, v234, v236\n\t"
    "v_mul_f32_e32 v237, v235, v237\n\t"
    "s_waitcnt lgkmcnt(5)\n\t"
    "v_mfma_f32_16x16x32_bf16 v[212:215], v[208:211], v[176:179], v[212:215]\n\t"
    "v_mfma_f32_32x32x16_bf16 v[144:159], v[176:179], v[96:99], v[144:159]\n\t"
    "ds_read_b64_tr_b16 v[188:189], v201 offset:49152\n\t"
    "ds_read_b64_tr_b16 v[190:191], v201 offset:49664\n\t"
    "v_add_u32_e32 v200, s52, v200\n\t"
    "v_exp_f32_e32 v236, v236\n\t"
    "v_exp_f32_e32 v237, v237\n\t"
    "s_waitcnt lgkmcnt(4)\n\t"
    "v_mfma_f32_16x16x32_bf16 v[212:215], v[208:211], v[180:183], v[212:215]\n\t"
    "v_mfma_f32_32x32x16_bf16 v[144:159], v[180:183], v[100:103], v[144:159]\n\t"
    "ds_read_b64_tr_b16 v[176:177], v201 offset:51200\n\t"
    "ds_read_b64_tr_b16 v[178:179], v201 offset:51712\n\t"
    "v_add_u32_e32 v242, 0x100, v242\n\t"
    "v_add_f32_e32 v238, 1.0, v236\n\t"
    "v_add_f32_e32 v239, 1.0, v237\n\t"
    "v_rcp_f32_e32 v238, v238\n\t"
    "v_rcp_f32_e32 v239, v239\n\t"
    "s_waitcnt lgkmcnt(4)\n\t"
    "v_mfma_f32_16x16x32_bf16 v[212:215], v[208:211], v[184:187], v[212:215]\n\t"
    "v_mfma_f32_32x32x16_bf16 v[144:159], v[184:187], v[104:107], v[144:159]\n\t"
    "ds_read_b64_tr_b16 v[180:181], v201 offset:53248\n\t"
    "ds_read_b64_tr_b16 v[182:183], v201 offset:53760\n\t"
    "v_mul_f32_e32 v238, v234, v238\n\t"
    "v_mul_f32_e32 v239, v235, v239\n\t"
    "v_cvt_pk_bf16_f32 v221, v238, v239\n\t"
    "v_fma_f32 v232, v172, v240, v228\n\t"
    "v_fma_f32 v233, v173, v240, v229\n\t"
    "s_waitcnt lgkmcnt(4)\n\t"
    "v_mfma_f32_32x32x16_bf16 v[144:159], v[188:191], v[108:111], v[144:159]\n\t"
    "ds_read_b64_tr_b16 v[184:185], v202 offset:49152\n\t"
    "ds_read_b64_tr_b16 v[186:187], v202 offset:49664\n\t"
    "v_add_u32_e32 v201, s52, v201\n\t"
    "v_mul_f32_e32 v236, v232, v232\n\t"
    "v_mul_f32_e32 v237, v233, v233\n\t"
    "v_fma_f32 v236, v236, s53, v241\n\t"
    "v_fma_f32 v237, v237, s53, v241\n\t"
    "v_mul_f32_e32 v236, v232, v236\n\t"
    "v_mul_f32_e32 v237, v233, v237\n\t"
    "s_waitcnt lgkmcnt(4)\n\t"
    "v_mfma_f32_32x32x16_bf16 v[144:159], v[176:179], v[112:115], v[144:159]\n\t"
    "ds_read_b64_tr_b16 v[188:189], v202 offset:51200\n\t"
    "ds_read_b64_tr_b16 v[190:191], v202 offset:51712\n\t"
    "v_cvt_pk_bf16_f32 v213, v212, v212\n\t"
    "v_lshlrev_b32_e32 v213, 16, v213\n\t"
    "v_sub_f32_e32 v213, v212, v213\n\t"
    "v_cvt_pk_bf16_f32 v213, v212, v213\n\t"
    "v_add_u32_e32 v214, s55, v243\n\t"
    "ds_write_b32 v214, v213\n\t"
    "v_exp_f32_e32 v236, v236\n\t"
    "v_exp_f32_e32 v237, v237\n\t"
    "s_waitcnt lgkmcnt(5)\n\t"
    "v_mfma_f32_32x32x16_bf16 v[144:159], v[180:183], v[116:119], v[144:159]\n\t"
    "ds_read_b64_tr_b16 v[176:177], v202 offset:53248\n\t"
    "ds_read_b64_tr_b16 v[178:179], v202 offset:53760\n\t"
    "v_add_f32_e32 v238, 1.0, v236\n\t"
    "v_add_f32_e32 v239, 1.0, v237\n\t"
    "v_rcp_f32_e32 v238, v238\n\t"
    "v_rcp_f32_e32 v239, v239\n\t"
    "s_waitcnt lgkmcnt(5)\n\t"
    "v_mfma_f32_32x32x16_bf16 v[144:159], v[184:187], v[120:123], v[144:159]\n\t"
    "ds_read_b64_tr_b16 v[180:181], v203 offset:49152\n\t"
    "ds_read_b64_tr_b16 v[182:183], v203 offset:49664\n\t"
    "s_waitcnt lgkmcnt(4)\n\t"
    "s_waitcnt vmcnt(8)\n\t"
    "s_barrier\n\t"
    "ds_read_b128 v[244:247], v243\n\t"
    "v_add_u32_e32 v202, s52, v202\n\t"
    "v_mul_f32_e32 v238, v232, v238\n\t"
    "v_mul_f32_e32 v239, v233, v239\n\t"
    "v_cvt_pk_bf16_f32 v222, v238, v239\n\t"
    "v_fma_f32 v234, v174, v240, v230\n\t"
    "v_fma_f32 v235, v175, v240, v231\n\t"
    "v_mfma_f32_32x32x16_bf16 v[144:159], v[188:191], v[124:127], v[144:159]\n\t"
    "ds_read_b64_tr_b16 v[184:185], v203 offset:51200\n\t"
    "ds_read_b64_tr_b16 v[186:187], v203 offset:51712\n\t"
    "v_mul_f32_e32 v236, v234, v234\n\t"
    "v_mul_f32_e32 v237, v235, v235\n\t"
    "v_fma_f32 v236, v236, s53, v241\n\t"
    "v_fma_f32 v237, v237, s53, v241\n\t"
    "v_mul_f32_e32 v236, v234, v236\n\t"
    "v_mul_f32_e32 v237, v235, v237\n\t"
    "s_waitcnt lgkmcnt(5)\n\t"
    "v_mfma_f32_32x32x16_bf16 v[144:159], v[176:179], v[128:131], v[144:159]\n\t"
    "ds_read_b64_tr_b16 v[188:189], v203 offset:53248\n\t"
    "ds_read_b64_tr_b16 v[190:191], v203 offset:53760\n\t"
    "ds_read_b128 v[224:227], v242 offset:0\n\t"
    "v_exp_f32_e32 v236, v236\n\t"
    "v_exp_f32_e32 v237, v237\n\t"
    "s_waitcnt lgkmcnt(6)\n\t"
    "v_mfma_f32_32x32x16_bf16 v[144:159], v[180:183], v[132:135], v[144:159]\n\t"
    "ds_read_b64_tr_b16 v[176:177], v200 offset:0\n\t"
    "ds_read_b64_tr_b16 v[178:179], v200 offset:512\n\t"
    "v_add_u32_e32 v203, s52, v203\n\t"
    "v_add_f32_e32 v238, 1.0, v236\n\t"
    "v_add_f32_e32 v239, 1.0, v237\n\t"
    "v_rcp_f32_e32 v238, v238\n\t"
    "v_rcp_f32_e32 v239, v239\n\t"
    "s_waitcnt lgkmcnt(5)\n\t"
    "v_mfma_f32_32x32x16_bf16 v[144:159], v[184:187], v[136:139], v[144:159]\n\t"
    "ds_read_b64_tr_b16 v[180:181], v200 offset:2048\n\t"
    "ds_read_b64_tr_b16 v[182:183], v200 offset:2560\n\t"
    "v_mul_f32_e32 v238, v234, v238\n\t"
    "v_mul_f32_e32 v239, v235, v239\n\t"
    "v_cvt_pk_bf16_f32 v223, v238, v239\n\t"
    "global_store_dwordx4 v207, v[220:223], s[44:45] offset:1024 sc1\n\t"
    "s_waitcnt lgkmcnt(5)\n\t"
    "v_mfma_f32_32x32x16_bf16 v[144:159], v[244:247], v[216:219], v[144:159]\n\t"
    "v_mfma_f32_32x32x16_bf16 v[160:175], v[188:191], v[140:143], v[144:159]\n\t"
    "ds_read_b64_tr_b16 v[184:185], v200 offset:4096\n\t"
    "ds_read_b64_tr_b16 v[186:187], v200 offset:4608\n\t"
    "s_sub_u32 s52, 0, s52\n\t"
    "s_mov_b32 s46, s50\n\t"
    "s_mov_b32 s50, s51\n\t"
    "s_mov_b32 s51, s46\n\t"
    "s_add_u32 s40, s40, 128\n\t"
    "s_addc_u32 s41, s41, 0\n\t"
    "s_add_u32 s44, s44, 4096\n\t"
    "s_addc_u32 s45, s45, 0\n\t"
    "s_waitcnt lgkmcnt(4)\n\t"
    "v_mfma_f32_16x16x32_bf16 v[212:215], v[208:211], v[176:179], 0\n\t"
    "v_mfma_f32_32x32x16_bf16 v[144:159], v[176:179], v[0:3], 0\n\t"
    "ds_read_b64_tr_b16 v[188:189], v201 offset:0\n\t"
    "ds_read_b64_tr_b16 v[190:191], v201 offset:512\n\t"
    "s_waitcnt lgkmcnt(4)\n\t"
    "v_mfma_f32_16x16x32_bf16 v[212:215], v[208:211], v[180:183], v[212:215]\n\t"
    "v_mfma_f32_32x32x16_bf16 v[144:159], v[180:183], v[4:7], v[144:159]\n\t"
    "ds_read_b64_tr_b16 v[176:177], v201 offset:2048\n\t"
    "ds_read_b64_tr_b16 v[178:179], v201 offset:2560\n\t"
    "s_waitcnt lgkmcnt(4)\n\t"
    "v_mfma_f32_16x16x32_bf16 v[212:215], v[208:211], v[184:187], v[212:215]\n\t"
    "v_mfma_f32_32x32x16_bf16 v[144:159], v[184:187], v[8:11], v[144:159]\n\t"
    "ds_read_b64_tr_b16 v[180:181], v201 offset:4096\n\t"
    "ds_read_b64_tr_b16 v[182:183], v201 offset:4608\n\t"
    "v_fma_f32 v232, v160, v240, v224\n\t"
    "v_fma_f32 v233, v161, v240, v225\n\t"
    "s_waitcnt lgkmcnt(4)\n\t"
    "v_mfma_f32_32x32x16_bf16 v[144:159], v[188:191], v[12:15], v[144:159]\n\t"
    "ds_read_b64_tr_b16 v[184:185], v202 offset:0\n\t"
    "ds_read_b64_tr_b16 v[186:187], v202 offset:512\n\t"
    "v_mul_f32_e32 v236, v232, v232\n\t"
    "v_mul_f32_e32 v237, v233, v233\n\t"
    "v_fma_f32 v236, v236, s53, v241\n\t"
    "v_fma_f32 v237, v237, s53, v241\n\t"
    "v_mul_f32_e32 v236, v232, v236\n\t"
    "v_mul_f32_e32 v237, v233, v237\n\t"
    "s_waitcnt lgkmcnt(4)\n\t"
    "v_mfma_f32_32x32x16_bf16 v[144:159], v[176:179], v[16:19], v[144:159]\n\t"
    "ds_read_b64_tr_b16 v[188:189], v202 offset:2048\n\t"
    "ds_read_b64_tr_b16 v[190:191], v202 offset:2560\n\t"
    "v_exp_f32_e32 v236, v236\n\t"
    "v_exp_f32_e32 v237, v237\n\t"
    "s_waitcnt lgkmcnt(4)\n\t"
    "v_mfma_f32_32x32x16_bf16 v[144:159], v[180:183], v[20:23], v[144:159]\n\t"
    "ds_read_b64_tr_b16 v[176:177], v202 offset:4096\n\t"
    "ds_read_b64_tr_b16 v[178:179], v202 offset:4608\n\t"
    "ds_read_b128 v[228:231], v242 offset:32\n\t"
    "v_add_f32_e32 v238, 1.0, v236\n\t"
    "v_add_f32_e32 v239, 1.0, v237\n\t"
    "v_rcp_f32_e32 v238, v238\n\t"
    "v_rcp_f32_e32 v239, v239\n\t"
    "s_waitcnt lgkmcnt(5)\n\t"
    "v_mfma_f32_32x32x16_bf16 v[144:159], v[184:187], v[24:27], v[144:159]\n\t"
    "ds_read_b64_tr_b16 v[180:181], v203 offset:0\n\t"
    "ds_read_b64_tr_b16 v[182:183], v203 offset:512\n\t"
    "s_waitcnt vmcnt(5)\n\t"
    "s_barrier\n\t"
    "v_mul_f32_e32 v238, v232, v238\n\t"
    "v_mul_f32_e32 v239, v233, v239\n\t"
    "v_cvt_pk_bf16_f32 v220, v238, v239\n\t"
    "v_fma_f32 v234, v162, v240, v226\n\t"
    "v_fma_f32 v235, v163, v240, v227\n\t"
    "s_waitcnt lgkmcnt(5)\n\t"
    "v_mfma_f32_32x32x16_bf16 v[144:159], v[188:191], v[28:31], v[144:159]\n\t"
    "ds_read_b64_tr_b16 v[184:185], v203 offset:2048\n\t"
    "ds_read_b64_tr_b16 v[186:187], v203 offset:2560\n\t"
    "v_mul_f32_e32 v236, v234, v234\n\t"
    "v_mul_f32_e32 v237, v235, v235\n\t"
    "v_fma_f32 v236, v236, s53, v241\n\t"
    "v_fma_f32 v237, v237, s53, v241\n\t"
    "v_mul_f32_e32 v236, v234, v236\n\t"
    "v_mul_f32_e32 v237, v235, v237\n\t"
    "s_waitcnt lgkmcnt(5)\n\t"
    "v_mfma_f32_32x32x16_bf16 v[144:159], v[176:179], v[32:35], v[144:159]\n\t"
    "ds_read_b64_tr_b16 v[188:189], v203 offset:4096\n\t"
    "ds_read_b64_tr_b16 v[190:191], v203 offset:4608\n\t"
    "v_exp_f32_e32 v236, v236\n\t"
    "v_exp_f32_e32 v237, v237\n\t"
    "s_waitcnt lgkmcnt(4)\n\t"
    "v_mfma_f32_32x32x16_bf16 v[144:159], v[180:183], v[36:39], v[144:159]\n\t"
    "ds_read_b64_tr_b16 v[176:177], v200 offset:24576\n\t"
    "ds_read_b64_tr_b16 v[178:179], v200 offset:25088\n\t"
    "v_add_f32_e32 v238, 1.0, v236\n\t"
    "v_add_f32_e32 v239, 1.0, v237\n\t"
    "v_rcp_f32_e32 v238, v238\n\t"
    "v_rcp_f32_e32 v239, v239\n\t"
    "s_waitcnt lgkmcnt(4)\n\t"
    "v_mfma_f32_32x32x16_bf16 v[144:159], v[184:187], v[40:43], v[144:159]\n\t"
    "ds_read_b64_tr_b16 v[180:181], v200 offset:26624\n\t"
    "ds_read_b64_tr_b16 v[182:183], v200 offset:27136\n\t"
    "v_mul_f32_e32 v238, v234, v238\n\t"
    "v_mul_f32_e32 v239, v235, v239\n\t"
    "v_cvt_pk_bf16_f32 v221, v238, v239\n\t"
    "v_fma_f32 v232, v164, v240, v228\n\t"
    "v_fma_f32 v233, v165, v240, v229\n\t"
    "s_waitcnt lgkmcnt(4)\n\t"
    "v_mfma_f32_32x32x16_bf16 v[144:159], v[188:191], v[44:47], v[144:159]\n\t"
    "ds_read_b64_tr_b16 v[184:185], v200 offset:28672\n\t"
    "ds_read_b64_tr_b16 v[186:187], v200 offset:29184\n\t"
    "v_mul_f32_e32 v236, v232, v232\n\t"
    "v_mul_f32_e32 v237, v233, v233\n\t"
    "v_fma_f32 v236, v236, s53, v241\n\t"
    "v_fma_f32 v237, v237, s53, v241\n\t"
    "v_mul_f32_e32 v236, v232, v236\n\t"
    "v_mul_f32_e32 v237, v233, v237\n\t"
    "s_waitcnt lgkmcnt(4)\n\t"
    "v_mfma_f32_16x16x32_bf16 v[212:215], v[208:211], v[176:179], v[212:215]\n\t"
    "v_mfma_f32_32x32x16_bf16 v[144:159], v[176:179], v[48:51], v[144:159]\n\t"
    "ds_read_b64_tr_b16 v[188:189], v201 offset:24576\n\t"
    "ds_read_b64_tr_b16 v[190:191], v201 offset:25088\n\t"
    "v_exp_f32_e32 v236, v236\n\t"
    "v_exp_f32_e32 v237, v237\n\t"
    "s_waitcnt lgkmcnt(4)\n\t"
    "v_mfma_f32_16x16x32_bf16 v[212:215], v[208:211], v[180:183], v[212:215]\n\t"
    "v_mfma_f32_32x32x16_bf16 v[144:159], v[180:183], v[52:55], v[144:159]\n\t"
    "ds_read_b64_tr_b16 v[176:177], v201 offset:26624\n\t"
    "ds_read_b64_tr_b16 v[178:179], v201 offset:27136\n\t"
    "ds_read_b128 v[224:227], v242 offset:64\n\t"
    "v_add_f32_e32 v238, 1.0, v236\n\t"
    "v_add_f32_e32 v239, 1.0, v237\n\t"
    "v_rcp_f32_e32 v238, v238\n\t"
    "v_rcp_f32_e32 v239, v239\n\t"
    "s_waitcnt lgkmcnt(5)\n\t"
    "v_mfma_f32_16x16x32_bf16 v[212:215], v[208:211], v[184:187], v[212:215]\n\t"
    "v_mfma_f32_32x32x16_bf16 v[144:159], v[184:187], v[56:59], v[144:159]\n\t"
    "ds_read_b64_tr_b16 v[180:181], v201 offset:28672\n\t"
    "ds_read_b64_tr_b16 v[182:183], v201 offset:29184\n\t"
    "v_mul_f32_e32 v238, v232, v238\n\t"
    "v_mul_f32_e32 v239, v233, v239\n\t"
    "v_cvt_pk_bf16_f32 v222, v238, v239\n\t"
    "v_fma_f32 v234, v166, v240, v230\n\t"
    "v_fma_f32 v235, v167, v240, v231\n\t"
    "s_waitcnt lgkmcnt(5)\n\t"
    "v_mfma_f32_32x32x16_bf16 v[144:159], v[188:191], v[60:63], v[144:159]\n\t"
    "ds_read_b64_tr_b16 v[184:185], v202 offset:24576\n\t"
    "ds_read_b64_tr_b16 v[186:187], v202 offset:25088\n\t"
    "v_mul_f32_e32 v236, v234, v234\n\t"
    "v_mul_f32_e32 v237, v235, v235\n\t"
    "v_fma_f32 v236, v236, s53, v241\n\t"
    "v_fma_f32 v237, v237, s53, v241\n\t"
    "v_mul_f32_e32 v236, v234, v236\n\t"
    "v_mul_f32_e32 v237, v235, v237\n\t"
    "s_waitcnt lgkmcnt(5)\n\t"
    "v_mfma_f32_32x32x16_bf16 v[144:159], v[176:179], v[64:67], v[144:159]\n\t"
    "ds_read_b64_tr_b16 v[188:189], v202 offset:26624\n\t"
    "ds_read_b64_tr_b16 v[190:191], v202 offset:27136\n\t"
    "v_exp_f32_e32 v236, v236\n\t"
    "v_exp_f32_e32 v237, v237\n\t"
    "s_waitcnt lgkmcnt(4)\n\t"
    "v_mfma_f32_32x32x16_bf16 v[144:159], v[180:183], v[68:71], v[144:159]\n\t"
    "ds_read_b64_tr_b16 v[176:177], v202 offset:28672\n\t"
    "ds_read_b64_tr_b16 v[178:179], v202 offset:29184\n\t"
    "v_add_f32_e32 v238, 1.0, v236\n\t"
    "v_add_f32_e32 v239, 1.0, v237\n\t"
    "v_rcp_f32_e32 v238, v238\n\t"
    "v_rcp_f32_e32 v239, v239\n\t"
    "s_waitcnt lgkmcnt(4)\n\t"
    "v_mfma_f32_32x32x16_bf16 v[144:159], v[184:187], v[72:75], v[144:159]\n\t"
    "ds_read_b64_tr_b16 v[180:181], v203 offset:24576\n\t"
    "ds_read_b64_tr_b16 v[182:183], v203 offset:25088\n\t"
    "s_waitcnt vmcnt(2)\n\t"
    "s_barrier\n\t"
    "v_mul_f32_e32 v238, v234, v238\n\t"
    "v_mul_f32_e32 v239, v235, v239\n\t"
    "v_cvt_pk_bf16_f32 v223, v238, v239\n\t"
    "global_store_dwordx4 v207, v[220:223], s[44:45] offset:0 sc1\n\t"
    "v_fma_f32 v232, v168, v240, v224\n\t"
    "v_fma_f32 v233, v169, v240, v225\n\t"
    "s_waitcnt lgkmcnt(4)\n\t"
    "v_mfma_f32_32x32x16_bf16 v[144:159], v[188:191], v[76:79], v[144:159]\n\t"
    "ds_read_b64_tr_b16 v[184:185], v203 offset:26624\n\t"
    "ds_read_b64_tr_b16 v[186:187], v203 offset:27136\n\t"
    "v_mul_f32_e32 v236, v232, v232\n\t"
    "v_mul_f32_e32 v237, v233, v233\n\t"
    "v_fma_f32 v236, v236, s53, v241\n\t"
    "v_fma_f32 v237, v237, s53, v241\n\t"
    "v_mul_f32_e32 v236, v232, v236\n\t"
    "v_mul_f32_e32 v237, v233, v237\n\t"
    "s_waitcnt lgkmcnt(4)\n\t"
    "v_mfma_f32_32x32x16_bf16 v[144:159], v[176:179], v[80:83], v[144:159]\n\t"
    "ds_read_b64_tr_b16 v[188:189], v203 offset:28672\n\t"
    "ds_read_b64_tr_b16 v[190:191], v203 offset:29184\n\t"
    "v_exp_f32_e32 v236, v236\n\t"
    "v_exp_f32_e32 v237, v237\n\t"
    "s_waitcnt lgkmcnt(4)\n\t"
    "v_mfma_f32_32x32x16_bf16 v[144:159], v[180:183], v[84:87], v[144:159]\n\t"
    "ds_read_b64_tr_b16 v[176:177], v200 offset:49152\n\t"
    "ds_read_b64_tr_b16 v[178:179], v200 offset:49664\n\t"
    "ds_read_b128 v[228:231], v242 offset:96\n\t"
    "v_add_f32_e32 v238, 1.0, v236\n\t"
    "v_add_f32_e32 v239, 1.0, v237\n\t"
    "v_rcp_f32_e32 v238, v238\n\t"
    "v_rcp_f32_e32 v239, v239\n\t"
    "s_waitcnt lgkmcnt(5)\n\t"
    "v_mfma_f32_32x32x16_bf16 v[144:159], v[184:187], v[88:91], v[144:159]\n\t"
    "ds_read_b64_tr_b16 v[180:181], v200 offset:51200\n\t"
    "ds_read_b64_tr_b16 v[182:183], v200 offset:51712\n\t"
    "v_mul_f32_e32 v238, v232, v238\n\t"
    "v_mul_f32_e32 v239, v233, v239\n\t"
    "v_cvt_pk_bf16_f32 v220, v238, v239\n\t"
    "v_fma_f32 v234, v170, v240, v226\n\t"
    "v_fma_f32 v235, v171, v240, v227\n\t"
    "s_waitcnt lgkmcnt(5)\n\t"
    "v_mfma_f32_32x32x16_bf16 v[144:159], v[188:191], v[92:95], v[144:159]\n\t"
    "ds_read_b64_tr_b16 v[184:185], v200 offset:53248\n\t"
    "ds_read_b64_tr_b16 v[186:187], v200 offset:53760\n\t"
    "v_mul_f32_e32 v236, v234, v234\n\t"
    "v_mul_f32_e32 v237, v235, v235\n\t"
    "v_fma_f32 v236, v236, s53, v241\n\t"
    "v_fma_f32 v237, v237, s53, v241\n\t"
    "v_mul_f32_e32 v236, v234, v236\n\t"
    "v_mul_f32_e32 v237, v235, v237\n\t"
    "s_waitcnt lgkmcnt(5)\n\t"
    "v_mfma_f32_16x16x32_bf16 v[212:215], v[208:211], v[176:179], v[212:215]\n\t"
    "v_mfma_f32_32x32x16_bf16 v[144:159], v[176:179], v[96:99], v[144:159]\n\t"
    "ds_read_b64_tr_b16 v[188:189], v201 offset:49152\n\t"
    "ds_read_b64_tr_b16 v[190:191], v201 offset:49664\n\t"
    "v_exp_f32_e32 v236, v236\n\t"
    "v_exp_f32_e32 v237, v237\n\t"
    "s_waitcnt lgkmcnt(4)\n\t"
    "v_mfma_f32_16x16x32_bf16 v[212:215], v[208:211], v[180:183], v[212:215]\n\t"
    "v_mfma_f32_32x32x16_bf16 v[144:159], v[180:183], v[100:103], v[144:159]\n\t"
    "ds_read_b64_tr_b16 v[176:177], v201 offset:51200\n\t"
    "ds_read_b64_tr_b16 v[178:179], v201 offset:51712\n\t"
    "v_add_u32_e32 v242, 0x100, v242\n\t"
    "v_add_f32_e32 v238, 1.0, v236\n\t"
    "v_add_f32_e32 v239, 1.0, v237\n\t"
    "v_rcp_f32_e32 v238, v238\n\t"
    "v_rcp_f32_e32 v239, v239\n\t"
    "s_waitcnt lgkmcnt(4)\n\t"
    "v_mfma_f32_16x16x32_bf16 v[212:215], v[208:211], v[184:187], v[212:215]\n\t"
    "v_mfma_f32_32x32x16_bf16 v[144:159], v[184:187], v[104:107], v[144:159]\n\t"
    "ds_read_b64_tr_b16 v[180:181], v201 offset:53248\n\t"
    "ds_read_b64_tr_b16 v[182:183], v201 offset:53760\n\t"
    "v_mul_f32_e32 v238, v234, v238\n\t"
    "v_mul_f32_e32 v239, v235, v239\n\t"
    "v_cvt_pk_bf16_f32 v221, v238, v239\n\t"
    "v_fma_f32 v232, v172, v240, v228\n\t"
    "v_fma_f32 v233, v173, v240, v229\n\t"
    "s_waitcnt lgkmcnt(4)\n\t"
    "v_mfma_f32_32x32x16_bf16 v[144:159], v[188:191], v[108:111], v[144:159]\n\t"
    "ds_read_b64_tr_b16 v[184:185], v202 offset:49152\n\t"
    "ds_read_b64_tr_b16 v[186:187], v202 offset:49664\n\t"
    "v_mul_f32_e32 v236, v232, v232\n\t"
    "v_mul_f32_e32 v237, v233, v233\n\t"
    "v_fma_f32 v236, v236, s53, v241\n\t"
    "v_fma_f32 v237, v237, s53, v241\n\t"
    "v_mul_f32_e32 v236, v232, v236\n\t"
    "v_mul_f32_e32 v237, v233, v237\n\t"
    "s_waitcnt lgkmcnt(4)\n\t"
    "v_mfma_f32_32x32x16_bf16 v[144:159], v[176:179], v[112:115], v[144:159]\n\t"
    "ds_read_b64_tr_b16 v[188:189], v202 offset:51200\n\t"
    "ds_read_b64_tr_b16 v[190:191], v202 offset:51712\n\t"
    "v_cvt_pk_bf16_f32 v213, v212, v212\n\t"
    "v_lshlrev_b32_e32 v213, 16, v213\n\t"
    "v_sub_f32_e32 v213, v212, v213\n\t"
    "v_cvt_pk_bf16_f32 v213, v212, v213\n\t"
    "v_add_u32_e32 v214, s55, v243\n\t"
    "ds_write_b32 v214, v213\n\t"
    "v_exp_f32_e32 v236, v236\n\t"
    "v_exp_f32_e32 v237, v237\n\t"
    "s_waitcnt lgkmcnt(5)\n\t"
    "v_mfma_f32_32x32x16_bf16 v[144:159], v[180:183], v[116:119], v[144:159]\n\t"
    "ds_read_b64_tr_b16 v[176:177], v202 offset:53248\n\t"
    "ds_read_b64_tr_b16 v[178:179], v202 offset:53760\n\t"
    "v_add_f32_e32 v238, 1.0, v236\n\t"
    "v_add_f32_e32 v239, 1.0, v237\n\t"
    "v_rcp_f32_e32 v238, v238\n\t"
    "v_rcp_f32_e32 v239, v239\n\t"
    "s_waitcnt lgkmcnt(5)\n\t"
    "v_mfma_f32_32x32x16_bf16 v[144:159], v[184:187], v[120:123], v[144:159]\n\t"
    "ds_read_b64_tr_b16 v[180:181], v203 offset:49152\n\t"
    "ds_read_b64_tr_b16 v[182:183], v203 offset:49664\n\t"
    "s_waitcnt lgkmcnt(4)\n\t"
    "s_barrier\n\t"
    "ds_read_b128 v[244:247], v243\n\t"
    "v_mul_f32_e32 v238, v232, v238\n\t"
    "v_mul_f32_e32 v239, v233, v239\n\t"
    "v_cvt_pk_bf16_f32 v222, v238, v239\n\t"
    "v_fma_f32 v234, v174, v240, v230\n\t"
    "v_fma_f32 v235, v175, v240, v231\n\t"
    "v_mfma_f32_32x32x16_bf16 v[144:159], v[188:191], v[124:127], v[144:159]\n\t"
    "ds_read_b64_tr_b16 v[184:185], v203 offset:51200\n\t"
    "ds_read_b64_tr_b16 v[186:187], v203 offset:51712\n\t"
    "v_mul_f32_e32 v236, v234, v234\n\t"
    "v_mul_f32_e32 v237, v235, v235\n\t"
    "v_fma_f32 v236, v236, s53, v241\n\t"
    "v_fma_f32 v237, v237, s53, v241\n\t"
    "v_mul_f32_e32 v236, v234, v236\n\t"
    "v_mul_f32_e32 v237, v235, v237\n\t"
    "s_waitcnt lgkmcnt(5)\n\t"
    "v_mfma_f32_32x32x16_bf16 v[144:159], v[176:179], v[128:131], v[144:159]\n\t"
    "ds_read_b64_tr_b16 v[188:189], v203 offset:53248\n\t"
    "ds_read_b64_tr_b16 v[190:191], v203 offset:53760\n\t"
    "ds_read_b128 v[224:227], v242 offset:0\n\t"
    "v_exp_f32_e32 v236, v236\n\t"
    "v_exp_f32_e32 v237, v237\n\t"
    "s_waitcnt lgkmcnt(6)\n\t"
    "v_mfma_f32_32x32x16_bf16 v[144:159], v[180:183], v[132:135], v[144:159]\n\t"
    "v_add_f32_e32 v238, 1.0, v236\n\t"
    "v_add_f32_e32 v239, 1.0, v237\n\t"
    "v_rcp_f32_e32 v238, v238\n\t"
    "v_rcp_f32_e32 v239, v239\n\t"
    "s_waitcnt lgkmcnt(3)\n\t"
    "v_mfma_f32_32x32x16_bf16 v[144:159], v[184:187], v[136:139], v[144:159]\n\t"
    "v_mul_f32_e32 v238, v234, v238\n\t"
    "v_mul_f32_e32 v239, v235, v239\n\t"
    "v_cvt_pk_bf16_f32 v223, v238, v239\n\t"
    "global_store_dwordx4 v207, v[220:223], s[44:45] offset:1024 sc1\n\t"
    "s_waitcnt lgkmcnt(1)\n\t"
    "v_mfma_f32_32x32x16_bf16 v[144:159], v[244:247], v[216:219], v[144:159]\n\t"
    "v_mfma_f32_32x32x16_bf16 v[160:175], v[188:191], v[140:143], v[144:159]\n\t"
    "s_sub_u32 s52, 0, s52\n\t"
    "s_mov_b32 s46, s50\n\t"
    "s_mov_b32 s50, s51\n\t"
    "s_mov_b32 s51, s46\n\t"
    "s_add_u32 s40, s40, 128\n\t"
    "s_addc_u32 s41, s41, 0\n\t"
    "s_add_u32 s44, s44, 4096\n\t"
    "s_addc_u32 s45, s45, 0\n\t"
    "v_lshlrev_b32_e32 v0, 2, v207\n\t"
    "global_load_dword v1, v0, s[38:39]\n\t"
    "ds_read_b128 v[228:231], v242 offset:32\n\t"
    "s_nop 11\n\t"
    "s_waitcnt lgkmcnt(1)\n\t"
    "v_fma_f32 v232, v160, v240, v224\n\t"
    "v_fma_f32 v233, v161, v240, v225\n\t"
    "v_mul_f32_e32 v236, v232, v232\n\t"
    "v_mul_f32_e32 v237, v233, v233\n\t"
    "v_fma_f32 v236, v236, s53, v241\n\t"
    "v_fma_f32 v237, v237, s53, v241\n\t"
    "v_mul_f32_e32 v236, v232, v236\n\t"
    "v_mul_f32_e32 v237, v233, v237\n\t"
    "v_exp_f32_e32 v236, v236\n\t"
    "v_exp_f32_e32 v237, v237\n\t"
    "v_add_f32_e32 v238, 1.0, v236\n\t"
    "v_add_f32_e32 v239, 1.0, v237\n\t"
    "v_rcp_f32_e32 v238, v238\n\t"
    "v_rcp_f32_e32 v239, v239\n\t"
    "v_mul_f32_e32 v238, v232, v238\n\t"
    "v_mul_f32_e32 v239, v233, v239\n\t"
    "v_cvt_pk_bf16_f32 v220, v238, v239\n\t"
    "v_fma_f32 v234, v162, v240, v226\n\t"
    "v_fma_f32 v235, v163, v240, v227\n\t"
    "v_mul_f32_e32 v236, v234, v234\n\t"
    "v_mul_f32_e32 v237, v235, v235\n\t"
    "v_fma_f32 v236, v236, s53, v241\n\t"
    "v_fma_f32 v237, v237, s53, v241\n\t"
    "v_mul_f32_e32 v236, v234, v236\n\t"
    "v_mul_f32_e32 v237, v235, v237\n\t"
    "v_exp_f32_e32 v236, v236\n\t"
    "v_exp_f32_e32 v237, v237\n\t"
    "v_add_f32_e32 v238, 1.0, v236\n\t"
    "v_add_f32_e32 v239, 1.0, v237\n\t"
    "v_rcp_f32_e32 v238, v238\n\t"
    "v_rcp_f32_e32 v239, v239\n\t"
    "ds_read_b128 v[224:227], v242 offset:64\n\t"
    "v_mul_f32_e32 v238, v234, v238\n\t"
    "v_mul_f32_e32 v239, v235, v239\n\t"
    "v_cvt_pk_bf16_f32 v221, v238, v239\n\t"
    "s_waitcnt lgkmcnt(1)\n\t"
    "v_fma_f32 v232, v164, v240, v228\n\t"
    "v_fma_f32 v233, v165, v240, v229\n\t"
    "v_mul_f32_e32 v236, v232, v232\n\t"
    "v_mul_f32_e32 v237, v233, v233\n\t"
    "v_fma_f32 v236, v236, s53, v241\n\t"
    "v_fma_f32 v237, v237, s53, v241\n\t"
    "v_mul_f32_e32 v236, v232, v236\n\t"
    "v_mul_f32_e32 v237, v233, v237\n\t"
    "v_exp_f32_e32 v236, v236\n\t"
    "v_exp_f32_e32 v237, v237\n\t"
    "v_add_f32_e32 v238, 1.0, v236\n\t"
    "v_add_f32_e32 v239, 1.0, v237\n\t"
    "v_rcp_f32_e32 v238, v238\n\t"
    "v_rcp_f32_e32 v239, v239\n\t"
    "v_mul_f32_e32 v238, v232, v238\n\t"
    "v_mul_f32_e32 v239, v233, v239\n\t"
    "v_cvt_pk_bf16_f32 v222, v238, v239\n\t"
    "v_fma_f32 v234, v166, v240, v230\n\t"
    "v_fma_f32 v235, v167, v240, v231\n\t"
    "v_mul_f32_e32 v236, v234, v234\n\t"
    "v_mul_f32_e32 v237, v235, v235\n\t"
    "v_fma_f32 v236, v236, s53, v241\n\t"
    "v_fma_f32 v237, v237, s53, v241\n\t"
    "v_mul_f32_e32 v236, v234, v236\n\t"
    "v_mul_f32_e32 v237, v235, v237\n\t"
    "v_exp_f32_e32 v236, v236\n\t"
    "v_exp_f32_e32 v237, v237\n\t"
    "v_add_f32_e32 v238, 1.0, v236\n\t"
    "v_add_f32_e32 v239, 1.0, v237\n\t"
    "v_rcp_f32_e32 v238, v238\n\t"
    "v_rcp_f32_e32 v239, v239\n\t"
    "ds_read_b128 v[228:231], v242 offset:96\n\t"
    "v_mul_f32_e32 v238, v234, v238\n\t"
    "v_mul_f32_e32 v239, v235, v239\n\t"
    "v_cvt_pk_bf16_f32 v223, v238, v239\n\t"
    "global_store_dwordx4 v207, v[220:223], s[44:45] offset:0 sc1\n\t"
    "s_waitcnt lgkmcnt(1)\n\t"
    "v_fma_f32 v232, v168, v240, v224\n\t"
    "v_fma_f32 v233, v169, v240, v225\n\t"
    "v_mul_f32_e32 v236, v232, v232\n\t"
    "v_mul_f32_e32 v237, v233, v233\n\t"
    "v_fma_f32 v236, v236, s53, v241\n\t"
    "v_fma_f32 v237, v237, s53, v241\n\t"
    "v_mul_f32_e32 v236, v232, v236\n\t"
    "v_mul_f32_e32 v237, v233, v237\n\t"
    "v_exp_f32_e32 v236, v236\n\t"
    "v_exp_f32_e32 v237, v237\n\t"
    "v_add_f32_e32 v238, 1.0, v236\n\t"
    "v_add_f32_e32 v239, 1.0, v237\n\t"
    "v_rcp_f32_e32 v238, v238\n\t"
    "v_rcp_f32_e32 v239, v239\n\t"
    "v_mul_f32_e32 v238, v232, v238\n\t"
    "v_mul_f32_e32 v239, v233, v239\n\t"
    "v_cvt_pk_bf16_f32 v220, v238, v239\n\t"
    "v_fma_f32 v234, v170, v240, v226\n\t"
    "v_fma_f32 v235, v171, v240, v227\n\t"
    "v_mul_f32_e32 v236, v234, v234\n\t"
    "v_mul_f32_e32 v237, v235, v235\n\t"
    "v_fma_f32 v236, v236, s53, v241\n\t"
    "v_fma_f32 v237, v237, s53, v241\n\t"
    "v_mul_f32_e32 v236, v234, v236\n\t"
    "v_mul_f32_e32 v237, v235, v237\n\t"
    "v_exp_f32_e32 v236, v236\n\t"
    "v_exp_f32_e32 v237, v237\n\t"
    "v_add_f32_e32 v238, 1.0, v236\n\t"
    "v_add_f32_e32 v239, 1.0, v237\n\t"
    "v_rcp_f32_e32 v238, v238\n\t"
    "v_rcp_f32_e32 v239, v239\n\t"
    "v_mul_f32_e32 v238, v234, v238\n\t"
    "v_mul_f32_e32 v239, v235, v239\n\t"
    "v_cvt_pk_bf16_f32 v221, v238, v239\n\t"
    "s_waitcnt lgkmcnt(0)\n\t"
    "v_fma_f32 v232, v172, v240, v228\n\t"
    "v_fma_f32 v233, v173, v240, v229\n\t"
    "v_mul_f32_e32 v236, v232, v232\n\t"
    "v_mul_f32_e32 v237, v233, v233\n\t"
    "v_fma_f32 v236, v236, s53, v241\n\t"
    "v_fma_f32 v237, v237, s53, v241\n\t"
    "v_mul_f32_e32 v236, v232, v236\n\t"
    "v_mul_f32_e32 v237, v233, v237\n\t"
    "v_exp_f32_e32 v236, v236\n\t"
    "v_exp_f32_e32 v237, v237\n\t"
    "v_add_f32_e32 v238, 1.0, v236\n\t"
    "v_add_f32_e32 v239, 1.0, v237\n\t"
    "v_rcp_f32_e32 v238, v238\n\t"
    "v_rcp_f32_e32 v239, v239\n\t"
    "v_mul_f32_e32 v238, v232, v238\n\t"
    "v_mul_f32_e32 v239, v233, v239\n\t"
    "v_cvt_pk_bf16_f32 v222, v238, v239\n\t"
    "v_fma_f32 v234, v174, v240, v230\n\t"
    "v_fma_f32 v235, v175, v240, v231\n\t"
    "v_mul_f32_e32 v236, v234, v234\n\t"
    "v_mul_f32_e32 v237, v235, v235\n\t"
    "v_fma_f32 v236, v236, s53, v241\n\t"
    "v_fma_f32 v237, v237, s53, v241\n\t"
    "v_mul_f32_e32 v236, v234, v236\n\t"
    "v_mul_f32_e32 v237, v235, v237\n\t"
    "v_exp_f32_e32 v236, v236\n\t"
    "v_exp_f32_e32 v237, v237\n\t"
    "v_add_f32_e32 v238, 1.0, v236\n\t"
    "v_add_f32_e32 v239, 1.0, v237\n\t"
    "v_rcp_f32_e32 v238, v238\n\t"
    "v_rcp_f32_e32 v239, v239\n\t"
    "v_mul_f32_e32 v238, v234, v238\n\t"
    "v_mul_f32_e32 v239, v235, v239\n\t"
    "v_cvt_pk_bf16_f32 v223, v238, v239\n\t"
    "global_store_dwordx4 v207, v[220:223], s[44:45] offset:1024 sc1\n\t"
    "s_waitcnt lgkmcnt(0)\n\t"
    "s_waitcnt vmcnt(2)\n\t"
      : "+{v[160:175]}"(ad), "+{v[200:207]}"(vs), "+{v[228:231]}"(on), "+{v[240:243]}"(ms),
        "+{s[40:41]}"(wb), "+{s[44:45]}"(hb), "+{s48}"(sex), "+{s50}"(smp), "+{s51}"(smo), "+{s52}"(sdl) K1_TSOUT
      : "{v[208:211]}"(sel), "{v[248:251]}"(wvk), "{s[42:43]}"(xb), "{s[36:37]}"(xc), "{s[38:39]}"(pfa), "{s53}"(c2), "{s55}"(wm * 4), "{s56}"(wn * 8)
      : K1_CLOB, "v212", "v213", "v214", "v215", "v216", "v217", "v218", "v219", "v220", "v221", "v222", "v223", "v224", "v225",
        "v226", "v227", "v232", "v233", "v234", "v235", "v236", "v237", "v238", "v239", "v244", "v245", "v246", "v247");
  } else {
    unsigned long long tm;
    {
      const int st = lane >> 3, q = (lane >> 1) & 3, h2 = lane & 1;
      const int col = (step0 + st) * 64 + wn * 32;
      const bool isK = col >= 576;
      const unsigned bm = isK ? (unsigned)(WS_KV + rb * KV_GRP + wm * KV_T) : (unsigned)(WS_QF + (rb * 4 + wm) * 1024);
      const unsigned bt = isK ? bm + KV_K2 : (unsigned)(WS_Q2 + (rb * 128 + wm * 32) * 4);
      const unsigned sm = isK ? (unsigned)KV_HEAD : 65536u, sq = isK ? (unsigned)KV_HEAD : (unsigned)(S * 4);
      const unsigned Gq = (unsigned)((col - (isK ? 576 : 0)) >> 2) + 2 * q + h2;
      const unsigned m = (Gq * 57u) >> 9, r = Gq - 9 * m;
      const unsigned odd = r > 4 ? 1u : 0u, gi = r - 5 * odd;
      const unsigned e = r != 4 ? bm + (2 * m + odd) * sm + (gi >> 1) * 512 + (gi & 1) * 8 : bt + 2 * m * sq;
      *(LAS unsigned*)((lcp)smem + K1_TAB + wid * 256 + st * 32 + h2 * 16 + q * 4) = e;
      tm = __builtin_amdgcn_ballot_w64(r == 4);
    }
    u32x8 qv = {(unsigned)l31 * 16, (unsigned)l31 * 4, lds0 + K1_TAB + wid * 256 + hh * 16,
                (unsigned)(WS_KV + rb * KV_GRP + wm * KV_T + 1024) + hh * 336, (unsigned)l31, 0u, 0u, 0u};
    f32x8 ga;
    wvk[0] = lds0 + K1_BIAS + hh * 16 + wn * 192;
    int sgs = step0, scol = step0 * 64 - 1152 + wn * 32;
    asm volatile("s_waitcnt vmcnt(%1)" : "+v"(b1v) : "n"(K1_NPRE) : "memory");
    *(LAS unsigned*)((lcp)smem + K1_BIAS + tid * 4) = b1v;
    asm volatile(
    "s_cmp_eq_u32 s56, 3\n\t"
    "s_cbranch_scc1 .Lk1v0%=\n\t"
    "s_waitcnt lgkmcnt(0)\n\t"
    "s_barrier\n\t"
    "ds_read_b128 v[0:3], v160\n\t"
    "ds_read_b64_tr_b16 v[176:177], v172 offset:0\n\t"
    "ds_read_b64_tr_b16 v[178:179], v172 offset:512\n\t"
    "ds_read_b128 v[4:7], v161\n\t"
    "ds_read_b64_tr_b16 v[180:181], v172 offset:2048\n\t"
    "ds_read_b64_tr_b16 v[182:183], v172 offset:2560\n\t"
    "ds_read_b128 v[8:11], v162\n\t"
    "ds_read_b64_tr_b16 v[184:185], v172 offset:4096\n\t"
    "ds_read_b64_tr_b16 v[186:187], v172 offset:4608\n\t"
    "s_waitcnt lgkmcnt(6)\n\t"
    "v_mfma_f32_32x32x16_bf16 v[144:159], v[176:179], v[0:3], 0\n\t"
    "ds_read_b64_tr_b16 v[188:189], v173 offset:0\n\t"
    "ds_read_b64_tr_b16 v[190:191], v173 offset:512\n\t"
    "ds_read_b128 v[12:15], v163\n\t"
    "ds_read_b128 v[16:19], v164\n\t"
    "ds_read_b128 v[20:23], v165\n\t"
    "s_add_u32 m0, s48, 106496\n\t"
    "s_add_u32 s46, s42, 256\n\t"
    "s_addc_u32 s47, s43, 0\n\t"
    "global_load_lds_dwordx4 v205, s[46:47]\n\t"
    "s_waitcnt lgkmcnt(8)\n\t"
    "v_mfma_f32_32x32x16_bf16 v[144:159], v[180:183], v[4:7], v[144:159]\n\t"
    "ds_read_b64_tr_b16 v[176:177], v173 offset:2048\n\t"
    "ds_read_b64_tr_b16 v[178:179], v173 offset:2560\n\t"
    "ds_read_b128 v[24:27], v166\n\t"
    "ds_read_b128 v[28:31], v167\n\t"
    "ds_read_b128 v[32:35], v168\n\t"
    "s_add_u32 m0, s48, 107520\n\t"
    "s_add_u32 s46, s42, 256\n\t"
    "s_addc_u32 s47, s43, 0\n\t"
    "global_load_lds_dwordx4 v206, s[46:47]\n\t"
    "s_waitcnt lgkmcnt(10)\n\t"
    "v_mfma_f32_32x32x16_bf16 v[144:159], v[184:187], v[8:11], v[144:159]\n\t"
    "ds_read_b64_tr_b16 v[180:181], v173 offset:4096\n\t"
    "ds_read_b64_tr_b16 v[182:183], v173 offset:4608\n\t"
    "ds_read_b128 v[36:39], v169\n\t"
    "ds_read_b128 v[40:43], v170\n\t"
    "ds_read_b128 v[44:47], v171\n\t"
    "s_add_u32 m0, s50, 49152\n\t"
    "s_add_u32 s46, s40, 0\n\t"
    "s_addc_u32 s47, s41, 0\n\t"
    "global_load_lds_dwordx4 v250, s[46:47]\n\t"
    "s_waitcnt lgkmcnt(12)\n\t"
    "v_mfma_f32_32x32x16_bf16 v[144:159], v[188:191], v[12:15], v[144:159]\n\t"
    "ds_read_b64_tr_b16 v[184:185], v174 offset:0\n\t"
    "ds_read_b64_tr_b16 v[186:187], v174 offset:512\n\t"
    "s_add_u32 m0, s50, 57344\n\t"
    "s_add_u32 s46, s40, 221184\n\t"
    "s_addc_u32 s47, s41, 0\n\t"
    "global_load_lds_dwordx4 v250, s[46:47]\n\t"
    "s_waitcnt lgkmcnt(10)\n\t"
    "v_mfma_f32_32x32x16_bf16 v[144:159], v[176:179], v[16:19], v[144:159]\n\t"
    "ds_read_b64_tr_b16 v[188:189], v174 offset:2048\n\t"
    "ds_read_b64_tr_b16 v[190:191], v174 offset:2560\n\t"
    "s_add_u32 m0, s50, 65536\n\t"
    "s_add_u32 s46, s40, 442368\n\t"
    "s_addc_u32 s47, s41, 0\n\t"
    "global_load_lds_dwordx4 v250, s[46:47]\n\t"
    "s_waitcnt lgkmcnt(7)\n\t"
    "v_mfma_f32_32x32x16_bf16 v[144:159], v[180:183], v[20:23], v[144:159]\n\t"
    "ds_read_b64_tr_b16 v[176:177], v174 offset:4096\n\t"
    "ds_read_b64_tr_b16 v[178:179], v174 offset:4608\n\t"
    "s_waitcnt lgkmcnt(6)\n\t"
    "s_barrier\n\t"
    "s_waitcnt lgkmcnt(4)\n\t"
    "v_mfma_f32_32x32x16_bf16 v[144:159], v[184:187], v[24:27], v[144:159]\n\t"
    "ds_read_b64_tr_b16 v[180:181], v175 offset:0\n\t"
    "ds_read_b64_tr_b16 v[182:183], v175 offset:512\n\t"
    "s_add_u32 m0, s48, 0\n\t"
    "s_add_u32 s46, s36, 0\n\t"
    "s_addc_u32 s47, s37, 0\n\t"
    "global_load_lds_dwordx4 v205, s[46:47]\n\t"
    "s_waitcnt lgkmcnt(4)\n\t"
    "v_mfma_f32_32x32x16_bf16 v[144:159], v[188:191], v[28:31], v[144:159]\n\t"
    "ds_read_b64_tr_b16 v[184:185], v175 offset:2048\n\t"
    "ds_read_b64_tr_b16 v[186:187], v175 offset:2560\n\t"
    "s_add_u32 m0, s48, 1024\n\t"
    "s_add_u32 s46, s36, 0\n\t"
    "s_addc_u32 s47, s37, 0\n\t"
    "global_load_lds_dwordx4 v206, s[46:47]\n\t"
    "s_waitcnt lgkmcnt(4)\n\t"
    "v_mfma_f32_32x32x16_bf16 v[144:159], v[176:179], v[32:35], v[144:159]\n\t"
    "ds_read_b64_tr_b16 v[188:189], v175 offset:4096\n\t"
    "ds_read_b64_tr_b16 v[190:191], v175 offset:4608\n\t"
    "s_add_u32 m0, s48, 16384\n\t"
    "s_add_u32 s46, s36, 128\n\t"
    "s_addc_u32 s47, s37, 0\n\t"
    "global_load_lds_dwordx4 v205, s[46:47]\n\t"
    "v_subrev_u32_e32 v160, s52, v160\n\t"
    "v_subrev_u32_e32 v161, s52, v161\n\t"
    "v_subrev_u32_e32 v162, s52, v162\n\t"
    "s_waitcnt lgkmcnt(4)\n\t"
    "v_mfma_f32_32x32x16_bf16 v[144:159], v[180:183], v[36:39], v[144:159]\n\t"
    "s_add_u32 m0, s48, 17408\n\t"
    "s_add_u32 s46, s36, 128\n\t"
    "s_addc_u32 s47, s37, 0\n\t"
    "global_load_lds_dwordx4 v206, s[46:47]\n\t"
    "v_subrev_u32_e32 v163, s52, v163\n\t"
    "v_subrev_u32_e32 v164, s52, v164\n\t"
    "v_subrev_u32_e32 v165, s52, v165\n\t"
    "s_waitcnt lgkmcnt(2)\n\t"
    "v_mfma_f32_32x32x16_bf16 v[144:159], v[184:187], v[40:43], v[144:159]\n\t"
    "v_subrev_u32_e32 v166, s52, v166\n\t"
    "v_subrev_u32_e32 v167, s52, v167\n\t"
    "v_subrev_u32_e32 v168, s52, v168\n\t"
    "s_waitcnt lgkmcnt(0)\n\t"
    "v_mfma_f32_32x32x16_bf16 v[144:159], v[188:191], v[44:47], v[144:159]\n\t"
    "v_subrev_u32_e32 v169, s52, v169\n\t"
    "v_subrev_u32_e32 v170, s52, v170\n\t"
    "v_subrev_u32_e32 v171, s52, v171\n\t"
    "s_waitcnt vmcnt(4)\n\t"
    "s_barrier\n\t"
    "ds_read_b128 v[48:51], v160\n\t"
    "ds_read_b64_tr_b16 v[176:177], v200 offset:49152\n\t"
    "ds_read_b64_tr_b16 v[178:179], v200 offset:49664\n\t"
    "ds_read_b128 v[52:55], v161\n\t"
    "ds_read_b64_tr_b16 v[180:181], v200 offset:51200\n\t"
    "ds_read_b64_tr_b16 v[182:183], v200 offset:51712\n\t"
    "ds_read_b128 v[56:59], v162\n\t"
    "ds_read_b64_tr_b16 v[184:185], v200 offset:53248\n\t"
    "ds_read_b64_tr_b16 v[186:187], v200 offset:53760\n\t"
    "s_waitcnt lgkmcnt(6)\n\t"
    "v_mfma_f32_32x32x16_bf16 v[144:159], v[176:179], v[48:51], v[144:159]\n\t"
    "ds_read_b64_tr_b16 v[188:189], v201 offset:49152\n\t"
    "ds_read_b64_tr_b16 v[190:191], v201 offset:49664\n\t"
    "ds_read_b128 v[60:63], v163\n\t"
    "ds_read_b128 v[64:67], v164\n\t"
    "ds_read_b128 v[68:71], v165\n\t"
    "s_add_u32 m0, s48, 32768\n\t"
    "s_add_u32 s46, s36, 256\n\t"
    "s_addc_u32 s47, s37, 0\n\t"
    "global_load_lds_dwordx4 v205, s[46:47]\n\t"
    "s_waitcnt lgkmcnt(8)\n\t"
    "v_mfma_f32_32x32x16_bf16 v[144:159], v[180:183], v[52:55], v[144:159]\n\t"
    "ds_read_b64_tr_b16 v[176:177], v201 offset:51200\n\t"
    "ds_read_b64_tr_b16 v[178:179], v201 offset:51712\n\t"
    "ds_read_b128 v[72:75], v166\n\t"
    "ds_read_b128 v[76:79], v167\n\t"
    "ds_read_b128 v[80:83], v168\n\t"
    "s_add_u32 m0, s48, 33792\n\t"
    "s_add_u32 s46, s36, 256\n\t"
    "s_addc_u32 s47, s37, 0\n\t"
    "global_load_lds_dwordx4 v206, s[46:47]\n\t"
    "s_waitcnt lgkmcnt(10)\n\t"
    "v_mfma_f32_32x32x16_bf16 v[144:159], v[184:187], v[56:59], v[144:159]\n\t"
    "ds_read_b64_tr_b16 v[180:181], v201 offset:53248\n\t"
    "ds_read_b64_tr_b16 v[182:183], v201 offset:53760\n\t"
    "ds_read_b128 v[84:87], v169\n\t"
    "ds_read_b128 v[88:91], v170\n\t"
    "ds_read_b128 v[92:95], v171\n\t"
    "s_add_u32 m0, s51, 49152\n\t"
    "s_add_u32 s46, s40, 0\n\t"
    "s_addc_u32 s47, s41, 0\n\t"
    "global_load_lds_dwordx4 v251, s[46:47]\n\t"
    "s_waitcnt lgkmcnt(12)\n\t"
    "v_mfma_f32_32x32x16_bf16 v[144:159], v[188:191], v[60:63], v[144:159]\n\t"
    "ds_read_b64_tr_b16 v[184:185], v202 offset:49152\n\t"
    "ds_read_b64_tr_b16 v[186:187], v202 offset:49664\n\t"
    "s_add_u32 m0, s51, 57344\n\t"
    "s_add_u32 s46, s40, 221184\n\t"
    "s_addc_u32 s47, s41, 0\n\t"
    "global_load_lds_dwordx4 v251, s[46:47]\n\t"
    "s_waitcnt lgkmcnt(10)\n\t"
    "v_mfma_f32_32x32x16_bf16 v[144:159], v[176:179], v[64:67], v[144:159]\n\t"
    "ds_read_b64_tr_b16 v[188:189], v202 offset:51200\n\t"
    "ds_read_b64_tr_b16 v[190:191], v202 offset:51712\n\t"
    "s_add_u32 m0, s51, 65536\n\t"
    "s_add_u32 s46, s40, 442368\n\t"
    "s_addc_u32 s47, s41, 0\n\t"
    "global_load_lds_dwordx4 v251, s[46:47]\n\t"
    "s_waitcnt lgkmcnt(7)\n\t"
    "v_mfma_f32_32x32x16_bf16 v[144:159], v[180:183], v[68:71], v[144:159]\n\t"
    "ds_read_b64_tr_b16 v[176:177], v202 offset:53248\n\t"
    "ds_read_b64_tr_b16 v[178:179], v202 offset:53760\n\t"
    "s_waitcnt lgkmcnt(6)\n\t"
    "s_barrier\n\t"
    "s_waitcnt lgkmcnt(4)\n\t"
    "v_mfma_f32_32x32x16_bf16 v[144:159], v[184:187], v[72:75], v[144:159]\n\t"
    "ds_read_b64_tr_b16 v[180:181], v203 offset:49152\n\t"
    "ds_read_b64_tr_b16 v[182:183], v203 offset:49664\n\t"
    "s_add_u32 m0, s50, 0\n\t"
    "s_add_u32 s46, s40, 128\n\t"
    "s_addc_u32 s47, s41, 0\n\t"
    "global_load_lds_dwordx4 v249, s[46:47]\n\t"
    "s_waitcnt lgkmcnt(4)\n\t"
    "v_mfma_f32_32x32x16_bf16 v[144:159], v[188:191], v[76:79], v[144:159]\n\t"
    "ds_read_b64_tr_b16 v[184:185], v203 offset:51200\n\t"
    "ds_read_b64_tr_b16 v[186:187], v203 offset:51712\n\t"
    "s_add_u32 m0, s50, 8192\n\t"
    "s_add_u32 s46, s40, 221312\n\t"
    "s_addc_u32 s47, s41, 0\n\t"
    "global_load_lds_dwordx4 v249, s[46:47]\n\t"
    "s_waitcnt lgkmcnt(4)\n\t"
    "v_mfma_f32_32x32x16_bf16 v[144:159], v[176:179], v[80:83], v[144:159]\n\t"
    "ds_read_b64_tr_b16 v[188:189], v203 offset:53248\n\t"
    "ds_read_b64_tr_b16 v[190:191], v203 offset:53760\n\t"
    "s_add_u32 m0, s50, 16384\n\t"
    "s_add_u32 s46, s40, 442496\n\t"
    "s_addc_u32 s47, s41, 0\n\t"
    "global_load_lds_dwordx4 v249, s[46:47]\n\t"
    "v_add_u32_e32 v160, s52, v160\n\t"
    "v_add_u32_e32 v161, s52, v161\n\t"
    "v_add_u32_e32 v162, s52, v162\n\t"
    "s_waitcnt lgkmcnt(4)\n\t"
    "v_mfma_f32_32x32x16_bf16 v[144:159], v[180:183], v[84:87], v[144:159]\n\t"
    "s_add_u32 m0, s50, 24576\n\t"
    "s_add_u32 s46, s40, 128\n\t"
    "s_addc_u32 s47, s41, 0\n\t"
    "global_load_lds_dwordx4 v250, s[46:47]\n\t"
    "v_add_u32_e32 v163, s52, v163\n\t"
    "v_add_u32_e32 v164, s52, v164\n\t"
    "v_add_u32_e32 v165, s52, v165\n\t"
    "s_waitcnt lgkmcnt(2)\n\t"
    "v_mfma_f32_32x32x16_bf16 v[144:159], v[184:187], v[88:91], v[144:159]\n\t"
    "v_add_u32_e32 v166, s52, v166\n\t"
    "v_add_u32_e32 v167, s52, v167\n\t"
    "v_add_u32_e32 v168, s52, v168\n\t"
    "s_waitcnt lgkmcnt(0)\n\t"
    "v_mfma_f32_32x32x16_bf16 v[144:159], v[188:191], v[92:95], v[144:159]\n\t"
    "v_add_u32_e32 v169, s52, v169\n\t"
    "v_add_u32_e32 v170, s52, v170\n\t"
    "v_add_u32_e32 v171, s52, v171\n\t"
    "s_waitcnt vmcnt(4)\n\t"
    "s_barrier\n\t"
    "ds_read_b128 v[96:99], v160\n\t"
    "ds_read_b64_tr_b16 v[176:177], v172 offset:0\n\t"
    "ds_read_b64_tr_b16 v[178:179], v172 offset:512\n\t"
    "ds_read_b128 v[100:103], v161\n\t"
    "ds_read_b64_tr_b16 v[180:181], v172 offset:2048\n\t"
    "ds_read_b64_tr_b16 v[182:183], v172 offset:2560\n\t"
    "ds_read_b128 v[104:107], v162\n\t"
    "ds_read_b64_tr_b16 v[184:185], v172 offset:4096\n\t"
    "ds_read_b64_tr_b16 v[186:187], v172 offset:4608\n\t"
    "s_waitcnt lgkmcnt(6)\n\t"
    "v_mfma_f32_32x32x16_bf16 v[144:159], v[176:179], v[96:99], v[144:159]\n\t"
    "ds_read_b64_tr_b16 v[188:189], v173 offset:0\n\t"
    "ds_read_b64_tr_b16 v[190:191], v173 offset:512\n\t"
    "ds_read_b128 v[108:111], v163\n\t"
    "ds_read_b128 v[112:115], v164\n\t"
    "ds_read_b128 v[116:119], v165\n\t"
    "s_add_u32 m0, s50, 32768\n\t"
    "s_add_u32 s46, s40, 221312\n\t"
    "s_addc_u32 s47, s41, 0\n\t"
    "global_load_lds_dwordx4 v250, s[46:47]\n\t"
    "s_waitcnt lgkmcnt(8)\n\t"
    "v_mfma_f32_32x32x16_bf16 v[144:159], v[180:183], v[100:103], v[144:159]\n\t"
    "ds_read_b64_tr_b16 v[176:177], v173 offset:2048\n\t"
    "ds_read_b64_tr_b16 v[178:179], v173 offset:2560\n\t"
    "ds_read_b128 v[120:123], v166\n\t"
    "ds_read_b128 v[124:127], v167\n\t"
    "ds_read_b128 v[128:131], v168\n\t"
    "s_add_u32 m0, s50, 40960\n\t"
    "s_add_u32 s46, s40, 442496\n\t"
    "s_addc_u32 s47, s41, 0\n\t"
    "global_load_lds_dwordx4 v250, s[46:47]\n\t"
    "s_waitcnt lgkmcnt(10)\n\t"
    "v_mfma_f32_32x32x16_bf16 v[144:159], v[184:187], v[104:107], v[144:159]\n\t"
    "ds_read_b64_tr_b16 v[180:181], v173 offset:4096\n\t"
    "ds_read_b64_tr_b16 v[182:183], v173 offset:4608\n\t"
    "ds_read_b128 v[132:135], v169\n\t"
    "ds_read_b128 v[136:139], v170\n\t"
    "ds_read_b128 v[140:143], v171\n\t"
    "s_add_u32 m0, s50, 49152\n\t"
    "s_add_u32 s46, s40, 128\n\t"
    "s_addc_u32 s47, s41, 0\n\t"
    "global_load_lds_dwordx4 v251, s[46:47]\n\t"
    "s_waitcnt lgkmcnt(12)\n\t"
    "v_mfma_f32_32x32x16_bf16 v[144:159], v[188:191], v[108:111], v[144:159]\n\t"
    "ds_read_b64_tr_b16 v[184:185], v174 offset:0\n\t"
    "ds_read_b64_tr_b16 v[186:187], v174 offset:512\n\t"
    "s_add_u32 m0, s50, 57344\n\t"
    "s_add_u32 s46, s40, 221312\n\t"
    "s_addc_u32 s47, s41, 0\n\t"
    "global_load_lds_dwordx4 v251, s[46:47]\n\t"
    "s_waitcnt lgkmcnt(10)\n\t"
    "v_mfma_f32_32x32x16_bf16 v[144:159], v[176:179], v[112:115], v[144:159]\n\t"
    "ds_read_b64_tr_b16 v[188:189], v174 offset:2048\n\t"
    "ds_read_b64_tr_b16 v[190:191], v174 offset:2560\n\t"
    "s_add_u32 m0, s50, 65536\n\t"
    "s_add_u32 s46, s40, 442496\n\t"
    "s_addc_u32 s47, s41, 0\n\t"
    "global_load_lds_dwordx4 v251, s[46:47]\n\t"
    "s_waitcnt lgkmcnt(7)\n\t"
    "v_mfma_f32_32x32x16_bf16 v[144:159], v[180:183], v[116:119], v[144:159]\n\t"
    "ds_read_b64_tr_b16 v[176:177], v174 offset:4096\n\t"
    "ds_read_b64_tr_b16 v[178:179], v174 offset:4608\n\t"
    "s_waitcnt lgkmcnt(4)\n\t"
    "v_mfma_f32_32x32x16_bf16 v[144:159], v[184:187], v[120:123], v[144:159]\n\t"
    "ds_read_b64_tr_b16 v[180:181], v175 offset:0\n\t"
    "ds_read_b64_tr_b16 v[182:183], v175 offset:512\n\t"
    "s_waitcnt lgkmcnt(4)\n\t"
    "v_mfma_f32_32x32x16_bf16 v[144:159], v[188:191], v[124:127], v[144:159]\n\t"
    "s_waitcnt vmcnt(6)\n\t"
    "s_barrier\n\t"
    "ds_read_b64_tr_b16 v[184:185], v175 offset:2048\n\t"
    "ds_read_b64_tr_b16 v[186:187], v175 offset:2560\n\t"
    "s_waitcnt lgkmcnt(4)\n\t"
    "v_mfma_f32_32x32x16_bf16 v[144:159], v[176:179], v[128:131], v[144:159]\n\t"
    "ds_read_b64_tr_b16 v[188:189], v175 offset:4096\n\t"
    "ds_read_b64_tr_b16 v[190:191], v175 offset:4608\n\t"
    "ds_read_b128 v[208:211], v226\n\t"
    "s_waitcnt lgkmcnt(5)\n\t"
    "v_mfma_f32_32x32x16_bf16 v[144:159], v[180:183], v[132:135], v[144:159]\n\t"
    "ds_read_b64_tr_b16 v[176:177], v200 offset:0\n\t"
    "ds_read_b64_tr_b16 v[178:179], v200 offset:512\n\t"
    "s_waitcnt lgkmcnt(5)\n\t"
    "v_mfma_f32_32x32x16_bf16 v[144:159], v[184:187], v[136:139], v[144:159]\n\t"
    "ds_read_b64_tr_b16 v[180:181], v200 offset:2048\n\t"
    "ds_read_b64_tr_b16 v[182:183], v200 offset:2560\n\t"
    "s_waitcnt lgkmcnt(5)\n\t"
    "v_mfma_f32_32x32x16_bf16 v[160:175], v[188:191], v[140:143], v[144:159]\n\t"
    "ds_read_b64_tr_b16 v[184:185], v200 offset:4096\n\t"
    "ds_read_b64_tr_b16 v[186:187], v200 offset:4608\n\t"
    "s_add_u32 s40, s40, 128\n\t"
    "s_addc_u32 s41, s41, 0\n\t"
    "s_waitcnt lgkmcnt(4)\n\t"
    "v_mfma_f32_32x32x16_bf16 v[144:159], v[176:179], v[0:3], 0\n\t"
    "ds_read_b64_tr_b16 v[188:189], v201 offset:0\n\t"
    "ds_read_b64_tr_b16 v[190:191], v201 offset:512\n\t"
    "s_cmp_ge_u32 s62, 9\n\t"
    "s_cselect_b32 s60, 1.0, 0x3eae1a93\n\t"
    "s_movk_i32 s61, 0x2000\n\t"
    "s_cmov_b32 s61, 159744\n\t"
    "s_waitcnt lgkmcnt(4)\n\t"
    "v_mfma_f32_32x32x16_bf16 v[144:159], v[180:183], v[4:7], v[144:159]\n\t"
    "ds_read_b64_tr_b16 v[176:177], v201 offset:2048\n\t"
    "ds_read_b64_tr_b16 v[178:179], v201 offset:2560\n\t"
    "s_add_u32 m0, s51, 0\n\t"
    "s_add_u32 s46, s40, 128\n\t"
    "s_addc_u32 s47, s41, 0\n\t"
    "global_load_lds_dwordx4 v249, s[46:47]\n\t"
    "s_waitcnt lgkmcnt(4)\n\t"
    "v_mfma_f32_32x32x16_bf16 v[144:159], v[184:187], v[8:11], v[144:159]\n\t"
    "ds_read_b64_tr_b16 v[180:181], v201 offset:4096\n\t"
    "ds_read_b64_tr_b16 v[182:183], v201 offset:4608\n\t"
    "v_mul_f32_e32 v212, s60, v160\n\t"
    "v_mul_f32_e32 v213, s60, v161\n\t"
    "v_mul_f32_e32 v214, s60, v162\n\t"
    "v_mul_f32_e32 v215, s60, v163\n\t"
    "s_waitcnt lgkmcnt(4)\n\t"
    "v_mfma_f32_32x32x16_bf16 v[144:159], v[188:191], v[12:15], v[144:159]\n\t"
    "ds_read_b64_tr_b16 v[184:185], v202 offset:0\n\t"
    "ds_read_b64_tr_b16 v[186:187], v202 offset:512\n\t"
    "s_add_u32 m0, s51, 8192\n\t"
    "s_add_u32 s46, s40, 221312\n\t"
    "s_addc_u32 s47, s41, 0\n\t"
    "global_load_lds_dwordx4 v249, s[46:47]\n\t"
    "v_cvt_pk_bf16_f32 v216, v212, v213\n\t"
    "v_cvt_pk_bf16_f32 v217, v214, v215\n\t"
    "v_add_u32_e32 v218, v208, v224\n\t"
    "s_waitcnt lgkmcnt(4)\n\t"
    "v_mfma_f32_32x32x16_bf16 v[144:159], v[176:179], v[16:19], v[144:159]\n\t"
    "ds_read_b64_tr_b16 v[188:189], v202 offset:2048\n\t"
    "ds_read_b64_tr_b16 v[190:191], v202 offset:2560\n\t"
    "s_and_b32 s46, s58, 3\n\t"
    "s_cbranch_scc1 .Lk1t0%=\n\t"
    "global_store_dwordx2 v218, v[216:217], s[44:45]\n\t"
    ".Lk1b0%=:\n\t"
    "s_waitcnt lgkmcnt(4)\n\t"
    "v_mfma_f32_32x32x16_bf16 v[144:159], v[180:183], v[20:23], v[144:159]\n\t"
    "ds_read_b64_tr_b16 v[176:177], v202 offset:4096\n\t"
    "ds_read_b64_tr_b16 v[178:179], v202 offset:4608\n\t"
    "s_add_u32 m0, s51, 16384\n\t"
    "s_add_u32 s46, s40, 442496\n\t"
    "s_addc_u32 s47, s41, 0\n\t"
    "global_load_lds_dwordx4 v249, s[46:47]\n\t"
    "s_waitcnt lgkmcnt(4)\n\t"
    "v_mfma_f32_32x32x16_bf16 v[144:159], v[184:187], v[24:27], v[144:159]\n\t"
    "ds_read_b64_tr_b16 v[180:181], v203 offset:0\n\t"
    "ds_read_b64_tr_b16 v[182:183], v203 offset:512\n\t"
    "s_waitcnt vmcnt(7)\n\t"
    "s_barrier\n\t"
    "s_waitcnt lgkmcnt(4)\n\t"
    "v_mfma_f32_32x32x16_bf16 v[144:159], v[188:191], v[28:31], v[144:159]\n\t"
    "ds_read_b64_tr_b16 v[184:185], v203 offset:2048\n\t"
    "ds_read_b64_tr_b16 v[186:187], v203 offset:2560\n\t"
    "s_add_u32 m0, s51, 24576\n\t"
    "s_add_u32 s46, s40, 128\n\t"
    "s_addc_u32 s47, s41, 0\n\t"
    "global_load_lds_dwordx4 v250, s[46:47]\n\t"
    "s_waitcnt lgkmcnt(4)\n\t"
    "v_mfma_f32_32x32x16_bf16 v[144:159], v[176:179], v[32:35], v[144:159]\n\t"
    "ds_read_b64_tr_b16 v[188:189], v203 offset:4096\n\t"
    "ds_read_b64_tr_b16 v[190:191], v203 offset:4608\n\t"
    "s_waitcnt lgkmcnt(4)\n\t"
    "v_mfma_f32_32x32x16_bf16 v[144:159], v[180:183], v[36:39], v[144:159]\n\t"
    "ds_read_b64_tr_b16 v[176:177], v200 offset:24576\n\t"
    "ds_read_b64_tr_b16 v[178:179], v200 offset:25088\n\t"
    "s_add_u32 m0, s51, 32768\n\t"
    "s_add_u32 s46, s40, 221312\n\t"
    "s_addc_u32 s47, s41, 0\n\t"
    "global_load_lds_dwordx4 v250, s[46:47]\n\t"
    "s_waitcnt lgkmcnt(4)\n\t"
    "v_mfma_f32_32x32x16_bf16 v[144:159], v[184:187], v[40:43], v[144:159]\n\t"
    "ds_read_b64_tr_b16 v[180:181], v200 offset:26624\n\t"
    "ds_read_b64_tr_b16 v[182:183], v200 offset:27136\n\t"
    "v_mul_f32_e32 v212, s60, v164\n\t"
    "v_mul_f32_e32 v213, s60, v165\n\t"
    "v_mul_f32_e32 v214, s60, v166\n\t"
    "v_mul_f32_e32 v215, s60, v167\n\t"
    "s_waitcnt lgkmcnt(4)\n\t"
    "v_mfma_f32_32x32x16_bf16 v[144:159], v[188:191], v[44:47], v[144:159]\n\t"
    "ds_read_b64_tr_b16 v[184:185], v200 offset:28672\n\t"
    "ds_read_b64_tr_b16 v[186:187], v200 offset:29184\n\t"
    "s_add_u32 m0, s51, 40960\n\t"
    "s_add_u32 s46, s40, 442496\n\t"
    "s_addc_u32 s47, s41, 0\n\t"
    "global_load_lds_dwordx4 v250, s[46:47]\n\t"
    "v_cvt_pk_bf16_f32 v216, v212, v213\n\t"
    "v_cvt_pk_bf16_f32 v217, v214, v215\n\t"
    "v_add_u32_e32 v218, v209, v224\n\t"
    "s_waitcnt lgkmcnt(4)\n\t"
    "v_mfma_f32_32x32x16_bf16 v[144:159], v[176:179], v[48:51], v[144:159]\n\t"
    "ds_read_b64_tr_b16 v[188:189], v201 offset:24576\n\t"
    "ds_read_b64_tr_b16 v[190:191], v201 offset:25088\n\t"
    "s_and_b32 s46, s58, 12\n\t"
    "s_cbranch_scc1 .Lk1t1%=\n\t"
    "global_store_dwordx2 v218, v[216:217], s[44:45]\n\t"
    ".Lk1b1%=:\n\t"
    "s_waitcnt lgkmcnt(4)\n\t"
    "v_mfma_f32_32x32x16_bf16 v[144:159], v[180:183], v[52:55], v[144:159]\n\t"
    "ds_read_b64_tr_b16 v[176:177], v201 offset:26624\n\t"
    "ds_read_b64_tr_b16 v[178:179], v201 offset:27136\n\t"
    "s_add_u32 m0, s51, 49152\n\t"
    "s_add_u32 s46, s40, 128\n\t"
    "s_addc_u32 s47, s41, 0\n\t"
    "global_load_lds_dwordx4 v251, s[46:47]\n\t"
    "s_waitcnt lgkmcnt(4)\n\t"
    "v_mfma_f32_32x32x16_bf16 v[144:159], v[184:187], v[56:59], v[144:159]\n\t"
    "ds_read_b64_tr_b16 v[180:181], v201 offset:28672\n\t"
    "ds_read_b64_tr_b16 v[182:183], v201 offset:29184\n\t"
    "s_waitcnt lgkmcnt(4)\n\t"
    "v_mfma_f32_32x32x16_bf16 v[144:159], v[188:191], v[60:63], v[144:159]\n\t"
    "ds_read_b64_tr_b16 v[184:185], v202 offset:24576\n\t"
    "ds_read_b64_tr_b16 v[186:187], v202 offset:25088\n\t"
    "s_add_u32 m0, s51, 57344\n\t"
    "s_add_u32 s46, s40, 221312\n\t"
    "s_addc_u32 s47, s41, 0\n\t"
    "global_load_lds_dwordx4 v251, s[46:47]\n\t"
    "s_waitcnt lgkmcnt(4)\n\t"
    "v_mfma_f32_32x32x16_bf16 v[144:159], v[176:179], v[64:67], v[144:159]\n\t"
    "ds_read_b64_tr_b16 v[188:189], v202 offset:26624\n\t"
    "ds_read_b64_tr_b16 v[190:191], v202 offset:27136\n\t"
    "s_waitcnt lgkmcnt(4)\n\t"
    "v_mfma_f32_32x32x16_bf16 v[144:159], v[180:183], v[68:71], v[144:159]\n\t"
    "ds_read_b64_tr_b16 v[176:177], v202 offset:28672\n\t"
    "ds_read_b64_tr_b16 v[178:179], v202 offset:29184\n\t"
    "s_add_u32 m0, s51, 65536\n\t"
    "s_add_u32 s46, s40, 442496\n\t"
    "s_addc_u32 s47, s41, 0\n\t"
    "global_load_lds_dwordx4 v251, s[46:47]\n\t"
    "s_waitcnt lgkmcnt(4)\n\t"
    "v_mfma_f32_32x32x16_bf16 v[144:159], v[184:187], v[72:75], v[144:159]\n\t"
    "ds_read_b64_tr_b16 v[180:181], v203 offset:24576\n\t"
    "ds_read_b64_tr_b16 v[182:183], v203 offset:25088\n\t"
    "s_waitcnt vmcnt(11)\n\t"
    "s_barrier\n\t"
    "v_mul_f32_e32 v212, s60, v168\n\t"
    "v_mul_f32_e32 v213, s60, v169\n\t"
    "v_mul_f32_e32 v214, s60, v170\n\t"
    "v_mul_f32_e32 v215, s60, v171\n\t"
    "s_waitcnt lgkmcnt(4)\n\t"
    "v_mfma_f32_32x32x16_bf16 v[144:159], v[188:191], v[76:79], v[144:159]\n\t"
    "ds_read_b64_tr_b16 v[184:185], v203 offset:26624\n\t"
    "ds_read_b64_tr_b16 v[186:187], v203 offset:27136\n\t"
    "s_add_u32 m0, s50, 0\n\t"
    "s_add_u32 s46, s40, 256\n\t"
    "s_addc_u32 s47, s41, 0\n\t"
    "global_load_lds_dwordx4 v249, s[46:47]\n\t"
    "v_cvt_pk_bf16_f32 v216, v212, v213\n\t"
    "v_cvt_pk_bf16_f32 v217, v214, v215\n\t"
    "v_add_u32_e32 v218, v210, v224\n\t"
    "s_waitcnt lgkmcnt(4)\n\t"
    "v_mfma_f32_32x32x16_bf16 v[144:159], v[176:179], v[80:83], v[144:159]\n\t"
    "ds_read_b64_tr_b16 v[188:189], v203 offset:28672\n\t"
    "ds_read_b64_tr_b16 v[190:191], v203 offset:29184\n\t"
    "s_and_b32 s46, s58, 48\n\t"
    "s_cbranch_scc1 .Lk1t2%=\n\t"
    "global_store_dwordx2 v218, v[216:217], s[44:45]\n\t"
    ".Lk1b2%=:\n\t"
    "s_waitcnt lgkmcnt(4)\n\t"
    "v_mfma_f32_32x32x16_bf16 v[144:159], v[180:183], v[84:87], v[144:159]\n\t"
    "ds_read_b64_tr_b16 v[176:177], v200 offset:49152\n\t"
    "ds_read_b64_tr_b16 v[178:179], v200 offset:49664\n\t"
    "s_add_u32 m0, s50, 8192\n\t"
    "s_add_u32 s46, s40, 221440\n\t"
    "s_addc_u32 s47, s41, 0\n\t"
    "global_load_lds_dwordx4 v249, s[46:47]\n\t"
    "s_waitcnt lgkmcnt(4)\n\t"
    "v_mfma_f32_32x32x16_bf16 v[144:159], v[184:187], v[88:91], v[144:159]\n\t"
    "ds_read_b64_tr_b16 v[180:181], v200 offset:51200\n\t"
    "ds_read_b64_tr_b16 v[182:183], v200 offset:51712\n\t"
    "s_waitcnt lgkmcnt(4)\n\t"
    "v_mfma_f32_32x32x16_bf16 v[144:159], v[188:191], v[92:95], v[144:159]\n\t"
    "ds_read_b64_tr_b16 v[184:185], v200 offset:53248\n\t"
    "ds_read_b64_tr_b16 v[186:187], v200 offset:53760\n\t"
    "s_add_u32 m0, s50, 16384\n\t"
    "s_add_u32 s46, s40, 442624\n\t"
    "s_addc_u32 s47, s41, 0\n\t"
    "global_load_lds_dwordx4 v249, s[46:47]\n\t"
    "s_waitcnt lgkmcnt(4)\n\t"
    "v_mfma_f32_32x32x16_bf16 v[144:159], v[176:179], v[96:99], v[144:159]\n\t"
    "ds_read_b64_tr_b16 v[188:189], v201 offset:49152\n\t"
    "ds_read_b64_tr_b16 v[190:191], v201 offset:49664\n\t"
    "v_add_u32_e32 v200, s52, v200\n\t"
    "s_waitcnt lgkmcnt(4)\n\t"
    "v_mfma_f32_32x32x16_bf16 v[144:159], v[180:183], v[100:103], v[144:159]\n\t"
    "ds_read_b64_tr_b16 v[176:177], v201 offset:51200\n\t"
    "ds_read_b64_tr_b16 v[178:179], v201 offset:51712\n\t"
    "s_waitcnt lgkmcnt(4)\n\t"
    "v_mfma_f32_32x32x16_bf16 v[144:159], v[184:187], v[104:107], v[144:159]\n\t"
    "ds_read_b64_tr_b16 v[180:181], v201 offset:53248\n\t"
    "ds_read_b64_tr_b16 v[182:183], v201 offset:53760\n\t"
    "v_mul_f32_e32 v212, s60, v172\n\t"
    "v_mul_f32_e32 v213, s60, v173\n\t"
    "v_mul_f32_e32 v214, s60, v174\n\t"
    "v_mul_f32_e32 v215, s60, v175\n\t"
    "s_waitcnt lgkmcnt(4)\n\t"
    "v_mfma_f32_32x32x16_bf16 v[144:159], v[188:191], v[108:111], v[144:159]\n\t"
    "ds_read_b64_tr_b16 v[184:185], v202 offset:49152\n\t"
    "ds_read_b64_tr_b16 v[186:187], v202 offset:49664\n\t"
    "v_add_u32_e32 v201, s52, v201\n\t"
    "v_cvt_pk_bf16_f32 v216, v212, v213\n\t"
    "v_cvt_pk_bf16_f32 v217, v214, v215\n\t"
    "v_add_u32_e32 v218, v211, v224\n\t"
    "s_waitcnt lgkmcnt(4)\n\t"
    "v_mfma_f32_32x32x16_bf16 v[144:159], v[176:179], v[112:115], v[144:159]\n\t"
    "ds_read_b64_tr_b16 v[188:189], v202 offset:51200\n\t"
    "ds_read_b64_tr_b16 v[190:191], v202 offset:51712\n\t"
    "s_and_b32 s46, s58, 192\n\t"
    "s_cbranch_scc1 .Lk1t3%=\n\t"
    "global_store_dwordx2 v218, v[216:217], s[44:45]\n\t"
    ".Lk1b3%=:\n\t"
    "s_waitcnt lgkmcnt(4)\n\t"
    "v_mfma_f32_32x32x16_bf16 v[144:159], v[180:183], v[116:119], v[144:159]\n\t"
    "ds_read_b64_tr_b16 v[176:177], v202 offset:53248\n\t"
    "ds_read_b64_tr_b16 v[178:179], v202 offset:53760\n\t"
    "s_waitcnt lgkmcnt(4)\n\t"
    "v_mfma_f32_32x32x16_bf16 v[144:159], v[184:187], v[120:123], v[144:159]\n\t"
    "ds_read_b64_tr_b16 v[180:181], v203 offset:49152\n\t"
    "ds_read_b64_tr_b16 v[182:183], v203 offset:49664\n\t"
    "s_waitcnt vmcnt(12)\n\t"
    "s_barrier\n\t"
    "v_add_u32_e32 v202, s52, v202\n\t"
    "v_add_u32_e32 v226, 32, v226\n\t"
    "s_waitcnt lgkmcnt(4)\n\t"
    "v_mfma_f32_32x32x16_bf16 v[144:159], v[188:191], v[124:127], v[144:159]\n\t"
    "ds_read_b64_tr_b16 v[184:185], v203 offset:51200\n\t"
    "ds_read_b64_tr_b16 v[186:187], v203 offset:51712\n\t"
    "s_add_u32 m0, s50, 24576\n\t"
    "s_add_u32 s46, s40, 256\n\t"
    "s_addc_u32 s47, s41, 0\n\t"
    "global_load_lds_dwordx4 v250, s[46:47]\n\t"
    "s_waitcnt lgkmcnt(4)\n\t"
    "v_mfma_f32_32x32x16_bf16 v[144:159], v[176:179], v[128:131], v[144:159]\n\t"
    "ds_read_b64_tr_b16 v[188:189], v203 offset:53248\n\t"
    "ds_read_b64_tr_b16 v[190:191], v203 offset:53760\n\t"
    "ds_read_b128 v[208:211], v226\n\t"
    "s_waitcnt lgkmcnt(5)\n\t"
    "v_mfma_f32_32x32x16_bf16 v[144:159], v[180:183], v[132:135], v[144:159]\n\t"
    "ds_read_b64_tr_b16 v[176:177], v200 offset:0\n\t"
    "ds_read_b64_tr_b16 v[178:179], v200 offset:512\n\t"
    "s_add_u32 m0, s50, 32768\n\t"
    "s_add_u32 s46, s40, 221440\n\t"
    "s_addc_u32 s47, s41, 0\n\t"
    "global_load_lds_dwordx4 v250, s[46:47]\n\t"
    "v_add_u32_e32 v203, s52, v203\n\t"
    "s_waitcnt lgkmcnt(5)\n\t"
    "v_mfma_f32_32x32x16_bf16 v[144:159], v[184:187], v[136:139], v[144:159]\n\t"
    "ds_read_b64_tr_b16 v[180:181], v200 offset:2048\n\t"
    "ds_read_b64_tr_b16 v[182:183], v200 offset:2560\n\t"
    "s_waitcnt lgkmcnt(5)\n\t"
    "v_mfma_f32_32x32x16_bf16 v[160:175], v[188:191], v[140:143], v[144:159]\n\t"
    "ds_read_b64_tr_b16 v[184:185], v200 offset:4096\n\t"
    "ds_read_b64_tr_b16 v[186:187], v200 offset:4608\n\t"
    "s_add_u32 m0, s50, 40960\n\t"
    "s_add_u32 s46, s40, 442624\n\t"
    "s_addc_u32 s47, s41, 0\n\t"
    "global_load_lds_dwordx4 v250, s[46:47]\n\t"
    "s_sub_u32 s52, 0, s52\n\t"
    "s_mov_b32 s46, s50\n\t"
    "s_mov_b32 s50, s51\n\t"
    "s_mov_b32 s51, s46\n\t"
    "s_add_u32 s40, s40, 128\n\t"
    "s_addc_u32 s41, s41, 0\n\t"
    "s_lshr_b64 s[58:59], s[58:59], 8\n\t"
    "s_add_u32 s62, s62, 1\n\t"
    "s_add_u32 s63, s63, 64\n\t"
    "s_cmp_eq_u32 s56, 2\n\t"
    "s_cselect_b32 s54, 2, 3\n\t"
    ".p2align 4\n\t"
    ".Lk1q%=:\n\t"
    "s_waitcnt lgkmcnt(4)\n\t"
    "v_mfma_f32_32x32x16_bf16 v[144:159], v[176:179], v[0:3], 0\n\t"
    "ds_read_b64_tr_b16 v[188:189], v201 offset:0\n\t"
    "ds_read_b64_tr_b16 v[190:191], v201 offset:512\n\t"
    "s_cmp_ge_u32 s62, 9\n\t"
    "s_cselect_b32 s60, 1.0, 0x3eae1a93\n\t"
    "s_movk_i32 s61, 0x2000\n\t"
    "s_cmov_b32 s61, 159744\n\t"
    "s_waitcnt lgkmcnt(4)\n\t"
    "v_mfma_f32_32x32x16_bf16 v[144:159], v[180:183], v[4:7], v[144:159]\n\t"
    "ds_read_b64_tr_b16 v[176:177], v201 offset:2048\n\t"
    "ds_read_b64_tr_b16 v[178:179], v201 offset:2560\n\t"
    "s_waitcnt lgkmcnt(4)\n\t"
    "v_mfma_f32_32x32x16_bf16 v[144:159], v[184:187], v[8:11], v[144:159]\n\t"
    "ds_read_b64_tr_b16 v[180:181], v201 offset:4096\n\t"
    "ds_read_b64_tr_b16 v[182:183], v201 offset:4608\n\t"
    "v_mul_f32_e32 v212, s60, v160\n\t"
    "v_mul_f32_e32 v213, s60, v161\n\t"
    "v_mul_f32_e32 v214, s60, v162\n\t"
    "v_mul_f32_e32 v215, s60, v163\n\t"
    "s_waitcnt lgkmcnt(4)\n\t"
    "v_mfma_f32_32x32x16_bf16 v[144:159], v[188:191], v[12:15], v[144:159]\n\t"
    "ds_read_b64_tr_b16 v[184:185], v202 offset:0\n\t"
    "ds_read_b64_tr_b16 v[186:187], v202 offset:512\n\t"
    "v_cvt_pk_bf16_f32 v216, v212, v213\n\t"
    "v_cvt_pk_bf16_f32 v217, v214, v215\n\t"
    "v_add_u32_e32 v218, v208, v224\n\t"
    "s_waitcnt lgkmcnt(4)\n\t"
    "v_mfma_f32_32x32x16_bf16 v[144:159], v[176:179], v[16:19], v[144:159]\n\t"
    "ds_read_b64_tr_b16 v[188:189], v202 offset:2048\n\t"
    "ds_read_b64_tr_b16 v[190:191], v202 offset:2560\n\t"
    "s_and_b32 s46, s58, 3\n\t"
    "s_cbranch_scc1 .Lk1t4%=\n\t"
    "global_store_dwordx2 v218, v[216:217], s[44:45]\n\t"
    ".Lk1b4%=:\n\t"
    "s_waitcnt lgkmcnt(4)\n\t"
    "v_mfma_f32_32x32x16_bf16 v[144:159], v[180:183], v[20:23], v[144:159]\n\t"
    "ds_read_b64_tr_b16 v[176:177], v202 offset:4096\n\t"
    "ds_read_b64_tr_b16 v[178:179], v202 offset:4608\n\t"
    "s_waitcnt lgkmcnt(4)\n\t"
    "v_mfma_f32_32x32x16_bf16 v[144:159], v[184:187], v[24:27], v[144:159]\n\t"
    "ds_read_b64_tr_b16 v[180:181], v203 offset:0\n\t"
    "ds_read_b64_tr_b16 v[182:183], v203 offset:512\n\t"
    "s_waitcnt vmcnt(13)\n\t"
    "s_barrier\n\t"
    "s_waitcnt lgkmcnt(4)\n\t"
    "v_mfma_f32_32x32x16_bf16 v[144:159], v[188:191], v[28:31], v[144:159]\n\t"
    "ds_read_b64_tr_b16 v[184:185], v203 offset:2048\n\t"
    "ds_read_b64_tr_b16 v[186:187], v203 offset:2560\n\t"
    "s_add_u32 m0, s51, 49152\n\t"
    "s_add_u32 s46, s40, 128\n\t"
    "s_addc_u32 s47, s41, 0\n\t"
    "global_load_lds_dwordx4 v251, s[46:47]\n\t"
    "s_waitcnt lgkmcnt(4)\n\t"
    "v_mfma_f32_32x32x16_bf16 v[144:159], v[176:179], v[32:35], v[144:159]\n\t"
    "ds_read_b64_tr_b16 v[188:189], v203 offset:4096\n\t"
    "ds_read_b64_tr_b16 v[190:191], v203 offset:4608\n\t"
    "s_waitcnt lgkmcnt(4)\n\t"
    "v_mfma_f32_32x32x16_bf16 v[144:159], v[180:183], v[36:39], v[144:159]\n\t"
    "ds_read_b64_tr_b16 v[176:177], v200 offset:24576\n\t"
    "ds_read_b64_tr_b16 v[178:179], v200 offset:25088\n\t"
    "s_add_u32 m0, s51, 57344\n\t"
    "s_add_u32 s46, s40, 221312\n\t"
    "s_addc_u32 s47, s41, 0\n\t"
    "global_load_lds_dwordx4 v251, s[46:47]\n\t"
    "s_waitcnt lgkmcnt(4)\n\t"
    "v_mfma_f32_32x32x16_bf16 v[144:159], v[184:187], v[40:43], v[144:159]\n\t"
    "ds_read_b64_tr_b16 v[180:181], v200 offset:26624\n\t"
    "ds_read_b64_tr_b16 v[182:183], v200 offset:27136\n\t"
    "v_mul_f32_e32 v212, s60, v164\n\t"
    "v_mul_f32_e32 v213, s60, v165\n\t"
    "v_mul_f32_e32 v214, s60, v166\n\t"
    "v_mul_f32_e32 v215, s60, v167\n\t"
    "s_waitcnt lgkmcnt(4)\n\t"
    "v_mfma_f32_32x32x16_bf16 v[144:159], v[188:191], v[44:47], v[144:159]\n\t"
    "ds_read_b64_tr_b16 v[184:185], v200 offset:28672\n\t"
    "ds_read_b64_tr_b16 v[186:187], v200 offset:29184\n\t"
    "s_add_u32 m0, s51, 65536\n\t"
    "s_add_u32 s46, s40, 442496\n\t"
    "s_addc_u32 s47, s41, 0\n\t"
    "global_load_lds_dwordx4 v251, s[46:47]\n\t"
    "v_cvt_pk_bf16_f32 v216, v212, v213\n\t"
    "v_cvt_pk_bf16_f32 v217, v214, v215\n\t"
    "v_add_u32_e32 v218, v209, v224\n\t"
    "s_waitcnt lgkmcnt(4)\n\t"
    "v_mfma_f32_32x32x16_bf16 v[144:159], v[176:179], v[48:51], v[144:159]\n\t"
    "ds_read_b64_tr_b16 v[188:189], v201 offset:24576\n\t"
    "ds_read_b64_tr_b16 v[190:191], v201 offset:25088\n\t"
    "s_and_b32 s46, s58, 12\n\t"
    "s_cbranch_scc1 .Lk1t5%=\n\t"
    "global_store_dwordx2 v218, v[216:217], s[44:45]\n\t"
    ".Lk1b5%=:\n\t"
    "s_waitcnt lgkmcnt(4)\n\t"
    "v_mfma_f32_32x32x16_bf16 v[144:159], v[180:183], v[52:55], v[144:159]\n\t"
    "ds_read_b64_tr_b16 v[176:177], v201 offset:26624\n\t"
    "ds_read_b64_tr_b16 v[178:179], v201 offset:27136\n\t"
    "s_waitcnt lgkmcnt(4)\n\t"
    "v_mfma_f32_32x32x16_bf16 v[144:159], v[184:187], v[56:59], v[144:159]\n\t"
    "ds_read_b64_tr_b16 v[180:181], v201 offset:28672\n\t"
    "ds_read_b64_tr_b16 v[182:183], v201 offset:29184\n\t"
    "s_waitcnt lgkmcnt(4)\n\t"
    "v_mfma_f32_32x32x16_bf16 v[144:159], v[188:191], v[60:63], v[144:159]\n\t"
    "ds_read_b64_tr_b16 v[184:185], v202 offset:24576\n\t"
    "ds_read_b64_tr_b16 v[186:187], v202 offset:25088\n\t"
    "s_waitcnt lgkmcnt(4)\n\t"
    "v_mfma_f32_32x32x16_bf16 v[144:159], v[176:179], v[64:67], v[144:159]\n\t"
    "ds_read_b64_tr_b16 v[188:189], v202 offset:26624\n\t"
    "ds_read_b64_tr_b16 v[190:191], v202 offset:27136\n\t"
    "s_waitcnt lgkmcnt(4)\n\t"
    "v_mfma_f32_32x32x16_bf16 v[144:159], v[180:183], v[68:71], v[144:159]\n\t"
    "ds_read_b64_tr_b16 v[176:177], v202 offset:28672\n\t"
    "ds_read_b64_tr_b16 v[178:179], v202 offset:29184\n\t"
    "s_waitcnt lgkmcnt(4)\n\t"
    "v_mfma_f32_32x32x16_bf16 v[144:159], v[184:187], v[72:75], v[144:159]\n\t"
    "ds_read_b64_tr_b16 v[180:181], v203 offset:24576\n\t"
    "ds_read_b64_tr_b16 v[182:183], v203 offset:25088\n\t"
    "s_waitcnt vmcnt(13)\n\t"
    "s_barrier\n\t"
    "v_mul_f32_e32 v212, s60, v168\n\t"
    "v_mul_f32_e32 v213, s60, v169\n\t"
    "v_mul_f32_e32 v214, s60, v170\n\t"
    "v_mul_f32_e32 v215, s60, v171\n\t"
    "s_waitcnt lgkmcnt(4)\n\t"
    "v_mfma_f32_32x32x16_bf16 v[144:159], v[188:191], v[76:79], v[144:159]\n\t"
    "ds_read_b64_tr_b16 v[184:185], v203 offset:26624\n\t"
    "ds_read_b64_tr_b16 v[186:187], v203 offset:27136\n\t"
    "s_add_u32 m0, s50, 0\n\t"
    "s_add_u32 s46, s40, 256\n\t"
    "s_addc_u32 s47, s41, 0\n\t"
    "global_load_lds_dwordx4 v249, s[46:47]\n\t"
    "v_cvt_pk_bf16_f32 v216, v212, v213\n\t"
    "v_cvt_pk_bf16_f32 v217, v214, v215\n\t"
    "v_add_u32_e32 v218, v210, v224\n\t"
    "s_waitcnt lgkmcnt(4)\n\t"
    "v_mfma_f32_32x32x16_bf16 v[144:159], v[176:179], v[80:83], v[144:159]\n\t"
    "ds_read_b64_tr_b16 v[188:189], v203 offset:28672\n\t"
    "ds_read_b64_tr_b16 v[190:191], v203 offset:29184\n\t"
    "s_and_b32 s46, s58, 48\n\t"
    "s_cbranch_scc1 .Lk1t6%=\n\t"
    "global_store_dwordx2 v218, v[216:217], s[44:45]\n\t"
    ".Lk1b6%=:\n\t"
    "s_waitcnt lgkmcnt(4)\n\t"
    "v_mfma_f32_32x32x16_bf16 v[144:159], v[180:183], v[84:87], v[144:159]\n\t"
    "ds_read_b64_tr_b16 v[176:177], v200 offset:49152\n\t"
    "ds_read_b64_tr_b16 v[178:179], v200 offset:49664\n\t"
    "s_add_u32 m0, s50, 8192\n\t"
    "s_add_u32 s46, s40, 221440\n\t"
    "s_addc_u32 s47, s41, 0\n\t"
    "global_load_lds_dwordx4 v249, s[46:47]\n\t"
    "s_waitcnt lgkmcnt(4)\n\t"
    "v_mfma_f32_32x32x16_bf16 v[144:159], v[184:187], v[88:91], v[144:159]\n\t"
    "ds_read_b64_tr_b16 v[180:181], v200 offset:51200\n\t"
    "ds_read_b64_tr_b16 v[182:183], v200 offset:51712\n\t"
    "s_waitcnt lgkmcnt(4)\n\t"
    "v_mfma_f32_32x32x16_bf16 v[144:159], v[188:191], v[92:95], v[144:159]\n\t"
    "ds_read_b64_tr_b16 v[184:185], v200 offset:53248\n\t"
    "ds_read_b64_tr_b16 v[186:187], v200 offset:53760\n\t"
    "s_add_u32 m0, s50, 16384\n\t"
    "s_add_u32 s46, s40, 442624\n\t"
    "s_addc_u32 s47, s41, 0\n\t"
    "global_load_lds_dwordx4 v249, s[46:47]\n\t"
    "s_waitcnt lgkmcnt(4)\n\t"
    "v_mfma_f32_32x32x16_bf16 v[144:159], v[176:179], v[96:99], v[144:159]\n\t"
    "ds_read_b64_tr_b16 v[188:189], v201 offset:49152\n\t"
    "ds_read_b64_tr_b16 v[190:191], v201 offset:49664\n\t"
    "v_add_u32_e32 v200, s52, v200\n\t"
    "s_waitcnt lgkmcnt(4)\n\t"
    "v_mfma_f32_32x32x16_bf16 v[144:159], v[180:183], v[100:103], v[144:159]\n\t"
    "ds_read_b64_tr_b16 v[176:177], v201 offset:51200\n\t"
    "ds_read_b64_tr_b16 v[178:179], v201 offset:51712\n\t"
    "s_waitcnt lgkmcnt(4)\n\t"
    "v_mfma_f32_32x32x16_bf16 v[144:159], v[184:187], v[104:107], v[144:159]\n\t"
    "ds_read_b64_tr_b16 v[180:181], v201 offset:53248\n\t"
    "ds_read_b64_tr_b16 v[182:183], v201 offset:53760\n\t"
    "v_mul_f32_e32 v212, s60, v172\n\t"
    "v_mul_f32_e32 v213, s60, v173\n\t"
    "v_mul_f32_e32 v214, s60, v174\n\t"
    "v_mul_f32_e32 v215, s60, v175\n\t"
    "s_waitcnt lgkmcnt(4)\n\t"
    "v_mfma_f32_32x32x16_bf16 v[144:159], v[188:191], v[108:111], v[144:159]\n\t"
    "ds_read_b64_tr_b16 v[184:185], v202 offset:49152\n\t"
    "ds_read_b64_tr_b16 v[186:187], v202 offset:49664\n\t"
    "v_add_u32_e32 v201, s52, v201\n\t"
    "v_cvt_pk_bf16_f32 v216, v212, v213\n\t"
    "v_cvt_pk_bf16_f32 v217, v214, v215\n\t"
    "v_add_u32_e32 v218, v211, v224\n\t"
    "s_waitcnt lgkmcnt(4)\n\t"
    "v_mfma_f32_32x32x16_bf16 v[144:159], v[176:179], v[112:115], v[144:159]\n\t"
    "ds_read_b64_tr_b16 v[188:189], v202 offset:51200\n\t"
    "ds_read_b64_tr_b16 v[190:191], v202 offset:51712\n\t"
    "s_and_b32 s46, s58, 192\n\t"
    "s_cbranch_scc1 .Lk1t7%=\n\t"
    "global_store_dwordx2 v218, v[216:217], s[44:45]\n\t"
    ".Lk1b7%=:\n\t"
    "s_waitcnt lgkmcnt(4)\n\t"
    "v_mfma_f32_32x32x16_bf16 v[144:159], v[180:183], v[116:119], v[144:159]\n\t"
    "ds_read_b64_tr_b16 v[176:177], v202 offset:53248\n\t"
    "ds_read_b64_tr_b16 v[178:179], v202 offset:53760\n\t"
    "s_waitcnt lgkmcnt(4)\n\t"
    "v_mfma_f32_32x32x16_bf16 v[144:159], v[184:187], v[120:123], v[144:159]\n\t"
    "ds_read_b64_tr_b16 v[180:181], v203 offset:49152\n\t"
    "ds_read_b64_tr_b16 v[182:183], v203 offset:49664\n\t"
    "s_waitcnt vmcnt(14)\n\t"
    "s_barrier\n\t"
    "v_add_u32_e32 v202, s52, v202\n\t"
    "v_add_u32_e32 v226, 32, v226\n\t"
    "s_waitcnt lgkmcnt(4)\n\t"
    "v_mfma_f32_32x32x16_bf16 v[144:159], v[188:191], v[124:127], v[144:159]\n\t"
    "ds_read_b64_tr_b16 v[184:185], v203 offset:51200\n\t"
    "ds_read_b64_tr_b16 v[186:187], v203 offset:51712\n\t"
    "s_add_u32 m0, s50, 24576\n\t"
    "s_add_u32 s46, s40, 256\n\t"
    "s_addc_u32 s47, s41, 0\n\t"
    "global_load_lds_dwordx4 v250, s[46:47]\n\t"
    "s_waitcnt lgkmcnt(4)\n\t"
    "v_mfma_f32_32x32x16_bf16 v[144:159], v[176:179], v[128:131], v[144:159]\n\t"
    "ds_read_b64_tr_b16 v[188:189], v203 offset:53248\n\t"
    "ds_read_b64_tr_b16 v[190:191], v203 offset:53760\n\t"
    "ds_read_b128 v[208:211], v226\n\t"
    "s_waitcnt lgkmcnt(5)\n\t"
    "v_mfma_f32_32x32x16_bf16 v[144:159], v[180:183], v[132:135], v[144:159]\n\t"
    "ds_read_b64_tr_b16 v[176:177], v200 offset:0\n\t"
    "ds_read_b64_tr_b16 v[178:179], v200 offset:512\n\t"
    "s_add_u32 m0, s50, 32768\n\t"
    "s_add_u32 s46, s40, 221440\n\t"
    "s_addc_u32 s47, s41, 0\n\t"
    "global_load_lds_dwordx4 v250, s[46:47]\n\t"
    "v_add_u32_e32 v203, s52, v203\n\t"
    "s_waitcnt lgkmcnt(5)\n\t"
    "v_mfma_f32_32x32x16_bf16 v[144:159], v[184:187], v[136:139], v[144:159]\n\t"
    "ds_read_b64_tr_b16 v[180:181], v200 offset:2048\n\t"
    "ds_read_b64_tr_b16 v[182:183], v200 offset:2560\n\t"
    "s_waitcnt lgkmcnt(5)\n\t"
    "v_mfma_f32_32x32x16_bf16 v[160:175], v[188:191], v[140:143], v[144:159]\n\t"
    "ds_read_b64_tr_b16 v[184:185], v200 offset:4096\n\t"
    "ds_read_b64_tr_b16 v[186:187], v200 offset:4608\n\t"
    "s_add_u32 m0, s50, 40960\n\t"
    "s_add_u32 s46, s40, 442624\n\t"
    "s_addc_u32 s47, s41, 0\n\t"
    "global_load_lds_dwordx4 v250, s[46:47]\n\t"
    "s_sub_u32 s52, 0, s52\n\t"
    "s_mov_b32 s46, s50\n\t"
    "s_mov_b32 s50, s51\n\t"
    "s_mov_b32 s51, s46\n\t"
    "s_add_u32 s40, s40, 128\n\t"
    "s_addc_u32 s41, s41, 0\n\t"
    "s_lshr_b64 s[58:59], s[58:59], 8\n\t"
    "s_add_u32 s62, s62, 1\n\t"
    "s_add_u32 s63, s63, 64\n\t"
    "s_sub_u32 s54, s54, 1\n\t"
    "s_cmp_lg_u32 s54, 0\n\t"
    "s_cbranch_scc1 .Lk1q%=\n\t"
    "s_cmp_eq_u32 s56, 2\n\t"
    "s_cbranch_scc1 .Lk1qv%=\n\t"
    "s_waitcnt lgkmcnt(4)\n\t"
    "v_mfma_f32_32x32x16_bf16 v[144:159], v[176:179], v[0:3], 0\n\t"
    "ds_read_b64_tr_b16 v[188:189], v201 offset:0\n\t"
    "ds_read_b64_tr_b16 v[190:191], v201 offset:512\n\t"
    "s_cmp_ge_u32 s62, 9\n\t"
    "s_cselect_b32 s60, 1.0, 0x3eae1a93\n\t"
    "s_movk_i32 s61, 0x2000\n\t"
    "s_cmov_b32 s61, 159744\n\t"
    "s_waitcnt lgkmcnt(4)\n\t"
    "v_mfma_f32_32x32x16_bf16 v[144:159], v[180:183], v[4:7], v[144:159]\n\t"
    "ds_read_b64_tr_b16 v[176:177], v201 offset:2048\n\t"
    "ds_read_b64_tr_b16 v[178:179], v201 offset:2560\n\t"
    "s_waitcnt lgkmcnt(4)\n\t"
    "v_mfma_f32_32x32x16_bf16 v[144:159], v[184:187], v[8:11], v[144:159]\n\t"
    "ds_read_b64_tr_b16 v[180:181], v201 offset:4096\n\t"
    "ds_read_b64_tr_b16 v[182:183], v201 offset:4608\n\t"
    "v_mul_f32_e32 v212, s60, v160\n\t"
    "v_mul_f32_e32 v213, s60, v161\n\t"
    "v_mul_f32_e32 v214, s60, v162\n\t"
    "v_mul_f32_e32 v215, s60, v163\n\t"
    "s_waitcnt lgkmcnt(4)\n\t"
    "v_mfma_f32_32x32x16_bf16 v[144:159], v[188:191], v[12:15], v[144:159]\n\t"
    "ds_read_b64_tr_b16 v[184:185], v202 offset:0\n\t"
    "ds_read_b64_tr_b16 v[186:187], v202 offset:512\n\t"
    "v_cvt_pk_bf16_f32 v216, v212, v213\n\t"
    "v_cvt_pk_bf16_f32 v217, v214, v215\n\t"
    "v_add_u32_e32 v218, v208, v224\n\t"
    "s_waitcnt lgkmcnt(4)\n\t"
    "v_mfma_f32_32x32x16_bf16 v[144:159], v[176:179], v[16:19], v[144:159]\n\t"
    "ds_read_b64_tr_b16 v[188:189], v202 offset:2048\n\t"
    "ds_read_b64_tr_b16 v[190:191], v202 offset:2560\n\t"
    "s_and_b32 s46, s58, 3\n\t"
    "s_cbranch_scc1 .Lk1t8%=\n\t"
    "global_store_dwordx2 v218, v[216:217], s[44:45]\n\t"
    ".Lk1b8%=:\n\t"
    "s_waitcnt lgkmcnt(4)\n\t"
    "v_mfma_f32_32x32x16_bf16 v[144:159], v[180:183], v[20:23], v[144:159]\n\t"
    "ds_read_b64_tr_b16 v[176:177], v202 offset:4096\n\t"
    "ds_read_b64_tr_b16 v[178:179], v202 offset:4608\n\t"
    "s_waitcnt lgkmcnt(4)\n\t"
    "v_mfma_f32_32x32x16_bf16 v[144:159], v[184:187], v[24:27], v[144:159]\n\t"
    "ds_read_b64_tr_b16 v[180:181], v203 offset:0\n\t"
    "ds_read_b64_tr_b16 v[182:183], v203 offset:512\n\t"
    "s_waitcnt vmcnt(14)\n\t"
    "s_barrier\n\t"
    "s_waitcnt lgkmcnt(4)\n\t"
    "v_mfma_f32_32x32x16_bf16 v[144:159], v[188:191], v[28:31], v[144:159]\n\t"
    "ds_read_b64_tr_b16 v[184:185], v203 offset:2048\n\t"
    "ds_read_b64_tr_b16 v[186:187], v203 offset:2560\n\t"
    "s_add_u32 m0, s51, 49152\n\t"
    "s_add_u32 s46, s40, 128\n\t"
    "s_addc_u32 s47, s41, 0\n\t"
    "global_load_lds_dwordx4 v251, s[46:47]\n\t"
    "s_waitcnt lgkmcnt(4)\n\t"
    "v_mfma_f32_32x32x16_bf16 v[144:159], v[176:179], v[32:35], v[144:159]\n\t"
    "ds_read_b64_tr_b16 v[188:189], v203 offset:4096\n\t"
    "ds_read_b64_tr_b16 v[190:191], v203 offset:4608\n\t"
    "s_waitcnt lgkmcnt(4)\n\t"
    "v_mfma_f32_32x32x16_bf16 v[144:159], v[180:183], v[36:39], v[144:159]\n\t"
    "ds_read_b64_tr_b16 v[176:177], v200 offset:24576\n\t"
    "ds_read_b64_tr_b16 v[178:179], v200 offset:25088\n\t"
    "s_add_u32 m0, s51, 57344\n\t"
    "s_add_u32 s46, s40, 221312\n\t"
    "s_addc_u32 s47, s41, 0\n\t"
    "global_load_lds_dwordx4 v251, s[46:47]\n\t"
    "s_waitcnt lgkmcnt(4)\n\t"
    "v_mfma_f32_32x32x16_bf16 v[144:159], v[184:187], v[40:43], v[144:159]\n\t"
    "ds_read_b64_tr_b16 v[180:181], v200 offset:26624\n\t"
    "ds_read_b64_tr_b16 v[182:183], v200 offset:27136\n\t"
    "v_mul_f32_e32 v212, s60, v164\n\t"
    "v_mul_f32_e32 v213, s60, v165\n\t"
    "v_mul_f32_e32 v214, s60, v166\n\t"
    "v_mul_f32_e32 v215, s60, v167\n\t"
    "s_waitcnt lgkmcnt(4)\n\t"
    "v_mfma_f32_32x32x16_bf16 v[144:159], v[188:191], v[44:47], v[144:159]\n\t"
    "ds_read_b64_tr_b16 v[184:185], v200 offset:28672\n\t"
    "ds_read_b64_tr_b16 v[186:187], v200 offset:29184\n\t"
    "s_add_u32 m0, s51, 65536\n\t"
    "s_add_u32 s46, s40, 442496\n\t"
    "s_addc_u32 s47, s41, 0\n\t"
    "global_load_lds_dwordx4 v251, s[46:47]\n\t"
    "v_cvt_pk_bf16_f32 v216, v212, v213\n\t"
    "v_cvt_pk_bf16_f32 v217, v214, v215\n\t"
    "v_add_u32_e32 v218, v209, v224\n\t"
    "s_waitcnt lgkmcnt(4)\n\t"
    "v_mfma_f32_32x32x16_bf16 v[144:159], v[176:179], v[48:51], v[144:159]\n\t"
    "ds_read_b64_tr_b16 v[188:189], v201 offset:24576\n\t"
    "ds_read_b64_tr_b16 v[190:191], v201 offset:25088\n\t"
    "s_and_b32 s46, s58, 12\n\t"
    "s_cbranch_scc1 .Lk1t9%=\n\t"
    "global_store_dwordx2 v218, v[216:217], s[44:45]\n\t"
    ".Lk1b9%=:\n\t"
    "s_waitcnt lgkmcnt(4)\n\t"
    "v_mfma_f32_32x32x16_bf16 v[144:159], v[180:183], v[52:55], v[144:159]\n\t"
    "ds_read_b64_tr_b16 v[176:177], v201 offset:26624\n\t"
    "ds_read_b64_tr_b16 v[178:179], v201 offset:27136\n\t"
    "s_waitcnt lgkmcnt(4)\n\t"
    "v_mfma_f32_32x32x16_bf16 v[144:159], v[184:187], v[56:59], v[144:159]\n\t"
    "ds_read_b64_tr_b16 v[180:181], v201 offset:28672\n\t"
    "ds_read_b64_tr_b16 v[182:183], v201 offset:29184\n\t"
    "s_waitcnt lgkmcnt(4)\n\t"
    "v_mfma_f32_32x32x16_bf16 v[144:159], v[188:191], v[60:63], v[144:159]\n\t"
    "ds_read_b64_tr_b16 v[184:185], v202 offset:24576\n\t"
    "ds_read_b64_tr_b16 v[186:187], v202 offset:25088\n\t"
    "s_waitcnt lgkmcnt(4)\n\t"
    "v_mfma_f32_32x32x16_bf16 v[144:159], v[176:179], v[64:67], v[144:159]\n\t"
    "ds_read_b64_tr_b16 v[188:189], v202 offset:26624\n\t"
    "ds_read_b64_tr_b16 v[190:191], v202 offset:27136\n\t"
    "s_waitcnt lgkmcnt(4)\n\t"
    "v_mfma_f32_32x32x16_bf16 v[144:159], v[180:183], v[68:71], v[144:159]\n\t"
    "ds_read_b64_tr_b16 v[176:177], v202 offset:28672\n\t"
    "ds_read_b64_tr_b16 v[178:179], v202 offset:29184\n\t"
    "s_waitcnt lgkmcnt(4)\n\t"
    "v_mfma_f32_32x32x16_bf16 v[144:159], v[184:187], v[72:75], v[144:159]\n\t"
    "ds_read_b64_tr_b16 v[180:181], v203 offset:24576\n\t"
    "ds_read_b64_tr_b16 v[182:183], v203 offset:25088\n\t"
    "s_waitcnt vmcnt(14)\n\t"
    "s_barrier\n\t"
    "v_mul_f32_e32 v212, s60, v168\n\t"
    "v_mul_f32_e32 v213, s60, v169\n\t"
    "v_mul_f32_e32 v214, s60, v170\n\t"
    "v_mul_f32_e32 v215, s60, v171\n\t"
    "s_waitcnt lgkmcnt(4)\n\t"
    "v_mfma_f32_32x32x16_bf16 v[144:159], v[188:191], v[76:79], v[144:159]\n\t"
    "ds_read_b64_tr_b16 v[184:185], v203 offset:26624\n\t"
    "ds_read_b64_tr_b16 v[186:187], v203 offset:27136\n\t"
    "v_cvt_pk_bf16_f32 v216, v212, v213\n\t"
    "v_cvt_pk_bf16_f32 v217, v214, v215\n\t"
    "v_add_u32_e32 v218, v210, v224\n\t"
    "s_waitcnt lgkmcnt(4)\n\t"
    "v_mfma_f32_32x32x16_bf16 v[144:159], v[176:179], v[80:83], v[144:159]\n\t"
    "ds_read_b64_tr_b16 v[188:189], v203 offset:28672\n\t"
    "ds_read_b64_tr_b16 v[190:191], v203 offset:29184\n\t"
    "s_and_b32 s46, s58, 48\n\t"
    "s_cbranch_scc1 .Lk1t10%=\n\t"
    "global_store_dwordx2 v218, v[216:217], s[44:45]\n\t"
    ".Lk1b10%=:\n\t"
    "s_waitcnt lgkmcnt(4)\n\t"
    "v_mfma_f32_32x32x16_bf16 v[144:159], v[180:183], v[84:87], v[144:159]\n\t"
    "ds_read_b64_tr_b16 v[176:177], v200 offset:49152\n\t"
    "ds_read_b64_tr_b16 v[178:179], v200 offset:49664\n\t"
    "s_waitcnt lgkmcnt(4)\n\t"
    "v_mfma_f32_32x32x16_bf16 v[144:159], v[184:187], v[88:91], v[144:159]\n\t"
    "ds_read_b64_tr_b16 v[180:181], v200 offset:51200\n\t"
    "ds_read_b64_tr_b16 v[182:183], v200 offset:51712\n\t"
    "s_waitcnt lgkmcnt(4)\n\t"
    "v_mfma_f32_32x32x16_bf16 v[144:159], v[188:191], v[92:95], v[144:159]\n\t"
    "ds_read_b64_tr_b16 v[184:185], v200 offset:53248\n\t"
    "ds_read_b64_tr_b16 v[186:187], v200 offset:53760\n\t"
    "s_waitcnt lgkmcnt(4)\n\t"
    "v_mfma_f32_32x32x16_bf16 v[144:159], v[176:179], v[96:99], v[144:159]\n\t"
    "ds_read_b64_tr_b16 v[188:189], v201 offset:49152\n\t"
    "ds_read_b64_tr_b16 v[190:191], v201 offset:49664\n\t"
    "v_add_u32_e32 v200, s52, v200\n\t"
    "s_waitcnt lgkmcnt(4)\n\t"
    "v_mfma_f32_32x32x16_bf16 v[144:159], v[180:183], v[100:103], v[144:159]\n\t"
    "ds_read_b64_tr_b16 v[176:177], v201 offset:51200\n\t"
    "ds_read_b64_tr_b16 v[178:179], v201 offset:51712\n\t"
    "s_waitcnt lgkmcnt(4)\n\t"
    "v_mfma_f32_32x32x16_bf16 v[144:159], v[184:187], v[104:107], v[144:159]\n\t"
    "ds_read_b64_tr_b16 v[180:181], v201 offset:53248\n\t"
    "ds_read_b64_tr_b16 v[182:183], v201 offset:53760\n\t"
    "v_mul_f32_e32 v212, s60, v172\n\t"
    "v_mul_f32_e32 v213, s60, v173\n\t"
    "v_mul_f32_e32 v214, s60, v174\n\t"
    "v_mul_f32_e32 v215, s60, v175\n\t"
    "s_waitcnt lgkmcnt(4)\n\t"
    "v_mfma_f32_32x32x16_bf16 v[144:159], v[188:191], v[108:111], v[144:159]\n\t"
    "ds_read_b64_tr_b16 v[184:185], v202 offset:49152\n\t"
    "ds_read_b64_tr_b16 v[186:187], v202 offset:49664\n\t"
    "v_add_u32_e32 v201, s52, v201\n\t"
    "v_cvt_pk_bf16_f32 v216, v212, v213\n\t"
    "v_cvt_pk_bf16_f32 v217, v214, v215\n\t"
    "v_add_u32_e32 v218, v211, v224\n\t"
    "s_waitcnt lgkmcnt(4)\n\t"
    "v_mfma_f32_32x32x16_bf16 v[144:159], v[176:179], v[112:115], v[144:159]\n\t"
    "ds_read_b64_tr_b16 v[188:189], v202 offset:51200\n\t"
    "ds_read_b64_tr_b16 v[190:191], v202 offset:51712\n\t"
    "s_and_b32 s46, s58, 192\n\t"
    "s_cbranch_scc1 .Lk1t11%=\n\t"
    "global_store_dwordx2 v218, v[216:217], s[44:45]\n\t"
    ".Lk1b11%=:\n\t"
    "s_waitcnt lgkmcnt(4)\n\t"
    "v_mfma_f32_32x32x16_bf16 v[144:159], v[180:183], v[116:119], v[144:159]\n\t"
    "ds_read_b64_tr_b16 v[176:177], v202 offset:53248\n\t"
    "ds_read_b64_tr_b16 v[178:179], v202 offset:53760\n\t"
    "s_waitcnt lgkmcnt(4)\n\t"
    "v_mfma_f32_32x32x16_bf16 v[144:159], v[184:187], v[120:123], v[144:159]\n\t"
    "ds_read_b64_tr_b16 v[180:181], v203 offset:49152\n\t"
    "ds_read_b64_tr_b16 v[182:183], v203 offset:49664\n\t"
    "s_waitcnt vmcnt(11)\n\t"
    "s_barrier\n\t"
    "v_add_u32_e32 v202, s52, v202\n\t"
    "v_add_u32_e32 v226, 32, v226\n\t"
    "s_waitcnt lgkmcnt(4)\n\t"
    "v_mfma_f32_32x32x16_bf16 v[144:159], v[188:191], v[124:127], v[144:159]\n\t"
    "ds_read_b64_tr_b16 v[184:185], v203 offset:51200\n\t"
    "ds_read_b64_tr_b16 v[186:187], v203 offset:51712\n\t"
    "s_waitcnt lgkmcnt(4)\n\t"
    "v_mfma_f32_32x32x16_bf16 v[144:159], v[176:179], v[128:131], v[144:159]\n\t"
    "ds_read_b64_tr_b16 v[188:189], v203 offset:53248\n\t"
    "ds_read_b64_tr_b16 v[190:191], v203 offset:53760\n\t"
    "ds_read_b128 v[208:211], v226\n\t"
    "s_waitcnt lgkmcnt(5)\n\t"
    "v_mfma_f32_32x32x16_bf16 v[144:159], v[180:183], v[132:135], v[144:159]\n\t"
    "ds_read_b64_tr_b16 v[176:177], v200 offset:0\n\t"
    "ds_read_b64_tr_b16 v[178:179], v200 offset:512\n\t"
    "v_add_u32_e32 v203, s52, v203\n\t"
    "s_waitcnt lgkmcnt(5)\n\t"
    "v_mfma_f32_32x32x16_bf16 v[144:159], v[184:187], v[136:139], v[144:159]\n\t"
    "ds_read_b64_tr_b16 v[180:181], v200 offset:2048\n\t"
    "ds_read_b64_tr_b16 v[182:183], v200 offset:2560\n\t"
    "s_waitcnt lgkmcnt(5)\n\t"
    "v_mfma_f32_32x32x16_bf16 v[160:175], v[188:191], v[140:143], v[144:159]\n\t"
    "ds_read_b64_tr_b16 v[184:185], v200 offset:4096\n\t"
    "ds_read_b64_tr_b16 v[186:187], v200 offset:4608\n\t"
    "s_sub_u32 s52, 0, s52\n\t"
    "s_mov_b32 s46, s50\n\t"
    "s_mov_b32 s50, s51\n\t"
    "s_mov_b32 s51, s46\n\t"
    "s_add_u32 s40, s40, 128\n\t"
    "s_addc_u32 s41, s41, 0\n\t"
    "s_lshr_b64 s[58:59], s[58:59], 8\n\t"
    "s_add_u32 s62, s62, 1\n\t"
    "s_add_u32 s63, s63, 64\n\t"
    "s_waitcnt lgkmcnt(4)\n\t"
    "v_mfma_f32_32x32x16_bf16 v[144:159], v[176:179], v[0:3], 0\n\t"
    "ds_read_b64_tr_b16 v[188:189], v201 offset:0\n\t"
    "ds_read_b64_tr_b16 v[190:191], v201 offset:512\n\t"
    "s_cmp_ge_u32 s62, 9\n\t"
    "s_cselect_b32 s60, 1.0, 0x3eae1a93\n\t"
    "s_movk_i32 s61, 0x2000\n\t"
    "s_cmov_b32 s61, 159744\n\t"
    "s_waitcnt lgkmcnt(4)\n\t"
    "v_mfma_f32_32x32x16_bf16 v[144:159], v[180:183], v[4:7], v[144:159]\n\t"
    "ds_read_b64_tr_b16 v[176:177], v201 offset:2048\n\t"
    "ds_read_b64_tr_b16 v[178:179], v201 offset:2560\n\t"
    "s_waitcnt lgkmcnt(4)\n\t"
    "v_mfma_f32_32x32x16_bf16 v[144:159], v[184:187], v[8:11], v[144:159]\n\t"
    "ds_read_b64_tr_b16 v[180:181], v201 offset:4096\n\t"
    "ds_read_b64_tr_b16 v[182:183], v201 offset:4608\n\t"
    "v_mul_f32_e32 v212, s60, v160\n\t"
    "v_mul_f32_e32 v213, s60, v161\n\t"
    "v_mul_f32_e32 v214, s60, v162\n\t"
    "v_mul_f32_e32 v215, s60, v163\n\t"
    "s_waitcnt lgkmcnt(4)\n\t"
    "v_mfma_f32_32x32x16_bf16 v[144:159], v[188:191], v[12:15], v[144:159]\n\t"
    "ds_read_b64_tr_b16 v[184:185], v202 offset:0\n\t"
    "ds_read_b64_tr_b16 v[186:187], v202 offset:512\n\t"
    "v_cvt_pk_bf16_f32 v216, v212, v213\n\t"
    "v_cvt_pk_bf16_f32 v217, v214, v215\n\t"
    "v_add_u32_e32 v218, v208, v224\n\t"
    "s_waitcnt lgkmcnt(4)\n\t"
    "v_mfma_f32_32x32x16_bf16 v[144:159], v[176:179], v[16:19], v[144:159]\n\t"
    "ds_read_b64_tr_b16 v[188:189], v202 offset:2048\n\t"
    "ds_read_b64_tr_b16 v[190:191], v202 offset:2560\n\t"
    "s_and_b32 s46, s58, 3\n\t"
    "s_cbranch_scc1 .Lk1t12%=\n\t"
    "global_store_dwordx2 v218, v[216:217], s[44:45]\n\t"
    ".Lk1b12%=:\n\t"
    "s_waitcnt lgkmcnt(4)\n\t"
    "v_mfma_f32_32x32x16_bf16 v[144:159], v[180:183], v[20:23], v[144:159]\n\t"
    "ds_read_b64_tr_b16 v[176:177], v202 offset:4096\n\t"
    "ds_read_b64_tr_b16 v[178:179], v202 offset:4608\n\t"
    "s_waitcnt lgkmcnt(4)\n\t"
    "v_mfma_f32_32x32x16_bf16 v[144:159], v[184:187], v[24:27], v[144:159]\n\t"
    "ds_read_b64_tr_b16 v[180:181], v203 offset:0\n\t"
    "ds_read_b64_tr_b16 v[182:183], v203 offset:512\n\t"
    "s_waitcnt vmcnt(8)\n\t"
    "s_barrier\n\t"
    "s_waitcnt lgkmcnt(4)\n\t"
    "v_mfma_f32_32x32x16_bf16 v[144:159], v[188:191], v[28:31], v[144:159]\n\t"
    "ds_read_b64_tr_b16 v[184:185], v203 offset:2048\n\t"
    "ds_read_b64_tr_b16 v[186:187], v203 offset:2560\n\t"
    "s_waitcnt lgkmcnt(4)\n\t"
    "v_mfma_f32_32x32x16_bf16 v[144:159], v[176:179], v[32:35], v[144:159]\n\t"
    "ds_read_b64_tr_b16 v[188:189], v203 offset:4096\n\t"
    "ds_read_b64_tr_b16 v[190:191], v203 offset:4608\n\t"
    "s_waitcnt lgkmcnt(4)\n\t"
    "v_mfma_f32_32x32x16_bf16 v[144:159], v[180:183], v[36:39], v[144:159]\n\t"
    "ds_read_b64_tr_b16 v[176:177], v200 offset:24576\n\t"
    "ds_read_b64_tr_b16 v[178:179], v200 offset:25088\n\t"
    "s_waitcnt lgkmcnt(4)\n\t"
    "v_mfma_f32_32x32x16_bf16 v[144:159], v[184:187], v[40:43], v[144:159]\n\t"
    "ds_read_b64_tr_b16 v[180:181], v200 offset:26624\n\t"
    "ds_read_b64_tr_b16 v[182:183], v200 offset:27136\n\t"
    "v_mul_f32_e32 v212, s60, v164\n\t"
    "v_mul_f32_e32 v213, s60, v165\n\t"
    "v_mul_f32_e32 v214, s60, v166\n\t"
    "v_mul_f32_e32 v215, s60, v167\n\t"
    "s_waitcnt lgkmcnt(4)\n\t"
    "v_mfma_f32_32x32x16_bf16 v[144:159], v[188:191], v[44:47], v[144:159]\n\t"
    "ds_read_b64_tr_b16 v[184:185], v200 offset:28672\n\t"
    "ds_read_b64_tr_b16 v[186:187], v200 offset:29184\n\t"
    "v_cvt_pk_bf16_f32 v216, v212, v213\n\t"
    "v_cvt_pk_bf16_f32 v217, v214, v215\n\t"
    "v_add_u32_e32 v218, v209, v224\n\t"
    "s_waitcnt lgkmcnt(4)\n\t"
    "v_mfma_f32_32x32x16_bf16 v[144:159], v[176:179], v[48:51], v[144:159]\n\t"
    "ds_read_b64_tr_b16 v[188:189], v201 offset:24576\n\t"
    "ds_read_b64_tr_b16 v[190:191], v201 offset:25088\n\t"
    "s_and_b32 s46, s58, 12\n\t"
    "s_cbranch_scc1 .Lk1t13%=\n\t"
    "global_store_dwordx2 v218, v[216:217], s[44:45]\n\t"
    ".Lk1b13%=:\n\t"
    "s_waitcnt lgkmcnt(4)\n\t"
    "v_mfma_f32_32x32x16_bf16 v[144:159], v[180:183], v[52:55], v[144:159]\n\t"
    "ds_read_b64_tr_b16 v[176:177], v201 offset:26624\n\t"
    "ds_read_b64_tr_b16 v[178:179], v201 offset:27136\n\t"
    "s_waitcnt lgkmcnt(4)\n\t"
    "v_mfma_f32_32x32x16_bf16 v[144:159], v[184:187], v[56:59], v[144:159]\n\t"
    "ds_read_b64_tr_b16 v[180:181], v201 offset:28672\n\t"
    "ds_read_b64_tr_b16 v[182:183], v201 offset:29184\n\t"
    "s_waitcnt lgkmcnt(4)\n\t"
    "v_mfma_f32_32x32x16_bf16 v[144:159], v[188:191], v[60:63], v[144:159]\n\t"
    "ds_read_b64_tr_b16 v[184:185], v202 offset:24576\n\t"
    "ds_read_b64_tr_b16 v[186:187], v202 offset:25088\n\t"
    "s_waitcnt lgkmcnt(4)\n\t"
    "v_mfma_f32_32x32x16_bf16 v[144:159], v[176:179], v[64:67], v[144:159]\n\t"
    "ds_read_b64_tr_b16 v[188:189], v202 offset:26624\n\t"
    "ds_read_b64_tr_b16 v[190:191], v202 offset:27136\n\t"
    "s_waitcnt lgkmcnt(4)\n\t"
    "v_mfma_f32_32x32x16_bf16 v[144:159], v[180:183], v[68:71], v[144:159]\n\t"
    "ds_read_b64_tr_b16 v[176:177], v202 offset:28672\n\t"
    "ds_read_b64_tr_b16 v[178:179], v202 offset:29184\n\t"
    "s_waitcnt lgkmcnt(4)\n\t"
    "v_mfma_f32_32x32x16_bf16 v[144:159], v[184:187], v[72:75], v[144:159]\n\t"
    "ds_read_b64_tr_b16 v[180:181], v203 offset:24576\n\t"
    "ds_read_b64_tr_b16 v[182:183], v203 offset:25088\n\t"
    "s_waitcnt vmcnt(5)\n\t"
    "s_barrier\n\t"
    "v_mul_f32_e32 v212, s60, v168\n\t"
    "v_mul_f32_e32 v213, s60, v169\n\t"
    "v_mul_f32_e32 v214, s60, v170\n\t"
    "v_mul_f32_e32 v215, s60, v171\n\t"
    "s_waitcnt lgkmcnt(4)\n\t"
    "v_mfma_f32_32x32x16_bf16 v[144:159], v[188:191], v[76:79], v[144:159]\n\t"
    "ds_read_b64_tr_b16 v[184:185], v203 offset:26624\n\t"
    "ds_read_b64_tr_b16 v[186:187], v203 offset:27136\n\t"
    "v_cvt_pk_bf16_f32 v216, v212, v213\n\t"
    "v_cvt_pk_bf16_f32 v217, v214, v215\n\t"
    "v_add_u32_e32 v218, v210, v224\n\t"
    "s_waitcnt lgkmcnt(4)\n\t"
    "v_mfma_f32_32x32x16_bf16 v[144:159], v[176:179], v[80:83], v[144:159]\n\t"
    "ds_read_b64_tr_b16 v[188:189], v203 offset:28672\n\t"
    "ds_read_b64_tr_b16 v[190:191], v203 offset:29184\n\t"
    "s_and_b32 s46, s58, 48\n\t"
    "s_cbranch_scc1 .Lk1t14%=\n\t"
    "global_store_dwordx2 v218, v[216:217], s[44:45]\n\t"
    ".Lk1b14%=:\n\t"
    "s_waitcnt lgkmcnt(4)\n\t"
    "v_mfma_f32_32x32x16_bf16 v[144:159], v[180:183], v[84:87], v[144:159]\n\t"
    "ds_read_b64_tr_b16 v[176:177], v200 offset:49152\n\t"
    "ds_read_b64_tr_b16 v[178:179], v200 offset:49664\n\t"
    "s_waitcnt lgkmcnt(4)\n\t"
    "v_mfma_f32_32x32x16_bf16 v[144:159], v[184:187], v[88:91], v[144:159]\n\t"
    "ds_read_b64_tr_b16 v[180:181], v200 offset:51200\n\t"
    "ds_read_b64_tr_b16 v[182:183], v200 offset:51712\n\t"
    "s_waitcnt lgkmcnt(4)\n\t"
    "v_mfma_f32_32x32x16_bf16 v[144:159], v[188:191], v[92:95], v[144:159]\n\t"
    "ds_read_b64_tr_b16 v[184:185], v200 offset:53248\n\t"
    "ds_read_b64_tr_b16 v[186:187], v200 offset:53760\n\t"
    "s_waitcnt lgkmcnt(4)\n\t"
    "v_mfma_f32_32x32x16_bf16 v[144:159], v[176:179], v[96:99], v[144:159]\n\t"
    "ds_read_b64_tr_b16 v[188:189], v201 offset:49152\n\t"
    "ds_read_b64_tr_b16 v[190:191], v201 offset:49664\n\t"
    "s_waitcnt lgkmcnt(4)\n\t"
    "v_mfma_f32_32x32x16_bf16 v[144:159], v[180:183], v[100:103], v[144:159]\n\t"
    "ds_read_b64_tr_b16 v[176:177], v201 offset:51200\n\t"
    "ds_read_b64_tr_b16 v[178:179], v201 offset:51712\n\t"
    "s_waitcnt lgkmcnt(4)\n\t"
    "v_mfma_f32_32x32x16_bf16 v[144:159], v[184:187], v[104:107], v[144:159]\n\t"
    "ds_read_b64_tr_b16 v[180:181], v201 offset:53248\n\t"
    "ds_read_b64_tr_b16 v[182:183], v201 offset:53760\n\t"
    "v_mul_f32_e32 v212, s60, v172\n\t"
    "v_mul_f32_e32 v213, s60, v173\n\t"
    "v_mul_f32_e32 v214, s60, v174\n\t"
    "v_mul_f32_e32 v215, s60, v175\n\t"
    "s_waitcnt lgkmcnt(4)\n\t"
    "v_mfma_f32_32x32x16_bf16 v[144:159], v[188:191], v[108:111], v[144:159]\n\t"
    "ds_read_b64_tr_b16 v[184:185], v202 offset:49152\n\t"
    "ds_read_b64_tr_b16 v[186:187], v202 offset:49664\n\t"
    "v_cvt_pk_bf16_f32 v216, v212, v213\n\t"
    "v_cvt_pk_bf16_f32 v217, v214, v215\n\t"
    "v_add_u32_e32 v218, v211, v224\n\t"
    "s_waitcnt lgkmcnt(4)\n\t"
    "v_mfma_f32_32x32x16_bf16 v[144:159], v[176:179], v[112:115], v[144:159]\n\t"
    "ds_read_b64_tr_b16 v[188:189], v202 offset:51200\n\t"
    "ds_read_b64_tr_b16 v[190:191], v202 offset:51712\n\t"
    "s_and_b32 s46, s58, 192\n\t"
    "s_cbranch_scc1 .Lk1t15%=\n\t"
    "global_store_dwordx2 v218, v[216:217], s[44:45]\n\t"
    ".Lk1b15%=:\n\t"
    "s_waitcnt lgkmcnt(4)\n\t"
    "v_mfma_f32_32x32x16_bf16 v[144:159], v[180:183], v[116:119], v[144:159]\n\t"
    "ds_read_b64_tr_b16 v[176:177], v202 offset:53248\n\t"
    "ds_read_b64_tr_b16 v[178:179], v202 offset:53760\n\t"
    "s_waitcnt lgkmcnt(4)\n\t"
    "v_mfma_f32_32x32x16_bf16 v[144:159], v[184:187], v[120:123], v[144:159]\n\t"
    "ds_read_b64_tr_b16 v[180:181], v203 offset:49152\n\t"
    "ds_read_b64_tr_b16 v[182:183], v203 offset:49664\n\t"
    "v_add_u32_e32 v226, 32, v226\n\t"
    "s_waitcnt lgkmcnt(4)\n\t"
    "v_mfma_f32_32x32x16_bf16 v[144:159], v[188:191], v[124:127], v[144:159]\n\t"
    "ds_read_b64_tr_b16 v[184:185], v203 offset:51200\n\t"
    "ds_read_b64_tr_b16 v[186:187], v203 offset:51712\n\t"
    "s_waitcnt lgkmcnt(4)\n\t"
    "v_mfma_f32_32x32x16_bf16 v[144:159], v[176:179], v[128:131], v[144:159]\n\t"
    "ds_read_b64_tr_b16 v[188:189], v203 offset:53248\n\t"
    "ds_read_b64_tr_b16 v[190:191], v203 offset:53760\n\t"
    "ds_read_b128 v[208:211], v226\n\t"
    "s_waitcnt lgkmcnt(5)\n\t"
    "v_mfma_f32_32x32x16_bf16 v[144:159], v[180:183], v[132:135], v[144:159]\n\t"
    "s_waitcnt lgkmcnt(3)\n\t"
    "v_mfma_f32_32x32x16_bf16 v[144:159], v[184:187], v[136:139], v[144:159]\n\t"
    "s_waitcnt lgkmcnt(1)\n\t"
    "v_mfma_f32_32x32x16_bf16 v[160:175], v[188:191], v[140:143], v[144:159]\n\t"
    "s_sub_u32 s52, 0, s52\n\t"
    "s_mov_b32 s46, s50\n\t"
    "s_mov_b32 s50, s51\n\t"
    "s_mov_b32 s51, s46\n\t"
    "s_add_u32 s40, s40, 128\n\t"
    "s_addc_u32 s41, s41, 0\n\t"
    "s_lshr_b64 s[58:59], s[58:59], 8\n\t"
    "s_add_u32 s62, s62, 1\n\t"
    "s_add_u32 s63, s63, 64\n\t"
    "v_lshlrev_b32_e32 v0, 2, v207\n\t"
    "global_load_dword v1, v0, s[38:39]\n\t"
    "s_nop 11\n\t"
    "s_cmp_ge_u32 s62, 9\n\t"
    "s_cselect_b32 s60, 1.0, 0x3eae1a93\n\t"
    "s_movk_i32 s61, 0x2000\n\t"
    "s_cmov_b32 s61, 159744\n\t"
    "v_mul_f32_e32 v212, s60, v160\n\t"
    "v_mul_f32_e32 v213, s60, v161\n\t"
    "v_mul_f32_e32 v214, s60, v162\n\t"
    "v_mul_f32_e32 v215, s60, v163\n\t"
    "s_waitcnt lgkmcnt(0)\n\t"
    "v_cvt_pk_bf16_f32 v216, v212, v213\n\t"
    "v_cvt_pk_bf16_f32 v217, v214, v215\n\t"
    "v_add_u32_e32 v218, v208, v224\n\t"
    "s_and_b32 s46, s58, 3\n\t"
    "s_cbranch_scc1 .Lk1t16%=\n\t"
    "global_store_dwordx2 v218, v[216:217], s[44:45]\n\t"
    ".Lk1b16%=:\n\t"
    "v_mul_f32_e32 v212, s60, v164\n\t"
    "v_mul_f32_e32 v213, s60, v165\n\t"
    "v_mul_f32_e32 v214, s60, v166\n\t"
    "v_mul_f32_e32 v215, s60, v167\n\t"
    "v_cvt_pk_bf16_f32 v216, v212, v213\n\t"
    "v_cvt_pk_bf16_f32 v217, v214, v215\n\t"
    "v_add_u32_e32 v218, v209, v224\n\t"
    "s_and_b32 s46, s58, 12\n\t"
    "s_cbranch_scc1 .Lk1t17%=\n\t"
    "global_store_dwordx2 v218, v[216:217], s[44:45]\n\t"
    ".Lk1b17%=:\n\t"
    "v_mul_f32_e32 v212, s60, v168\n\t"
    "v_mul_f32_e32 v213, s60, v169\n\t"
    "v_mul_f32_e32 v214, s60, v170\n\t"
    "v_mul_f32_e32 v215, s60, v171\n\t"
    "v_cvt_pk_bf16_f32 v216, v212, v213\n\t"
    "v_cvt_pk_bf16_f32 v217, v214, v215\n\t"
    "v_add_u32_e32 v218, v210, v224\n\t"
    "s_and_b32 s46, s58, 48\n\t"
    "s_cbranch_scc1 .Lk1t18%=\n\t"
    "global_store_dwordx2 v218, v[216:217], s[44:45]\n\t"
    ".Lk1b18%=:\n\t"
    "v_mul_f32_e32 v212, s60, v172\n\t"
    "v_mul_f32_e32 v213, s60, v173\n\t"
    "v_mul_f32_e32 v214, s60, v174\n\t"
    "v_mul_f32_e32 v215, s60, v175\n\t"
    "v_cvt_pk_bf16_f32 v216, v212, v213\n\t"
    "v_cvt_pk_bf16_f32 v217, v214, v215\n\t"
    "v_add_u32_e32 v218, v211, v224\n\t"
    "s_and_b32 s46, s58, 192\n\t"
    "s_cbranch_scc1 .Lk1t19%=\n\t"
    "global_store_dwordx2 v218, v[216:217], s[44:45]\n\t"
    ".Lk1b19%=:\n\t"
    "s_waitcnt lgkmcnt(0)\n\t"
    "s_waitcnt vmcnt(4)\n\t"
    "s_branch .Lk1e%=\n\t"
    ".Lk1qv%=:\n\t"
    "s_waitcnt lgkmcnt(4)\n\t"
    "v_mfma_f32_32x32x16_bf16 v[144:159], v[0:3], v[176:179], 0\n\t"
    "ds_read_b64_tr_b16 v[188:189], v201 offset:0\n\t"
    "ds_read_b64_tr_b16 v[190:191], v201 offset:512\n\t"
    "s_cmp_ge_u32 s62, 9\n\t"
    "s_cselect_b32 s60, 1.0, 0x3eae1a93\n\t"
    "s_movk_i32 s61, 0x2000\n\t"
    "s_cmov_b32 s61, 159744\n\t"
    "s_waitcnt lgkmcnt(4)\n\t"
    "v_mfma_f32_32x32x16_bf16 v[144:159], v[4:7], v[180:183], v[144:159]\n\t"
    "ds_read_b64_tr_b16 v[176:177], v201 offset:2048\n\t"
    "ds_read_b64_tr_b16 v[178:179], v201 offset:2560\n\t"
    "s_waitcnt lgkmcnt(4)\n\t"
    "v_mfma_f32_32x32x16_bf16 v[144:159], v[8:11], v[184:187], v[144:159]\n\t"
    "ds_read_b64_tr_b16 v[180:181], v201 offset:4096\n\t"
    "ds_read_b64_tr_b16 v[182:183], v201 offset:4608\n\t"
    "v_mul_f32_e32 v212, s60, v160\n\t"
    "v_mul_f32_e32 v213, s60, v161\n\t"
    "v_mul_f32_e32 v214, s60, v162\n\t"
    "v_mul_f32_e32 v215, s60, v163\n\t"
    "s_waitcnt lgkmcnt(4)\n\t"
    "v_mfma_f32_32x32x16_bf16 v[144:159], v[12:15], v[188:191], v[144:159]\n\t"
    "ds_read_b64_tr_b16 v[184:185], v202 offset:0\n\t"
    "ds_read_b64_tr_b16 v[186:187], v202 offset:512\n\t"
    "v_cvt_pk_bf16_f32 v216, v212, v213\n\t"
    "v_cvt_pk_bf16_f32 v217, v214, v215\n\t"
    "v_add_u32_e32 v218, v208, v224\n\t"
    "s_waitcnt lgkmcnt(4)\n\t"
    "v_mfma_f32_32x32x16_bf16 v[144:159], v[16:19], v[176:179], v[144:159]\n\t"
    "ds_read_b64_tr_b16 v[188:189], v202 offset:2048\n\t"
    "ds_read_b64_tr_b16 v[190:191], v202 offset:2560\n\t"
    "s_and_b32 s46, s58, 3\n\t"
    "s_cbranch_scc1 .Lk1t20%=\n\t"
    "global_store_dwordx2 v218, v[216:217], s[44:45]\n\t"
    ".Lk1b20%=:\n\t"
    "s_waitcnt lgkmcnt(4)\n\t"
    "v_mfma_f32_32x32x16_bf16 v[144:159], v[20:23], v[180:183], v[144:159]\n\t"
    "ds_read_b64_tr_b16 v[176:177], v202 offset:4096\n\t"
    "ds_read_b64_tr_b16 v[178:179], v202 offset:4608\n\t"
    "s_waitcnt lgkmcnt(4)\n\t"
    "v_mfma_f32_32x32x16_bf16 v[144:159], v[24:27], v[184:187], v[144:159]\n\t"
    "ds_read_b64_tr_b16 v[180:181], v203 offset:0\n\t"
    "ds_read_b64_tr_b16 v[182:183], v203 offset:512\n\t"
    "s_waitcnt vmcnt(14)\n\t"
    "s_barrier\n\t"
    "s_waitcnt lgkmcnt(4)\n\t"
    "v_mfma_f32_32x32x16_bf16 v[144:159], v[28:31], v[188:191], v[144:159]\n\t"
    "ds_read_b64_tr_b16 v[184:185], v203 offset:2048\n\t"
    "ds_read_b64_tr_b16 v[186:187], v203 offset:2560\n\t"
    "s_add_u32 m0, s51, 49152\n\t"
    "s_add_u32 s46, s40, 128\n\t"
    "s_addc_u32 s47, s41, 0\n\t"
    "global_load_lds_dwordx4 v251, s[46:47]\n\t"
    "s_waitcnt lgkmcnt(4)\n\t"
    "v_mfma_f32_32x32x16_bf16 v[144:159], v[32:35], v[176:179], v[144:159]\n\t"
    "ds_read_b64_tr_b16 v[188:189], v203 offset:4096\n\t"
    "ds_read_b64_tr_b16 v[190:191], v203 offset:4608\n\t"
    "s_waitcnt lgkmcnt(4)\n\t"
    "v_mfma_f32_32x32x16_bf16 v[144:159], v[36:39], v[180:183], v[144:159]\n\t"
    "ds_read_b64_tr_b16 v[176:177], v200 offset:24576\n\t"
    "ds_read_b64_tr_b16 v[178:179], v200 offset:25088\n\t"
    "s_add_u32 m0, s51, 57344\n\t"
    "s_add_u32 s46, s40, 221312\n\t"
    "s_addc_u32 s47, s41, 0\n\t"
    "global_load_lds_dwordx4 v251, s[46:47]\n\t"
    "s_waitcnt lgkmcnt(4)\n\t"
    "v_mfma_f32_32x32x16_bf16 v[144:159], v[40:43], v[184:187], v[144:159]\n\t"
    "ds_read_b64_tr_b16 v[180:181], v200 offset:26624\n\t"
    "ds_read_b64_tr_b16 v[182:183], v200 offset:27136\n\t"
    "v_mul_f32_e32 v212, s60, v164\n\t"
    "v_mul_f32_e32 v213, s60, v165\n\t"
    "v_mul_f32_e32 v214, s60, v166\n\t"
    "v_mul_f32_e32 v215, s60, v167\n\t"
    "s_waitcnt lgkmcnt(4)\n\t"
    "v_mfma_f32_32x32x16_bf16 v[144:159], v[44:47], v[188:191], v[144:159]\n\t"
    "ds_read_b64_tr_b16 v[184:185], v200 offset:28672\n\t"
    "ds_read_b64_tr_b16 v[186:187], v200 offset:29184\n\t"
    "s_add_u32 m0, s51, 65536\n\t"
    "s_add_u32 s46, s40, 442496\n\t"
    "s_addc_u32 s47, s41, 0\n\t"
    "global_load_lds_dwordx4 v251, s[46:47]\n\t"
    "v_cvt_pk_bf16_f32 v216, v212, v213\n\t"
    "v_cvt_pk_bf16_f32 v217, v214, v215\n\t"
    "v_add_u32_e32 v218, v209, v224\n\t"
    "s_waitcnt lgkmcnt(4)\n\t"
    "v_mfma_f32_32x32x16_bf16 v[144:159], v[48:51], v[176:179], v[144:159]\n\t"
    "ds_read_b64_tr_b16 v[188:189], v201 offset:24576\n\t"
    "ds_read_b64_tr_b16 v[190:191], v201 offset:25088\n\t"
    "s_and_b32 s46, s58, 12\n\t"
    "s_cbranch_scc1 .Lk1t21%=\n\t"
    "global_store_dwordx2 v218, v[216:217], s[44:45]\n\t"
    ".Lk1b21%=:\n\t"
    "s_waitcnt lgkmcnt(4)\n\t"
    "v_mfma_f32_32x32x16_bf16 v[144:159], v[52:55], v[180:183], v[144:159]\n\t"
    "ds_read_b64_tr_b16 v[176:177], v201 offset:26624\n\t"
    "ds_read_b64_tr_b16 v[178:179], v201 offset:27136\n\t"
    "s_waitcnt lgkmcnt(4)\n\t"
    "v_mfma_f32_32x32x16_bf16 v[144:159], v[56:59], v[184:187], v[144:159]\n\t"
    "ds_read_b64_tr_b16 v[180:181], v201 offset:28672\n\t"
    "ds_read_b64_tr_b16 v[182:183], v201 offset:29184\n\t"
    "s_waitcnt lgkmcnt(4)\n\t"
    "v_mfma_f32_32x32x16_bf16 v[144:159], v[60:63], v[188:191], v[144:159]\n\t"
    "ds_read_b64_tr_b16 v[184:185], v202 offset:24576\n\t"
    "ds_read_b64_tr_b16 v[186:187], v202 offset:25088\n\t"
    "s_waitcnt lgkmcnt(4)\n\t"
    "v_mfma_f32_32x32x16_bf16 v[144:159], v[64:67], v[176:179], v[144:159]\n\t"
    "ds_read_b64_tr_b16 v[188:189], v202 offset:26624\n\t"
    "ds_read_b64_tr_b16 v[190:191], v202 offset:27136\n\t"
    "s_waitcnt lgkmcnt(4)\n\t"
    "v_mfma_f32_32x32x16_bf16 v[144:159], v[68:71], v[180:183], v[144:159]\n\t"
    "ds_read_b64_tr_b16 v[176:177], v202 offset:28672\n\t"
    "ds_read_b64_tr_b16 v[178:179], v202 offset:29184\n\t"
    "s_waitcnt lgkmcnt(4)\n\t"
    "v_mfma_f32_32x32x16_bf16 v[144:159], v[72:75], v[184:187], v[144:159]\n\t"
    "ds_read_b64_tr_b16 v[180:181], v203 offset:24576\n\t"
    "ds_read_b64_tr_b16 v[182:183], v203 offset:25088\n\t"
    "s_waitcnt vmcnt(14)\n\t"
    "s_barrier\n\t"
    "v_mul_f32_e32 v212, s60, v168\n\t"
    "v_mul_f32_e32 v213, s60, v169\n\t"
    "v_mul_f32_e32 v214, s60, v170\n\t"
    "v_mul_f32_e32 v215, s60, v171\n\t"
    "s_waitcnt lgkmcnt(4)\n\t"
    "v_mfma_f32_32x32x16_bf16 v[144:159], v[76:79], v[188:191], v[144:159]\n\t"
    "ds_read_b64_tr_b16 v[184:185], v203 offset:26624\n\t"
    "ds_read_b64_tr_b16 v[186:187], v203 offset:27136\n\t"
    "s_add_u32 m0, s50, 0\n\t"
    "s_add_u32 s46, s40, 256\n\t"
    "s_addc_u32 s47, s41, 0\n\t"
    "global_load_lds_dwordx4 v249, s[46:47]\n\t"
    "v_cvt_pk_bf16_f32 v216, v212, v213\n\t"
    "v_cvt_pk_bf16_f32 v217, v214, v215\n\t"
    "v_add_u32_e32 v218, v210, v224\n\t"
    "s_waitcnt lgkmcnt(4)\n\t"
    "v_mfma_f32_32x32x16_bf16 v[144:159], v[80:83], v[176:179], v[144:159]\n\t"
    "ds_read_b64_tr_b16 v[188:189], v203 offset:28672\n\t"
    "ds_read_b64_tr_b16 v[190:191], v203 offset:29184\n\t"
    "s_and_b32 s46, s58, 48\n\t"
    "s_cbranch_scc1 .Lk1t22%=\n\t"
    "global_store_dwordx2 v218, v[216:217], s[44:45]\n\t"
    ".Lk1b22%=:\n\t"
    "s_waitcnt lgkmcnt(4)\n\t"
    "v_mfma_f32_32x32x16_bf16 v[144:159], v[84:87], v[180:183], v[144:159]\n\t"
    "ds_read_b64_tr_b16 v[176:177], v200 offset:49152\n\t"
    "ds_read_b64_tr_b16 v[178:179], v200 offset:49664\n\t"
    "s_add_u32 m0, s50, 8192\n\t"
    "s_add_u32 s46, s40, 221440\n\t"
    "s_addc_u32 s47, s41, 0\n\t"
    "global_load_lds_dwordx4 v249, s[46:47]\n\t"
    "s_waitcnt lgkmcnt(4)\n\t"
    "v_mfma_f32_32x32x16_bf16 v[144:159], v[88:91], v[184:187], v[144:159]\n\t"
    "ds_read_b64_tr_b16 v[180:181], v200 offset:51200\n\t"
    "ds_read_b64_tr_b16 v[182:183], v200 offset:51712\n\t"
    "s_waitcnt lgkmcnt(4)\n\t"
    "v_mfma_f32_32x32x16_bf16 v[144:159], v[92:95], v[188:191], v[144:159]\n\t"
    "ds_read_b64_tr_b16 v[184:185], v200 offset:53248\n\t"
    "ds_read_b64_tr_b16 v[186:187], v200 offset:53760\n\t"
    "s_add_u32 m0, s50, 16384\n\t"
    "s_add_u32 s46, s40, 442624\n\t"
    "s_addc_u32 s47, s41, 0\n\t"
    "global_load_lds_dwordx4 v249, s[46:47]\n\t"
    "s_waitcnt lgkmcnt(4)\n\t"
    "v_mfma_f32_32x32x16_bf16 v[144:159], v[96:99], v[176:179], v[144:159]\n\t"
    "ds_read_b64_tr_b16 v[188:189], v201 offset:49152\n\t"
    "ds_read_b64_tr_b16 v[190:191], v201 offset:49664\n\t"
    "v_add_u32_e32 v200, s52, v200\n\t"
    "s_waitcnt lgkmcnt(4)\n\t"
    "v_mfma_f32_32x32x16_bf16 v[144:159], v[100:103], v[180:183], v[144:159]\n\t"
    "ds_read_b64_tr_b16 v[176:177], v201 offset:51200\n\t"
    "ds_read_b64_tr_b16 v[178:179], v201 offset:51712\n\t"
    "s_waitcnt lgkmcnt(4)\n\t"
    "v_mfma_f32_32x32x16_bf16 v[144:159], v[104:107], v[184:187], v[144:159]\n\t"
    "ds_read_b64_tr_b16 v[180:181], v201 offset:53248\n\t"
    "ds_read_b64_tr_b16 v[182:183], v201 offset:53760\n\t"
    "v_mul_f32_e32 v212, s60, v172\n\t"
    "v_mul_f32_e32 v213, s60, v173\n\t"
    "v_mul_f32_e32 v214, s60, v174\n\t"
    "v_mul_f32_e32 v215, s60, v175\n\t"
    "s_waitcnt lgkmcnt(4)\n\t"
    "v_mfma_f32_32x32x16_bf16 v[144:159], v[108:111], v[188:191], v[144:159]\n\t"
    "ds_read_b64_tr_b16 v[184:185], v202 offset:49152\n\t"
    "ds_read_b64_tr_b16 v[186:187], v202 offset:49664\n\t"
    "v_add_u32_e32 v201, s52, v201\n\t"
    "v_cvt_pk_bf16_f32 v216, v212, v213\n\t"
    "v_cvt_pk_bf16_f32 v217, v214, v215\n\t"
    "v_add_u32_e32 v218, v211, v224\n\t"
    "s_waitcnt lgkmcnt(4)\n\t"
    "v_mfma_f32_32x32x16_bf16 v[144:159], v[112:115], v[176:179], v[144:159]\n\t"
    "ds_read_b64_tr_b16 v[188:189], v202 offset:51200\n\t"
    "ds_read_b64_tr_b16 v[190:191], v202 offset:51712\n\t"
    "s_and_b32 s46, s58, 192\n\t"
    "s_cbranch_scc1 .Lk1t23%=\n\t"
    "global_store_dwordx2 v218, v[216:217], s[44:45]\n\t"
    ".Lk1b23%=:\n\t"
    "s_waitcnt lgkmcnt(4)\n\t"
    "v_mfma_f32_32x32x16_bf16 v[144:159], v[116:119], v[180:183], v[144:159]\n\t"
    "ds_read_b64_tr_b16 v[176:177], v202 offset:53248\n\t"
    "ds_read_b64_tr_b16 v[178:179], v202 offset:53760\n\t"
    "s_waitcnt lgkmcnt(4)\n\t"
    "v_mfma_f32_32x32x16_bf16 v[144:159], v[120:123], v[184:187], v[144:159]\n\t"
    "ds_read_b64_tr_b16 v[180:181], v203 offset:49152\n\t"
    "ds_read_b64_tr_b16 v[182:183], v203 offset:49664\n\t"
    "s_waitcnt vmcnt(14)\n\t"
    "s_barrier\n\t"
    "v_add_u32_e32 v202, s52, v202\n\t"
    "v_add_u32_e32 v226, 32, v226\n\t"
    "s_waitcnt lgkmcnt(4)\n\t"
    "v_mfma_f32_32x32x16_bf16 v[144:159], v[124:127], v[188:191], v[144:159]\n\t"
    "ds_read_b64_tr_b16 v[184:185], v203 offset:51200\n\t"
    "ds_read_b64_tr_b16 v[186:187], v203 offset:51712\n\t"
    "s_add_u32 m0, s50, 24576\n\t"
    "s_add_u32 s46, s40, 256\n\t"
    "s_addc_u32 s47, s41, 0\n\t"
    "global_load_lds_dwordx4 v250, s[46:47]\n\t"
    "s_waitcnt lgkmcnt(4)\n\t"
    "v_mfma_f32_32x32x16_bf16 v[144:159], v[128:131], v[176:179], v[144:159]\n\t"
    "ds_read_b64_tr_b16 v[188:189], v203 offset:53248\n\t"
    "ds_read_b64_tr_b16 v[190:191], v203 offset:53760\n\t"
    "ds_read_b128 v[208:211], v226\n\t"
    "s_waitcnt lgkmcnt(5)\n\t"
    "v_mfma_f32_32x32x16_bf16 v[144:159], v[132:135], v[180:183], v[144:159]\n\t"
    "ds_read_b64_tr_b16 v[176:177], v200 offset:0\n\t"
    "ds_read_b64_tr_b16 v[178:179], v200 offset:512\n\t"
    "s_add_u32 m0, s50, 32768\n\t"
    "s_add_u32 s46, s40, 221440\n\t"
    "s_addc_u32 s47, s41, 0\n\t"
    "global_load_lds_dwordx4 v250, s[46:47]\n\t"
    "v_add_u32_e32 v203, s52, v203\n\t"
    "s_waitcnt lgkmcnt(5)\n\t"
    "v_mfma_f32_32x32x16_bf16 v[144:159], v[136:139], v[184:187], v[144:159]\n\t"
    "ds_read_b64_tr_b16 v[180:181], v200 offset:2048\n\t"
    "ds_read_b64_tr_b16 v[182:183], v200 offset:2560\n\t"
    "s_waitcnt lgkmcnt(5)\n\t"
    "v_mfma_f32_32x32x16_bf16 v[160:175], v[140:143], v[188:191], v[144:159]\n\t"
    "ds_read_b64_tr_b16 v[184:185], v200 offset:4096\n\t"
    "ds_read_b64_tr_b16 v[186:187], v200 offset:4608\n\t"
    "s_add_u32 m0, s50, 40960\n\t"
    "s_add_u32 s46, s40, 442624\n\t"
    "s_addc_u32 s47, s41, 0\n\t"
    "global_load_lds_dwordx4 v250, s[46:47]\n\t"
    "s_sub_u32 s52, 0, s52\n\t"
    "s_mov_b32 s46, s50\n\t"
    "s_mov_b32 s50, s51\n\t"
    "s_mov_b32 s51, s46\n\t"
    "s_add_u32 s40, s40, 128\n\t"
    "s_addc_u32 s41, s41, 0\n\t"
    "s_lshr_b64 s[58:59], s[58:59], 8\n\t"
    "s_add_u32 s62, s62, 1\n\t"
    "s_add_u32 s63, s63, 64\n\t"
    "s_branch .Lk1vt%=\n\t"
    ".Lk1v0%=:\n\t"
    "s_waitcnt lgkmcnt(0)\n\t"
    "s_barrier\n\t"
    "ds_read_b128 v[0:3], v160\n\t"
    "ds_read_b64_tr_b16 v[176:177], v172 offset:0\n\t"
    "ds_read_b64_tr_b16 v[178:179], v172 offset:512\n\t"
    "ds_read_b128 v[4:7], v161\n\t"
    "ds_read_b64_tr_b16 v[180:181], v172 offset:2048\n\t"
    "ds_read_b64_tr_b16 v[182:183], v172 offset:2560\n\t"
    "ds_read_b128 v[8:11], v162\n\t"
    "ds_read_b64_tr_b16 v[184:185], v172 offset:4096\n\t"
    "ds_read_b64_tr_b16 v[186:187], v172 offset:4608\n\t"
    "s_waitcnt lgkmcnt(6)\n\t"
    "v_mfma_f32_32x32x16_bf16 v[144:159], v[0:3], v[176:179], 0\n\t"
    "ds_read_b64_tr_b16 v[188:189], v173 offset:0\n\t"
    "ds_read_b64_tr_b16 v[190:191], v173 offset:512\n\t"
    "ds_read_b128 v[12:15], v163\n\t"
    "ds_read_b128 v[16:19], v164\n\t"
    "ds_read_b128 v[20:23], v165\n\t"
    "s_add_u32 m0, s48, 106496\n\t"
    "s_add_u32 s46, s42, 256\n\t"
    "s_addc_u32 s47, s43, 0\n\t"
    "global_load_lds_dwordx4 v205, s[46:47]\n\t"
    "ds_read_b128 v[240:243], v248 offset:0\n\t"
    "s_waitcnt lgkmcnt(9)\n\t"
    "v_mfma_f32_32x32x16_bf16 v[144:159], v[4:7], v[180:183], v[144:159]\n\t"
    "ds_read_b64_tr_b16 v[176:177], v173 offset:2048\n\t"
    "ds_read_b64_tr_b16 v[178:179], v173 offset:2560\n\t"
    "ds_read_b128 v[24:27], v166\n\t"
    "ds_read_b128 v[28:31], v167\n\t"
    "ds_read_b128 v[32:35], v168\n\t"
    "s_add_u32 m0, s48, 107520\n\t"
    "s_add_u32 s46, s42, 256\n\t"
    "s_addc_u32 s47, s43, 0\n\t"
    "global_load_lds_dwordx4 v206, s[46:47]\n\t"
    "ds_read_b128 v[244:247], v248 offset:32\n\t"
    "s_waitcnt lgkmcnt(6)\n\t"
    "v_mfma_f32_4x4x4_16b_bf16 v[232:235], v[240:241], v[0:1], 0\n\t"
    "v_mfma_f32_4x4x4_16b_bf16 v[236:239], v[242:243], v[2:3], 0\n\t"
    "v_mfma_f32_32x32x16_bf16 v[144:159], v[8:11], v[184:187], v[144:159]\n\t"
    "ds_read_b64_tr_b16 v[180:181], v173 offset:4096\n\t"
    "ds_read_b64_tr_b16 v[182:183], v173 offset:4608\n\t"
    "ds_read_b128 v[36:39], v169\n\t"
    "ds_read_b128 v[40:43], v170\n\t"
    "ds_read_b128 v[44:47], v171\n\t"
    "s_add_u32 m0, s50, 49152\n\t"
    "s_add_u32 s46, s40, 0\n\t"
    "s_addc_u32 s47, s41, 0\n\t"
    "global_load_lds_dwordx4 v250, s[46:47]\n\t"
    "ds_read_b128 v[240:243], v248 offset:64\n\t"
    "s_waitcnt lgkmcnt(6)\n\t"
    "v_mfma_f32_4x4x4_16b_bf16 v[232:235], v[244:245], v[4:5], v[232:235]\n\t"
    "v_mfma_f32_4x4x4_16b_bf16 v[236:239], v[246:247], v[6:7], v[236:239]\n\t"
    "v_mfma_f32_32x32x16_bf16 v[144:159], v[12:15], v[188:191], v[144:159]\n\t"
    "ds_read_b64_tr_b16 v[184:185], v174 offset:0\n\t"
    "ds_read_b64_tr_b16 v[186:187], v174 offset:512\n\t"
    "s_add_u32 m0, s50, 57344\n\t"
    "s_add_u32 s46, s40, 221184\n\t"
    "s_addc_u32 s47, s41, 0\n\t"
    "global_load_lds_dwordx4 v250, s[46:47]\n\t"
    "ds_read_b128 v[244:247], v248 offset:96\n\t"
    "s_waitcnt lgkmcnt(3)\n\t"
    "v_mfma_f32_4x4x4_16b_bf16 v[232:235], v[240:241], v[8:9], v[232:235]\n\t"
    "v_mfma_f32_4x4x4_16b_bf16 v[236:239], v[242:243], v[10:11], v[236:239]\n\t"
    "v_mfma_f32_32x32x16_bf16 v[144:159], v[16:19], v[176:179], v[144:159]\n\t"
    "ds_read_b64_tr_b16 v[188:189], v174 offset:2048\n\t"
    "ds_read_b64_tr_b16 v[190:191], v174 offset:2560\n\t"
    "s_add_u32 m0, s50, 65536\n\t"
    "s_add_u32 s46, s40, 442368\n\t"
    "s_addc_u32 s47, s41, 0\n\t"
    "global_load_lds_dwordx4 v250, s[46:47]\n\t"
    "ds_read_b128 v[240:243], v248 offset:128\n\t"
    "s_waitcnt lgkmcnt(3)\n\t"
    "v_mfma_f32_4x4x4_16b_bf16 v[232:235], v[244:245], v[12:13], v[232:235]\n\t"
    "v_mfma_f32_4x4x4_16b_bf16 v[236:239], v[246:247], v[14:15], v[236:239]\n\t"
    "v_mfma_f32_32x32x16_bf16 v[144:159], v[20:23], v[180:183], v[144:159]\n\t"
    "ds_read_b64_tr_b16 v[176:177], v174 offset:4096\n\t"
    "ds_read_b64_tr_b16 v[178:179], v174 offset:4608\n\t"
    "s_barrier\n\t"
    "ds_read_b128 v[244:247], v248 offset:160\n\t"
    "s_waitcnt lgkmcnt(3)\n\t"
    "v_mfma_f32_4x4x4_16b_bf16 v[232:235], v[240:241], v[16:17], v[232:235]\n\t"
    "v_mfma_f32_4x4x4_16b_bf16 v[236:239], v[242:243], v[18:19], v[236:239]\n\t"
    "v_mfma_f32_32x32x16_bf16 v[144:159], v[24:27], v[184:187], v[144:159]\n\t"
    "ds_read_b64_tr_b16 v[180:181], v175 offset:0\n\t"
    "ds_read_b64_tr_b16 v[182:183], v175 offset:512\n\t"
    "s_add_u32 m0, s48, 0\n\t"
    "s_add_u32 s46, s36, 0\n\t"
    "s_addc_u32 s47, s37, 0\n\t"
    "global_load_lds_dwordx4 v205, s[46:47]\n\t"
    "s_waitcnt lgkmcnt(2)\n\t"
    "v_mfma_f32_4x4x4_16b_bf16 v[232:235], v[244:245], v[20:21], v[232:235]\n\t"
    "v_mfma_f32_4x4x4_16b_bf16 v[236:239], v[246:247], v[22:23], v[236:239]\n\t"
    "v_mfma_f32_32x32x16_bf16 v[144:159], v[28:31], v[188:191], v[144:159]\n\t"
    "ds_read_b64_tr_b16 v[184:185], v175 offset:2048\n\t"
    "ds_read_b64_tr_b16 v[186:187], v175 offset:2560\n\t"
    "s_add_u32 m0, s48, 1024\n\t"
    "s_add_u32 s46, s36, 0\n\t"
    "s_addc_u32 s47, s37, 0\n\t"
    "global_load_lds_dwordx4 v206, s[46:47]\n\t"
    "v_mfma_f32_32x32x16_bf16 v[144:159], v[32:35], v[176:179], v[144:159]\n\t"
    "ds_read_b64_tr_b16 v[188:189], v175 offset:4096\n\t"
    "ds_read_b64_tr_b16 v[190:191], v175 offset:4608\n\t"
    "s_add_u32 m0, s48, 16384\n\t"
    "s_add_u32 s46, s36, 128\n\t"
    "s_addc_u32 s47, s37, 0\n\t"
    "global_load_lds_dwordx4 v205, s[46:47]\n\t"
    "v_subrev_u32_e32 v160, s52, v160\n\t"
    "v_subrev_u32_e32 v161, s52, v161\n\t"
    "v_subrev_u32_e32 v162, s52, v162\n\t"
    "s_waitcnt lgkmcnt(4)\n\t"
    "v_mfma_f32_32x32x16_bf16 v[144:159], v[36:39], v[180:183], v[144:159]\n\t"
    "s_add_u32 m0, s48, 17408\n\t"
    "s_add_u32 s46, s36, 128\n\t"
    "s_addc_u32 s47, s37, 0\n\t"
    "global_load_lds_dwordx4 v206, s[46:47]\n\t"
    "v_subrev_u32_e32 v163, s52, v163\n\t"
    "v_subrev_u32_e32 v164, s52, v164\n\t"
    "v_subrev_u32_e32 v165, s52, v165\n\t"
    "s_waitcnt lgkmcnt(2)\n\t"
    "v_mfma_f32_32x32x16_bf16 v[144:159], v[40:43], v[184:187], v[144:159]\n\t"
    "v_subrev_u32_e32 v166, s52, v166\n\t"
    "v_subrev_u32_e32 v167, s52, v167\n\t"
    "v_subrev_u32_e32 v168, s52, v168\n\t"
    "s_waitcnt lgkmcnt(0)\n\t"
    "v_mfma_f32_32x32x16_bf16 v[144:159], v[44:47], v[188:191], v[144:159]\n\t"
    "v_subrev_u32_e32 v169, s52, v169\n\t"
    "v_subrev_u32_e32 v170, s52, v170\n\t"
    "v_subrev_u32_e32 v171, s52, v171\n\t"
    "s_waitcnt vmcnt(4)\n\t"
    "s_barrier\n\t"
    "ds_read_b128 v[48:51], v160\n\t"
    "ds_read_b64_tr_b16 v[176:177], v200 offset:49152\n\t"
    "ds_read_b64_tr_b16 v[178:179], v200 offset:49664\n\t"
    "ds_read_b128 v[52:55], v161\n\t"
    "ds_read_b64_tr_b16 v[180:181], v200 offset:51200\n\t"
    "ds_read_b64_tr_b16 v[182:183], v200 offset:51712\n\t"
    "ds_read_b128 v[56:59], v162\n\t"
    "ds_read_b64_tr_b16 v[184:185], v200 offset:53248\n\t"
    "ds_read_b64_tr_b16 v[186:187], v200 offset:53760\n\t"
    "s_waitcnt lgkmcnt(6)\n\t"
    "v_mfma_f32_32x32x16_bf16 v[144:159], v[48:51], v[176:179], v[144:159]\n\t"
    "ds_read_b64_tr_b16 v[188:189], v201 offset:49152\n\t"
    "ds_read_b64_tr_b16 v[190:191], v201 offset:49664\n\t"
    "ds_read_b128 v[60:63], v163\n\t"
    "ds_read_b128 v[64:67], v164\n\t"
    "ds_read_b128 v[68:71], v165\n\t"
    "s_add_u32 m0, s48, 32768\n\t"
    "s_add_u32 s46, s36, 256\n\t"
    "s_addc_u32 s47, s37, 0\n\t"
    "global_load_lds_dwordx4 v205, s[46:47]\n\t"
    "ds_read_b128 v[240:243], v248 offset:384\n\t"
    "s_waitcnt lgkmcnt(9)\n\t"
    "v_mfma_f32_32x32x16_bf16 v[144:159], v[52:55], v[180:183], v[144:159]\n\t"
    "ds_read_b64_tr_b16 v[176:177], v201 offset:51200\n\t"
    "ds_read_b64_tr_b16 v[178:179], v201 offset:51712\n\t"
    "ds_read_b128 v[72:75], v166\n\t"
    "ds_read_b128 v[76:79], v167\n\t"
    "ds_read_b128 v[80:83], v168\n\t"
    "s_add_u32 m0, s48, 33792\n\t"
    "s_add_u32 s46, s36, 256\n\t"
    "s_addc_u32 s47, s37, 0\n\t"
    "global_load_lds_dwordx4 v206, s[46:47]\n\t"
    "ds_read_b128 v[244:247], v248 offset:416\n\t"
    "s_waitcnt lgkmcnt(6)\n\t"
    "v_mfma_f32_4x4x4_16b_bf16 v[232:235], v[240:241], v[48:49], v[232:235]\n\t"
    "v_mfma_f32_4x4x4_16b_bf16 v[236:239], v[242:243], v[50:51], v[236:239]\n\t"
    "v_mfma_f32_32x32x16_bf16 v[144:159], v[56:59], v[184:187], v[144:159]\n\t"
    "ds_read_b64_tr_b16 v[180:181], v201 offset:53248\n\t"
    "ds_read_b64_tr_b16 v[182:183], v201 offset:53760\n\t"
    "ds_read_b128 v[84:87], v169\n\t"
    "ds_read_b128 v[88:91], v170\n\t"
    "ds_read_b128 v[92:95], v171\n\t"
    "s_add_u32 m0, s51, 49152\n\t"
    "s_add_u32 s46, s40, 0\n\t"
    "s_addc_u32 s47, s41, 0\n\t"
    "global_load_lds_dwordx4 v251, s[46:47]\n\t"
    "ds_read_b128 v[240:243], v248 offset:448\n\t"
    "s_waitcnt lgkmcnt(6)\n\t"
    "v_mfma_f32_4x4x4_16b_bf16 v[232:235], v[244:245], v[52:53], v[232:235]\n\t"
    "v_mfma_f32_4x4x4_16b_bf16 v[236:239], v[246:247], v[54:55], v[236:239]\n\t"
    "v_mfma_f32_32x32x16_bf16 v[144:159], v[60:63], v[188:191], v[144:159]\n\t"
    "ds_read_b64_tr_b16 v[184:185], v202 offset:49152\n\t"
    "ds_read_b64_tr_b16 v[186:187], v202 offset:49664\n\t"
    "s_add_u32 m0, s51, 57344\n\t"
    "s_add_u32 s46, s40, 221184\n\t"
    "s_addc_u32 s47, s41, 0\n\t"
    "global_load_lds_dwordx4 v251, s[46:47]\n\t"
    "ds_read_b128 v[244:247], v248 offset:480\n\t"
    "s_waitcnt lgkmcnt(3)\n\t"
    "v_mfma_f32_4x4x4_16b_bf16 v[232:235], v[240:241], v[56:57], v[232:235]\n\t"
    "v_mfma_f32_4x4x4_16b_bf16 v[236:239], v[242:243], v[58:59], v[236:239]\n\t"
    "v_mfma_f32_32x32x16_bf16 v[144:159], v[64:67], v[176:179], v[144:159]\n\t"
    "ds_read_b64_tr_b16 v[188:189], v202 offset:51200\n\t"
    "ds_read_b64_tr_b16 v[190:191], v202 offset:51712\n\t"
    "s_add_u32 m0, s51, 65536\n\t"
    "s_add_u32 s46, s40, 442368\n\t"
    "s_addc_u32 s47, s41, 0\n\t"
    "global_load_lds_dwordx4 v251, s[46:47]\n\t"
    "ds_read_b128 v[240:243], v248 offset:512\n\t"
    "s_waitcnt lgkmcnt(3)\n\t"
    "v_mfma_f32_4x4x4_16b_bf16 v[232:235], v[244:245], v[60:61], v[232:235]\n\t"
    "v_mfma_f32_4x4x4_16b_bf16 v[236:239], v[246:247], v[62:63], v[236:239]\n\t"
    "v_mfma_f32_32x32x16_bf16 v[144:159], v[68:71], v[180:183], v[144:159]\n\t"
    "ds_read_b64_tr_b16 v[176:177], v202 offset:53248\n\t"
    "ds_read_b64_tr_b16 v[178:179], v202 offset:53760\n\t"
    "s_barrier\n\t"
    "ds_read_b128 v[244:247], v248 offset:544\n\t"
    "s_waitcnt lgkmcnt(3)\n\t"
    "v_mfma_f32_4x4x4_16b_bf16 v[232:235], v[240:241], v[64:65], v[232:235]\n\t"
    "v_mfma_f32_4x4x4_16b_bf16 v[236:239], v[242:243], v[66:67], v[236:239]\n\t"
    "v_mfma_f32_32x32x16_bf16 v[144:159], v[72:75], v[184:187], v[144:159]\n\t"
    "ds_read_b64_tr_b16 v[180:181], v203 offset:49152\n\t"
    "ds_read_b64_tr_b16 v[182:183], v203 offset:49664\n\t"
    "s_add_u32 m0, s50, 0\n\t"
    "s_add_u32 s46, s40, 128\n\t"
    "s_addc_u32 s47, s41, 0\n\t"
    "global_load_lds_dwordx4 v249, s[46:47]\n\t"
    "s_waitcnt lgkmcnt(2)\n\t"
    "v_mfma_f32_4x4x4_16b_bf16 v[232:235], v[244:245], v[68:69], v[232:235]\n\t"
    "v_mfma_f32_4x4x4_16b_bf16 v[236:239], v[246:247], v[70:71], v[236:239]\n\t"
    "v_mfma_f32_32x32x16_bf16 v[144:159], v[76:79], v[188:191], v[144:159]\n\t"
    "ds_read_b64_tr_b16 v[184:185], v203 offset:51200\n\t"
    "ds_read_b64_tr_b16 v[186:187], v203 offset:51712\n\t"
    "s_add_u32 m0, s50, 8192\n\t"
    "s_add_u32 s46, s40, 221312\n\t"
    "s_addc_u32 s47, s41, 0\n\t"
    "global_load_lds_dwordx4 v249, s[46:47]\n\t"
    "v_mfma_f32_32x32x16_bf16 v[144:159], v[80:83], v[176:179], v[144:159]\n\t"
    "ds_read_b64_tr_b16 v[188:189], v203 offset:53248\n\t"
    "ds_read_b64_tr_b16 v[190:191], v203 offset:53760\n\t"
    "s_add_u32 m0, s50, 16384\n\t"
    "s_add_u32 s46, s40, 442496\n\t"
    "s_addc_u32 s47, s41, 0\n\t"
    "global_load_lds_dwordx4 v249, s[46:47]\n\t"
    "v_add_u32_e32 v160, s52, v160\n\t"
    "v_add_u32_e32 v161, s52, v161\n\t"
    "v_add_u32_e32 v162, s52, v162\n\t"
    "s_waitcnt lgkmcnt(4)\n\t"
    "v_mfma_f32_32x32x16_bf16 v[144:159], v[84:87], v[180:183], v[144:159]\n\t"
    "s_add_u32 m0, s50, 24576\n\t"
    "s_add_u32 s46, s40, 128\n\t"
    "s_addc_u32 s47, s41, 0\n\t"
    "global_load_lds_dwordx4 v250, s[46:47]\n\t"
    "v_add_u32_e32 v163, s52, v163\n\t"
    "v_add_u32_e32 v164, s52, v164\n\t"
    "v_add_u32_e32 v165, s52, v165\n\t"
    "s_waitcnt lgkmcnt(2)\n\t"
    "v_mfma_f32_32x32x16_bf16 v[144:159], v[88:91], v[184:187], v[144:159]\n\t"
    "v_add_u32_e32 v166, s52, v166\n\t"
    "v_add_u32_e32 v167, s52, v167\n\t"
    "v_add_u32_e32 v168, s52, v168\n\t"
    "s_waitcnt lgkmcnt(0)\n\t"
    "v_mfma_f32_32x32x16_bf16 v[144:159], v[92:95], v[188:191], v[144:159]\n\t"
    "v_add_u32_e32 v169, s52, v169\n\t"
    "v_add_u32_e32 v170, s52, v170\n\t"
    "v_add_u32_e32 v171, s52, v171\n\t"
    "s_waitcnt vmcnt(4)\n\t"
    "s_barrier\n\t"
    "ds_read_b128 v[96:99], v160\n\t"
    "ds_read_b64_tr_b16 v[176:177], v172 offset:0\n\t"
    "ds_read_b64_tr_b16 v[178:179], v172 offset:512\n\t"
    "ds_read_b128 v[100:103], v161\n\t"
    "ds_read_b64_tr_b16 v[180:181], v172 offset:2048\n\t"
    "ds_read_b64_tr_b16 v[182:183], v172 offset:2560\n\t"
    "ds_read_b128 v[104:107], v162\n\t"
    "ds_read_b64_tr_b16 v[184:185], v172 offset:4096\n\t"
    "ds_read_b64_tr_b16 v[186:187], v172 offset:4608\n\t"
    "s_waitcnt lgkmcnt(6)\n\t"
    "v_mfma_f32_32x32x16_bf16 v[144:159], v[96:99], v[176:179], v[144:159]\n\t"
    "ds_read_b64_tr_b16 v[188:189], v173 offset:0\n\t"
    "ds_read_b64_tr_b16 v[190:191], v173 offset:512\n\t"
    "ds_read_b128 v[108:111], v163\n\t"
    "ds_read_b128 v[112:115], v164\n\t"
    "ds_read_b128 v[116:119], v165\n\t"
    "s_add_u32 m0, s50, 32768\n\t"
    "s_add_u32 s46, s40, 221312\n\t"
    "s_addc_u32 s47, s41, 0\n\t"
    "global_load_lds_dwordx4 v250, s[46:47]\n\t"
    "ds_read_b128 v[240:243], v248 offset:768\n\t"
    "s_waitcnt lgkmcnt(9)\n\t"
    "v_mfma_f32_32x32x16_bf16 v[144:159], v[100:103], v[180:183], v[144:159]\n\t"
    "ds_read_b64_tr_b16 v[176:177], v173 offset:2048\n\t"
    "ds_read_b64_tr_b16 v[178:179], v173 offset:2560\n\t"
    "ds_read_b128 v[120:123], v166\n\t"
    "ds_read_b128 v[124:127], v167\n\t"
    "ds_read_b128 v[128:131], v168\n\t"
    "s_add_u32 m0, s50, 40960\n\t"
    "s_add_u32 s46, s40, 442496\n\t"
    "s_addc_u32 s47, s41, 0\n\t"
    "global_load_lds_dwordx4 v250, s[46:47]\n\t"
    "ds_read_b128 v[244:247], v248 offset:800\n\t"
    "s_waitcnt lgkmcnt(6)\n\t"
    "v_mfma_f32_4x4x4_16b_bf16 v[232:235], v[240:241], v[96:97], v[232:235]\n\t"
    "v_mfma_f32_4x4x4_16b_bf16 v[236:239], v[242:243], v[98:99], v[236:239]\n\t"
    "v_mfma_f32_32x32x16_bf16 v[144:159], v[104:107], v[184:187], v[144:159]\n\t"
    "ds_read_b64_tr_b16 v[180:181], v173 offset:4096\n\t"
    "ds_read_b64_tr_b16 v[182:183], v173 offset:4608\n\t"
    "ds_read_b128 v[132:135], v169\n\t"
    "ds_read_b128 v[136:139], v170\n\t"
    "ds_read_b128 v[140:143], v171\n\t"
    "s_add_u32 m0, s50, 49152\n\t"
    "s_add_u32 s46, s40, 128\n\t"
    "s_addc_u32 s47, s41, 0\n\t"
    "global_load_lds_dwordx4 v251, s[46:47]\n\t"
    "ds_read_b128 v[240:243], v248 offset:832\n\t"
    "s_waitcnt lgkmcnt(6)\n\t"
    "v_mfma_f32_4x4x4_16b_bf16 v[232:235], v[244:245], v[100:101], v[232:235]\n\t"
    "v_mfma_f32_4x4x4_16b_bf16 v[236:239], v[246:247], v[102:103], v[236:239]\n\t"
    "v_mfma_f32_32x32x16_bf16 v[144:159], v[108:111], v[188:191], v[144:159]\n\t"
    "ds_read_b64_tr_b16 v[184:185], v174 offset:0\n\t"
    "ds_read_b64_tr_b16 v[186:187], v174 offset:512\n\t"
    "s_add_u32 m0, s50, 57344\n\t"
    "s_add_u32 s46, s40, 221312\n\t"
    "s_addc_u32 s47, s41, 0\n\t"
    "global_load_lds_dwordx4 v251, s[46:47]\n\t"
    "ds_read_b128 v[244:247], v248 offset:864\n\t"
    "s_waitcnt lgkmcnt(3)\n\t"
    "v_mfma_f32_4x4x4_16b_bf16 v[232:235], v[240:241], v[104:105], v[232:235]\n\t"
    "v_mfma_f32_4x4x4_16b_bf16 v[236:239], v[242:243], v[106:107], v[236:239]\n\t"
    "v_mfma_f32_32x32x16_bf16 v[144:159], v[112:115], v[176:179], v[144:159]\n\t"
    "ds_read_b64_tr_b16 v[188:189], v174 offset:2048\n\t"
    "ds_read_b64_tr_b16 v[190:191], v174 offset:2560\n\t"
    "s_add_u32 m0, s50, 65536\n\t"
    "s_add_u32 s46, s40, 442496\n\t"
    "s_addc_u32 s47, s41, 0\n\t"
    "global_load_lds_dwordx4 v251, s[46:47]\n\t"
    "ds_read_b128 v[240:243], v248 offset:896\n\t"
    "s_waitcnt lgkmcnt(3)\n\t"
    "v_mfma_f32_4x4x4_16b_bf16 v[232:235], v[244:245], v[108:109], v[232:235]\n\t"
    "v_mfma_f32_4x4x4_16b_bf16 v[236:239], v[246:247], v[110:111], v[236:239]\n\t"
    "v_mfma_f32_32x32x16_bf16 v[144:159], v[116:119], v[180:183], v[144:159]\n\t"
    "ds_read_b64_tr_b16 v[176:177], v174 offset:4096\n\t"
    "ds_read_b64_tr_b16 v[178:179], v174 offset:4608\n\t"
    "ds_read_b128 v[244:247], v248 offset:928\n\t"
    "s_waitcnt lgkmcnt(3)\n\t"
    "v_mfma_f32_4x4x4_16b_bf16 v[232:235], v[240:241], v[112:113], v[232:235]\n\t"
    "v_mfma_f32_4x4x4_16b_bf16 v[236:239], v[242:243], v[114:115], v[236:239]\n\t"
    "v_mfma_f32_32x32x16_bf16 v[144:159], v[120:123], v[184:187], v[144:159]\n\t"
    "ds_read_b64_tr_b16 v[180:181], v175 offset:0\n\t"
    "ds_read_b64_tr_b16 v[182:183], v175 offset:512\n\t"
    "s_waitcnt lgkmcnt(2)\n\t"
    "v_mfma_f32_4x4x4_16b_bf16 v[232:235], v[244:245], v[116:117], v[232:235]\n\t"
    "v_mfma_f32_4x4x4_16b_bf16 v[236:239], v[246:247], v[118:119], v[236:239]\n\t"
    "v_mfma_f32_32x32x16_bf16 v[144:159], v[124:127], v[188:191], v[144:159]\n\t"
    "s_waitcnt vmcnt(6)\n\t"
    "s_barrier\n\t"
    "ds_read_b64_tr_b16 v[184:185], v175 offset:2048\n\t"
    "ds_read_b64_tr_b16 v[186:187], v175 offset:2560\n\t"
    "v_mfma_f32_32x32x16_bf16 v[144:159], v[128:131], v[176:179], v[144:159]\n\t"
    "ds_read_b64_tr_b16 v[188:189], v175 offset:4096\n\t"
    "ds_read_b64_tr_b16 v[190:191], v175 offset:4608\n\t"
    "ds_read_b128 v[208:211], v226\n\t"
    "s_waitcnt lgkmcnt(5)\n\t"
    "v_mfma_f32_32x32x16_bf16 v[144:159], v[132:135], v[180:183], v[144:159]\n\t"
    "ds_read_b64_tr_b16 v[176:177], v200 offset:0\n\t"
    "ds_read_b64_tr_b16 v[178:179], v200 offset:512\n\t"
    "s_waitcnt lgkmcnt(5)\n\t"
    "v_mfma_f32_32x32x16_bf16 v[144:159], v[136:139], v[184:187], v[144:159]\n\t"
    "ds_read_b64_tr_b16 v[180:181], v200 offset:2048\n\t"
    "ds_read_b64_tr_b16 v[182:183], v200 offset:2560\n\t"
    "s_waitcnt lgkmcnt(5)\n\t"
    "v_mfma_f32_32x32x16_bf16 v[160:175], v[140:143], v[188:191], v[144:159]\n\t"
    "ds_read_b64_tr_b16 v[184:185], v200 offset:4096\n\t"
    "ds_read_b64_tr_b16 v[186:187], v200 offset:4608\n\t"
    "s_add_u32 s40, s40, 128\n\t"
    "s_addc_u32 s41, s41, 0\n\t"
    "s_waitcnt lgkmcnt(4)\n\t"
    "v_mfma_f32_32x32x16_bf16 v[144:159], v[0:3], v[176:179], 0\n\t"
    "ds_read_b64_tr_b16 v[188:189], v201 offset:0\n\t"
    "ds_read_b64_tr_b16 v[190:191], v201 offset:512\n\t"
    "s_waitcnt lgkmcnt(4)\n\t"
    "v_mfma_f32_32x32x16_bf16 v[144:159], v[4:7], v[180:183], v[144:159]\n\t"
    "ds_read_b64_tr_b16 v[176:177], v201 offset:2048\n\t"
    "ds_read_b64_tr_b16 v[178:179], v201 offset:2560\n\t"
    "s_add_u32 m0, s51, 0\n\t"
    "s_add_u32 s46, s40, 128\n\t"
    "s_addc_u32 s47, s41, 0\n\t"
    "global_load_lds_dwordx4 v249, s[46:47]\n\t"
    "v_add_u32_e32 v218, s63, v228\n\t"
    "v_mul_u32_u24_e32 v219, 0xe39, v218\n\t"
    "v_lshl_add_u32 v218, v218, 4, v227\n\t"
    "v_lshrrev_b32_e32 v219, 16, v219\n\t"
    "v_mul_u32_u24_e32 v219, 159456, v219\n\t"
    "v_add_u32_e32 v218, v218, v219\n\t"
    "s_waitcnt lgkmcnt(4)\n\t"
    "v_mfma_f32_32x32x16_bf16 v[144:159], v[8:11], v[184:187], v[144:159]\n\t"
    "ds_read_b64_tr_b16 v[180:181], v201 offset:4096\n\t"
    "ds_read_b64_tr_b16 v[182:183], v201 offset:4608\n\t"
    "v_cvt_pk_bf16_f32 v212, v160, v161\n\t"
    "s_waitcnt lgkmcnt(4)\n\t"
    "v_mfma_f32_32x32x16_bf16 v[144:159], v[12:15], v[188:191], v[144:159]\n\t"
    "ds_read_b64_tr_b16 v[184:185], v202 offset:0\n\t"
    "ds_read_b64_tr_b16 v[186:187], v202 offset:512\n\t"
    "s_add_u32 m0, s51, 8192\n\t"
    "s_add_u32 s46, s40, 221312\n\t"
    "s_addc_u32 s47, s41, 0\n\t"
    "global_load_lds_dwordx4 v249, s[46:47]\n\t"
    "v_cvt_pk_bf16_f32 v213, v162, v163\n\t"
    "s_waitcnt lgkmcnt(4)\n\t"
    "v_mfma_f32_32x32x16_bf16 v[144:159], v[16:19], v[176:179], v[144:159]\n\t"
    "ds_read_b64_tr_b16 v[188:189], v202 offset:2048\n\t"
    "ds_read_b64_tr_b16 v[190:191], v202 offset:2560\n\t"
    "v_cvt_pk_bf16_f32 v214, v164, v165\n\t"
    "s_waitcnt lgkmcnt(4)\n\t"
    "v_mfma_f32_32x32x16_bf16 v[144:159], v[20:23], v[180:183], v[144:159]\n\t"
    "ds_read_b64_tr_b16 v[176:177], v202 offset:4096\n\t"
    "ds_read_b64_tr_b16 v[178:179], v202 offset:4608\n\t"
    "s_add_u32 m0, s51, 16384\n\t"
    "s_add_u32 s46, s40, 442496\n\t"
    "s_addc_u32 s47, s41, 0\n\t"
    "global_load_lds_dwordx4 v249, s[46:47]\n\t"
    "v_cvt_pk_bf16_f32 v215, v166, v167\n\t"
    "global_store_dwordx4 v218, v[212:215], s[44:45]\n\t"
    "s_waitcnt lgkmcnt(4)\n\t"
    "v_mfma_f32_32x32x16_bf16 v[144:159], v[24:27], v[184:187], v[144:159]\n\t"
    "ds_read_b64_tr_b16 v[180:181], v203 offset:0\n\t"
    "ds_read_b64_tr_b16 v[182:183], v203 offset:512\n\t"
    "s_waitcnt vmcnt(7)\n\t"
    "s_barrier\n\t"
    "v_cvt_pk_bf16_f32 v208, v168, v169\n\t"
    "s_waitcnt lgkmcnt(4)\n\t"
    "v_mfma_f32_32x32x16_bf16 v[144:159], v[28:31], v[188:191], v[144:159]\n\t"
    "ds_read_b64_tr_b16 v[184:185], v203 offset:2048\n\t"
    "ds_read_b64_tr_b16 v[186:187], v203 offset:2560\n\t"
    "s_add_u32 m0, s51, 24576\n\t"
    "s_add_u32 s46, s40, 128\n\t"
    "s_addc_u32 s47, s41, 0\n\t"
    "global_load_lds_dwordx4 v250, s[46:47]\n\t"
    "v_cvt_pk_bf16_f32 v209, v170, v171\n\t"
    "s_waitcnt lgkmcnt(4)\n\t"
    "v_mfma_f32_32x32x16_bf16 v[144:159], v[32:35], v[176:179], v[144:159]\n\t"
    "ds_read_b64_tr_b16 v[188:189], v203 offset:4096\n\t"
    "ds_read_b64_tr_b16 v[190:191], v203 offset:4608\n\t"
    "v_cvt_pk_bf16_f32 v210, v172, v173\n\t"
    "s_waitcnt lgkmcnt(4)\n\t"
    "v_mfma_f32_32x32x16_bf16 v[144:159], v[36:39], v[180:183], v[144:159]\n\t"
    "ds_read_b64_tr_b16 v[176:177], v200 offset:24576\n\t"
    "ds_read_b64_tr_b16 v[178:179], v200 offset:25088\n\t"
    "s_add_u32 m0, s51, 32768\n\t"
    "s_add_u32 s46, s40, 221312\n\t"
    "s_addc_u32 s47, s41, 0\n\t"
    "global_load_lds_dwordx4 v250, s[46:47]\n\t"
    "v_cvt_pk_bf16_f32 v211, v174, v175\n\t"
    "global_store_dwordx4 v218, v[208:211], s[44:45] offset:672\n\t"
    "s_waitcnt lgkmcnt(4)\n\t"
    "v_mfma_f32_32x32x16_bf16 v[144:159], v[40:43], v[184:187], v[144:159]\n\t"
    "ds_read_b64_tr_b16 v[180:181], v200 offset:26624\n\t"
    "ds_read_b64_tr_b16 v[182:183], v200 offset:27136\n\t"
    "s_waitcnt lgkmcnt(4)\n\t"
    "v_mfma_f32_32x32x16_bf16 v[144:159], v[44:47], v[188:191], v[144:159]\n\t"
    "ds_read_b64_tr_b16 v[184:185], v200 offset:28672\n\t"
    "ds_read_b64_tr_b16 v[186:187], v200 offset:29184\n\t"
    "s_add_u32 m0, s51, 40960\n\t"
    "s_add_u32 s46, s40, 442496\n\t"
    "s_addc_u32 s47, s41, 0\n\t"
    "global_load_lds_dwordx4 v250, s[46:47]\n\t"
    "s_waitcnt lgkmcnt(4)\n\t"
    "v_mfma_f32_32x32x16_bf16 v[144:159], v[48:51], v[176:179], v[144:159]\n\t"
    "ds_read_b64_tr_b16 v[188:189], v201 offset:24576\n\t"
    "ds_read_b64_tr_b16 v[190:191], v201 offset:25088\n\t"
    "s_waitcnt lgkmcnt(4)\n\t"
    "v_mfma_f32_32x32x16_bf16 v[144:159], v[52:55], v[180:183], v[144:159]\n\t"
    "ds_read_b64_tr_b16 v[176:177], v201 offset:26624\n\t"
    "ds_read_b64_tr_b16 v[178:179], v201 offset:27136\n\t"
    "s_add_u32 m0, s51, 49152\n\t"
    "s_add_u32 s46, s40, 128\n\t"
    "s_addc_u32 s47, s41, 0\n\t"
    "global_load_lds_dwordx4 v251, s[46:47]\n\t"
    "s_waitcnt lgkmcnt(4)\n\t"
    "v_mfma_f32_32x32x16_bf16 v[144:159], v[56:59], v[184:187], v[144:159]\n\t"
    "ds_read_b64_tr_b16 v[180:181], v201 offset:28672\n\t"
    "ds_read_b64_tr_b16 v[182:183], v201 offset:29184\n\t"
    "s_waitcnt lgkmcnt(4)\n\t"
    "v_mfma_f32_32x32x16_bf16 v[144:159], v[60:63], v[188:191], v[144:159]\n\t"
    "ds_read_b64_tr_b16 v[184:185], v202 offset:24576\n\t"
    "ds_read_b64_tr_b16 v[186:187], v202 offset:25088\n\t"
    "s_add_u32 m0, s51, 57344\n\t"
    "s_add_u32 s46, s40, 221312\n\t"
    "s_addc_u32 s47, s41, 0\n\t"
    "global_load_lds_dwordx4 v251, s[46:47]\n\t"
    "s_waitcnt lgkmcnt(4)\n\t"
    "v_mfma_f32_32x32x16_bf16 v[144:159], v[64:67], v[176:179], v[144:159]\n\t"
    "ds_read_b64_tr_b16 v[188:189], v202 offset:26624\n\t"
    "ds_read_b64_tr_b16 v[190:191], v202 offset:27136\n\t"
    "s_waitcnt lgkmcnt(4)\n\t"
    "v_mfma_f32_32x32x16_bf16 v[144:159], v[68:71], v[180:183], v[144:159]\n\t"
    "ds_read_b64_tr_b16 v[176:177], v202 offset:28672\n\t"
    "ds_read_b64_tr_b16 v[178:179], v202 offset:29184\n\t"
    "s_add_u32 m0, s51, 65536\n\t"
    "s_add_u32 s46, s40, 442496\n\t"
    "s_addc_u32 s47, s41, 0\n\t"
    "global_load_lds_dwordx4 v251, s[46:47]\n\t"
    "s_waitcnt lgkmcnt(4)\n\t"
    "v_mfma_f32_32x32x16_bf16 v[144:159], v[72:75], v[184:187], v[144:159]\n\t"
    "ds_read_b64_tr_b16 v[180:181], v203 offset:24576\n\t"
    "ds_read_b64_tr_b16 v[182:183], v203 offset:25088\n\t"
    "s_waitcnt vmcnt(11)\n\t"
    "s_barrier\n\t"
    "s_waitcnt lgkmcnt(4)\n\t"
    "v_mfma_f32_32x32x16_bf16 v[144:159], v[76:79], v[188:191], v[144:159]\n\t"
    "ds_read_b64_tr_b16 v[184:185], v203 offset:26624\n\t"
    "ds_read_b64_tr_b16 v[186:187], v203 offset:27136\n\t"
    "s_add_u32 m0, s50, 0\n\t"
    "s_add_u32 s46, s40, 256\n\t"
    "s_addc_u32 s47, s41, 0\n\t"
    "global_load_lds_dwordx4 v249, s[46:47]\n\t"
    "s_waitcnt lgkmcnt(4)\n\t"
    "v_mfma_f32_32x32x16_bf16 v[144:159], v[80:83], v[176:179], v[144:159]\n\t"
    "ds_read_b64_tr_b16 v[188:189], v203 offset:28672\n\t"
    "ds_read_b64_tr_b16 v[190:191], v203 offset:29184\n\t"
    "s_waitcnt lgkmcnt(4)\n\t"
    "v_mfma_f32_32x32x16_bf16 v[144:159], v[84:87], v[180:183], v[144:159]\n\t"
    "ds_read_b64_tr_b16 v[176:177], v200 offset:49152\n\t"
    "ds_read_b64_tr_b16 v[178:179], v200 offset:49664\n\t"
    "s_add_u32 m0, s50, 8192\n\t"
    "s_add_u32 s46, s40, 221440\n\t"
    "s_addc_u32 s47, s41, 0\n\t"
    "global_load_lds_dwordx4 v249, s[46:47]\n\t"
    "s_waitcnt lgkmcnt(4)\n\t"
    "v_mfma_f32_32x32x16_bf16 v[144:159], v[88:91], v[184:187], v[144:159]\n\t"
    "ds_read_b64_tr_b16 v[180:181], v200 offset:51200\n\t"
    "ds_read_b64_tr_b16 v[182:183], v200 offset:51712\n\t"
    "s_waitcnt lgkmcnt(4)\n\t"
    "v_mfma_f32_32x32x16_bf16 v[144:159], v[92:95], v[188:191], v[144:159]\n\t"
    "ds_read_b64_tr_b16 v[184:185], v200 offset:53248\n\t"
    "ds_read_b64_tr_b16 v[186:187], v200 offset:53760\n\t"
    "s_add_u32 m0, s50, 16384\n\t"
    "s_add_u32 s46, s40, 442624\n\t"
    "s_addc_u32 s47, s41, 0\n\t"
    "global_load_lds_dwordx4 v249, s[46:47]\n\t"
    "s_waitcnt lgkmcnt(4)\n\t"
    "v_mfma_f32_32x32x16_bf16 v[144:159], v[96:99], v[176:179], v[144:159]\n\t"
    "ds_read_b64_tr_b16 v[188:189], v201 offset:49152\n\t"
    "ds_read_b64_tr_b16 v[190:191], v201 offset:49664\n\t"
    "v_add_u32_e32 v200, s52, v200\n\t"
    "s_waitcnt lgkmcnt(4)\n\t"
    "v_mfma_f32_32x32x16_bf16 v[144:159], v[100:103], v[180:183], v[144:159]\n\t"
    "ds_read_b64_tr_b16 v[176:177], v201 offset:51200\n\t"
    "ds_read_b64_tr_b16 v[178:179], v201 offset:51712\n\t"
    "s_waitcnt lgkmcnt(4)\n\t"
    "v_mfma_f32_32x32x16_bf16 v[144:159], v[104:107], v[184:187], v[144:159]\n\t"
    "ds_read_b64_tr_b16 v[180:181], v201 offset:53248\n\t"
    "ds_read_b64_tr_b16 v[182:183], v201 offset:53760\n\t"
    "s_waitcnt lgkmcnt(4)\n\t"
    "v_mfma_f32_32x32x16_bf16 v[144:159], v[108:111], v[188:191], v[144:159]\n\t"
    "ds_read_b64_tr_b16 v[184:185], v202 offset:49152\n\t"
    "ds_read_b64_tr_b16 v[186:187], v202 offset:49664\n\t"
    "v_add_u32_e32 v201, s52, v201\n\t"
    "s_waitcnt lgkmcnt(4)\n\t"
    "v_mfma_f32_32x32x16_bf16 v[144:159], v[112:115], v[176:179], v[144:159]\n\t"
    "ds_read_b64_tr_b16 v[188:189], v202 offset:51200\n\t"
    "ds_read_b64_tr_b16 v[190:191], v202 offset:51712\n\t"
    "s_waitcnt lgkmcnt(4)\n\t"
    "v_mfma_f32_32x32x16_bf16 v[144:159], v[116:119], v[180:183], v[144:159]\n\t"
    "ds_read_b64_tr_b16 v[176:177], v202 offset:53248\n\t"
    "ds_read_b64_tr_b16 v[178:179], v202 offset:53760\n\t"
    "s_waitcnt lgkmcnt(4)\n\t"
    "v_mfma_f32_32x32x16_bf16 v[144:159], v[120:123], v[184:187], v[144:159]\n\t"
    "ds_read_b64_tr_b16 v[180:181], v203 offset:49152\n\t"
    "ds_read_b64_tr_b16 v[182:183], v203 offset:49664\n\t"
    "s_waitcnt vmcnt(11)\n\t"
    "s_barrier\n\t"
    "v_add_u32_e32 v202, s52, v202\n\t"
    "v_add_u32_e32 v226, 32, v226\n\t"
    "s_waitcnt lgkmcnt(4)\n\t"
    "v_mfma_f32_32x32x16_bf16 v[144:159], v[124:127], v[188:191], v[144:159]\n\t"
    "ds_read_b64_tr_b16 v[184:185], v203 offset:51200\n\t"
    "ds_read_b64_tr_b16 v[186:187], v203 offset:51712\n\t"
    "s_add_u32 m0, s50, 24576\n\t"
    "s_add_u32 s46, s40, 256\n\t"
    "s_addc_u32 s47, s41, 0\n\t"
    "global_load_lds_dwordx4 v250, s[46:47]\n\t"
    "s_waitcnt lgkmcnt(4)\n\t"
    "v_mfma_f32_32x32x16_bf16 v[144:159], v[128:131], v[176:179], v[144:159]\n\t"
    "ds_read_b64_tr_b16 v[188:189], v203 offset:53248\n\t"
    "ds_read_b64_tr_b16 v[190:191], v203 offset:53760\n\t"
    "ds_read_b128 v[208:211], v226\n\t"
    "s_waitcnt lgkmcnt(5)\n\t"
    "v_mfma_f32_32x32x16_bf16 v[144:159], v[132:135], v[180:183], v[144:159]\n\t"
    "ds_read_b64_tr_b16 v[176:177], v200 offset:0\n\t"
    "ds_read_b64_tr_b16 v[178:179], v200 offset:512\n\t"
    "s_add_u32 m0, s50, 32768\n\t"
    "s_add_u32 s46, s40, 221440\n\t"
    "s_addc_u32 s47, s41, 0\n\t"
    "global_load_lds_dwordx4 v250, s[46:47]\n\t"
    "v_add_u32_e32 v203, s52, v203\n\t"
    "s_waitcnt lgkmcnt(5)\n\t"
    "v_mfma_f32_32x32x16_bf16 v[144:159], v[136:139], v[184:187], v[144:159]\n\t"
    "ds_read_b64_tr_b16 v[180:181], v200 offset:2048\n\t"
    "ds_read_b64_tr_b16 v[182:183], v200 offset:2560\n\t"
    "s_waitcnt lgkmcnt(5)\n\t"
    "v_mfma_f32_32x32x16_bf16 v[160:175], v[140:143], v[188:191], v[144:159]\n\t"
    "ds_read_b64_tr_b16 v[184:185], v200 offset:4096\n\t"
    "ds_read_b64_tr_b16 v[186:187], v200 offset:4608\n\t"
    "s_add_u32 m0, s50, 40960\n\t"
    "s_add_u32 s46, s40, 442624\n\t"
    "s_addc_u32 s47, s41, 0\n\t"
    "global_load_lds_dwordx4 v250, s[46:47]\n\t"
    "s_sub_u32 s52, 0, s52\n\t"
    "s_mov_b32 s46, s50\n\t"
    "s_mov_b32 s50, s51\n\t"
    "s_mov_b32 s51, s46\n\t"
    "s_add_u32 s40, s40, 128\n\t"
    "s_addc_u32 s41, s41, 0\n\t"
    "s_lshr_b64 s[58:59], s[58:59], 8\n\t"
    "s_add_u32 s62, s62, 1\n\t"
    "s_add_u32 s63, s63, 64\n\t"
    "s_mov_b32 s54, 2\n\t"
    ".p2align 4\n\t"
    ".Lk1v%=:\n\t"
    "s_waitcnt lgkmcnt(4)\n\t"
    "v_mfma_f32_32x32x16_bf16 v[144:159], v[0:3], v[176:179], 0\n\t"
    "ds_read_b64_tr_b16 v[188:189], v201 offset:0\n\t"
    "ds_read_b64_tr_b16 v[190:191], v201 offset:512\n\t"
    "s_waitcnt lgkmcnt(4)\n\t"
    "v_mfma_f32_32x32x16_bf16 v[144:159], v[4:7], v[180:183], v[144:159]\n\t"
    "ds_read_b64_tr_b16 v[176:177], v201 offset:2048\n\t"
    "ds_read_b64_tr_b16 v[178:179], v201 offset:2560\n\t"
    "v_add_u32_e32 v218, s63, v228\n\t"
    "v_mul_u32_u24_e32 v219, 0xe39, v218\n\t"
    "v_lshl_add_u32 v218, v218, 4, v227\n\t"
    "v_lshrrev_b32_e32 v219, 16, v219\n\t"
    "v_mul_u32_u24_e32 v219, 159456, v219\n\t"
    "v_add_u32_e32 v218, v218, v219\n\t"
    "s_waitcnt lgkmcnt(4)\n\t"
    "v_mfma_f32_32x32x16_bf16 v[144:159], v[8:11], v[184:187], v[144:159]\n\t"
    "ds_read_b64_tr_b16 v[180:181], v201 offset:4096\n\t"
    "ds_read_b64_tr_b16 v[182:183], v201 offset:4608\n\t"
    "v_cvt_pk_bf16_f32 v212, v160, v161\n\t"
    "s_waitcnt lgkmcnt(4)\n\t"
    "v_mfma_f32_32x32x16_bf16 v[144:159], v[12:15], v[188:191], v[144:159]\n\t"
    "ds_read_b64_tr_b16 v[184:185], v202 offset:0\n\t"
    "ds_read_b64_tr_b16 v[186:187], v202 offset:512\n\t"
    "v_cvt_pk_bf16_f32 v213, v162, v163\n\t"
    "s_waitcnt lgkmcnt(4)\n\t"
    "v_mfma_f32_32x32x16_bf16 v[144:159], v[16:19], v[176:179], v[144:159]\n\t"
    "ds_read_b64_tr_b16 v[188:189], v202 offset:2048\n\t"
    "ds_read_b64_tr_b16 v[190:191], v202 offset:2560\n\t"
    "v_cvt_pk_bf16_f32 v214, v164, v165\n\t"
    "s_waitcnt lgkmcnt(4)\n\t"
    "v_mfma_f32_32x32x16_bf16 v[144:159], v[20:23], v[180:183], v[144:159]\n\t"
    "ds_read_b64_tr_b16 v[176:177], v202 offset:4096\n\t"
    "ds_read_b64_tr_b16 v[178:179], v202 offset:4608\n\t"
    "v_cvt_pk_bf16_f32 v215, v166, v167\n\t"
    "global_store_dwordx4 v218, v[212:215], s[44:45]\n\t"
    "s_waitcnt lgkmcnt(4)\n\t"
    "v_mfma_f32_32x32x16_bf16 v[144:159], v[24:27], v[184:187], v[144:159]\n\t"
    "ds_read_b64_tr_b16 v[180:181], v203 offset:0\n\t"
    "ds_read_b64_tr_b16 v[182:183], v203 offset:512\n\t"
    "s_waitcnt vmcnt(10)\n\t"
    "s_barrier\n\t"
    "v_cvt_pk_bf16_f32 v208, v168, v169\n\t"
    "s_waitcnt lgkmcnt(4)\n\t"
    "v_mfma_f32_32x32x16_bf16 v[144:159], v[28:31], v[188:191], v[144:159]\n\t"
    "ds_read_b64_tr_b16 v[184:185], v203 offset:2048\n\t"
    "ds_read_b64_tr_b16 v[186:187], v203 offset:2560\n\t"
    "s_add_u32 m0, s51, 49152\n\t"
    "s_add_u32 s46, s40, 128\n\t"
    "s_addc_u32 s47, s41, 0\n\t"
    "global_load_lds_dwordx4 v251, s[46:47]\n\t"
    "v_cvt_pk_bf16_f32 v209, v170, v171\n\t"
    "s_waitcnt lgkmcnt(4)\n\t"
    "v_mfma_f32_32x32x16_bf16 v[144:159], v[32:35], v[176:179], v[144:159]\n\t"
    "ds_read_b64_tr_b16 v[188:189], v203 offset:4096\n\t"
    "ds_read_b64_tr_b16 v[190:191], v203 offset:4608\n\t"
    "v_cvt_pk_bf16_f32 v210, v172, v173\n\t"
    "s_waitcnt lgkmcnt(4)\n\t"
    "v_mfma_f32_32x32x16_bf16 v[144:159], v[36:39], v[180:183], v[144:159]\n\t"
    "ds_read_b64_tr_b16 v[176:177], v200 offset:24576\n\t"
    "ds_read_b64_tr_b16 v[178:179], v200 offset:25088\n\t"
    "s_add_u32 m0, s51, 57344\n\t"
    "s_add_u32 s46, s40, 221312\n\t"
    "s_addc_u32 s47, s41, 0\n\t"
    "global_load_lds_dwordx4 v251, s[46:47]\n\t"
    "v_cvt_pk_bf16_f32 v211, v174, v175\n\t"
    "global_store_dwordx4 v218, v[208:211], s[44:45] offset:672\n\t"
    "s_waitcnt lgkmcnt(4)\n\t"
    "v_mfma_f32_32x32x16_bf16 v[144:159], v[40:43], v[184:187], v[144:159]\n\t"
    "ds_read_b64_tr_b16 v[180:181], v200 offset:26624\n\t"
    "ds_read_b64_tr_b16 v[182:183], v200 offset:27136\n\t"
    "s_waitcnt lgkmcnt(4)\n\t"
    "v_mfma_f32_32x32x16_bf16 v[144:159], v[44:47], v[188:191], v[144:159]\n\t"
    "ds_read_b64_tr_b16 v[184:185], v200 offset:28672\n\t"
    "ds_read_b64_tr_b16 v[186:187], v200 offset:29184\n\t"
    "s_add_u32 m0, s51, 65536\n\t"
    "s_add_u32 s46, s40, 442496\n\t"
    "s_addc_u32 s47, s41, 0\n\t"
    "global_load_lds_dwordx4 v251, s[46:47]\n\t"
    "s_waitcnt lgkmcnt(4)\n\t"
    "v_mfma_f32_32x32x16_bf16 v[144:159], v[48:51], v[176:179], v[144:159]\n\t"
    "ds_read_b64_tr_b16 v[188:189], v201 offset:24576\n\t"
    "ds_read_b64_tr_b16 v[190:191], v201 offset:25088\n\t"
    "s_waitcnt lgkmcnt(4)\n\t"
    "v_mfma_f32_32x32x16_bf16 v[144:159], v[52:55], v[180:183], v[144:159]\n\t"
    "ds_read_b64_tr_b16 v[176:177], v201 offset:26624\n\t"
    "ds_read_b64_tr_b16 v[178:179], v201 offset:27136\n\t"
    "s_waitcnt lgkmcnt(4)\n\t"
    "v_mfma_f32_32x32x16_bf16 v[144:159], v[56:59], v[184:187], v[144:159]\n\t"
    "ds_read_b64_tr_b16 v[180:181], v201 offset:28672\n\t"
    "ds_read_b64_tr_b16 v[182:183], v201 offset:29184\n\t"
    "s_waitcnt lgkmcnt(4)\n\t"
    "v_mfma_f32_32x32x16_bf16 v[144:159], v[60:63], v[188:191], v[144:159]\n\t"
    "ds_read_b64_tr_b16 v[184:185], v202 offset:24576\n\t"
    "ds_read_b64_tr_b16 v[186:187], v202 offset:25088\n\t"
    "s_waitcnt lgkmcnt(4)\n\t"
    "v_mfma_f32_32x32x16_bf16 v[144:159], v[64:67], v[176:179], v[144:159]\n\t"
    "ds_read_b64_tr_b16 v[188:189], v202 offset:26624\n\t"
    "ds_read_b64_tr_b16 v[190:191], v202 offset:27136\n\t"
    "s_waitcnt lgkmcnt(4)\n\t"
    "v_mfma_f32_32x32x16_bf16 v[144:159], v[68:71], v[180:183], v[144:159]\n\t"
    "ds_read_b64_tr_b16 v[176:177], v202 offset:28672\n\t"
    "ds_read_b64_tr_b16 v[178:179], v202 offset:29184\n\t"
    "s_waitcnt lgkmcnt(4)\n\t"
    "v_mfma_f32_32x32x16_bf16 v[144:159], v[72:75], v[184:187], v[144:159]\n\t"
    "ds_read_b64_tr_b16 v[180:181], v203 offset:24576\n\t"
    "ds_read_b64_tr_b16 v[182:183], v203 offset:25088\n\t"
    "s_waitcnt vmcnt(11)\n\t"
    "s_barrier\n\t"
    "s_waitcnt lgkmcnt(4)\n\t"
    "v_mfma_f32_32x32x16_bf16 v[144:159], v[76:79], v[188:191], v[144:159]\n\t"
    "ds_read_b64_tr_b16 v[184:185], v203 offset:26624\n\t"
    "ds_read_b64_tr_b16 v[186:187], v203 offset:27136\n\t"
    "s_add_u32 m0, s50, 0\n\t"
    "s_add_u32 s46, s40, 256\n\t"
    "s_addc_u32 s47, s41, 0\n\t"
    "global_load_lds_dwordx4 v249, s[46:47]\n\t"
    "s_waitcnt lgkmcnt(4)\n\t"
    "v_mfma_f32_32x32x16_bf16 v[144:159], v[80:83], v[176:179], v[144:159]\n\t"
    "ds_read_b64_tr_b16 v[188:189], v203 offset:28672\n\t"
    "ds_read_b64_tr_b16 v[190:191], v203 offset:29184\n\t"
    "s_waitcnt lgkmcnt(4)\n\t"
    "v_mfma_f32_32x32x16_bf16 v[144:159], v[84:87], v[180:183], v[144:159]\n\t"
    "ds_read_b64_tr_b16 v[176:177], v200 offset:49152\n\t"
    "ds_read_b64_tr_b16 v[178:179], v200 offset:49664\n\t"
    "s_add_u32 m0, s50, 8192\n\t"
    "s_add_u32 s46, s40, 221440\n\t"
    "s_addc_u32 s47, s41, 0\n\t"
    "global_load_lds_dwordx4 v249, s[46:47]\n\t"
    "s_waitcnt lgkmcnt(4)\n\t"
    "v_mfma_f32_32x32x16_bf16 v[144:159], v[88:91], v[184:187], v[144:159]\n\t"
    "ds_read_b64_tr_b16 v[180:181], v200 offset:51200\n\t"
    "ds_read_b64_tr_b16 v[182:183], v200 offset:51712\n\t"
    "s_waitcnt lgkmcnt(4)\n\t"
    "v_mfma_f32_32x32x16_bf16 v[144:159], v[92:95], v[188:191], v[144:159]\n\t"
    "ds_read_b64_tr_b16 v[184:185], v200 offset:53248\n\t"
    "ds_read_b64_tr_b16 v[186:187], v200 offset:53760\n\t"
    "s_add_u32 m0, s50, 16384\n\t"
    "s_add_u32 s46, s40, 442624\n\t"
    "s_addc_u32 s47, s41, 0\n\t"
    "global_load_lds_dwordx4 v249, s[46:47]\n\t"
    "s_waitcnt lgkmcnt(4)\n\t"
    "v_mfma_f32_32x32x16_bf16 v[144:159], v[96:99], v[176:179], v[144:159]\n\t"
    "ds_read_b64_tr_b16 v[188:189], v201 offset:49152\n\t"
    "ds_read_b64_tr_b16 v[190:191], v201 offset:49664\n\t"
    "v_add_u32_e32 v200, s52, v200\n\t"
    "s_waitcnt lgkmcnt(4)\n\t"
    "v_mfma_f32_32x32x16_bf16 v[144:159], v[100:103], v[180:183], v[144:159]\n\t"
    "ds_read_b64_tr_b16 v[176:177], v201 offset:51200\n\t"
    "ds_read_b64_tr_b16 v[178:179], v201 offset:51712\n\t"
    "s_waitcnt lgkmcnt(4)\n\t"
    "v_mfma_f32_32x32x16_bf16 v[144:159], v[104:107], v[184:187], v[144:159]\n\t"
    "ds_read_b64_tr_b16 v[180:181], v201 offset:53248\n\t"
    "ds_read_b64_tr_b16 v[182:183], v201 offset:53760\n\t"
    "s_waitcnt lgkmcnt(4)\n\t"
    "v_mfma_f32_32x32x16_bf16 v[144:159], v[108:111], v[188:191], v[144:159]\n\t"
    "ds_read_b64_tr_b16 v[184:185], v202 offset:49152\n\t"
    "ds_read_b64_tr_b16 v[186:187], v202 offset:49664\n\t"
    "v_add_u32_e32 v201, s52, v201\n\t"
    "s_waitcnt lgkmcnt(4)\n\t"
    "v_mfma_f32_32x32x16_bf16 v[144:159], v[112:115], v[176:179], v[144:159]\n\t"
    "ds_read_b64_tr_b16 v[188:189], v202 offset:51200\n\t"
    "ds_read_b64_tr_b16 v[190:191], v202 offset:51712\n\t"
    "s_waitcnt lgkmcnt(4)\n\t"
    "v_mfma_f32_32x32x16_bf16 v[144:159], v[116:119], v[180:183], v[144:159]\n\t"
    "ds_read_b64_tr_b16 v[176:177], v202 offset:53248\n\t"
    "ds_read_b64_tr_b16 v[178:179], v202 offset:53760\n\t"
    "s_waitcnt lgkmcnt(4)\n\t"
    "v_mfma_f32_32x32x16_bf16 v[144:159], v[120:123], v[184:187], v[144:159]\n\t"
    "ds_read_b64_tr_b16 v[180:181], v203 offset:49152\n\t"
    "ds_read_b64_tr_b16 v[182:183], v203 offset:49664\n\t"
    "s_waitcnt vmcnt(11)\n\t"
    "s_barrier\n\t"
    "v_add_u32_e32 v202, s52, v202\n\t"
    "v_add_u32_e32 v226, 32, v226\n\t"
    "s_waitcnt lgkmcnt(4)\n\t"
    "v_mfma_f32_32x32x16_bf16 v[144:159], v[124:127], v[188:191], v[144:159]\n\t"
    "ds_read_b64_tr_b16 v[184:185], v203 offset:51200\n\t"
    "ds_read_b64_tr_b16 v[186:187], v203 offset:51712\n\t"
    "s_add_u32 m0, s50, 24576\n\t"
    "s_add_u32 s46, s40, 256\n\t"
    "s_addc_u32 s47, s41, 0\n\t"
    "global_load_lds_dwordx4 v250, s[46:47]\n\t"
    "s_waitcnt lgkmcnt(4)\n\t"
    "v_mfma_f32_32x32x16_bf16 v[144:159], v[128:131], v[176:179], v[144:159]\n\t"
    "ds_read_b64_tr_b16 v[188:189], v203 offset:53248\n\t"
    "ds_read_b64_tr_b16 v[190:191], v203 offset:53760\n\t"
    "ds_read_b128 v[208:211], v226\n\t"
    "s_waitcnt lgkmcnt(5)\n\t"
    "v_mfma_f32_32x32x16_bf16 v[144:159], v[132:135], v[180:183], v[144:159]\n\t"
    "ds_read_b64_tr_b16 v[176:177], v200 offset:0\n\t"
    "ds_read_b64_tr_b16 v[178:179], v200 offset:512\n\t"
    "s_add_u32 m0, s50, 32768\n\t"
    "s_add_u32 s46, s40, 221440\n\t"
    "s_addc_u32 s47, s41, 0\n\t"
    "global_load_lds_dwordx4 v250, s[46:47]\n\t"
    "v_add_u32_e32 v203, s52, v203\n\t"
    "s_waitcnt lgkmcnt(5)\n\t"
    "v_mfma_f32_32x32x16_bf16 v[144:159], v[136:139], v[184:187], v[144:159]\n\t"
    "ds_read_b64_tr_b16 v[180:181], v200 offset:2048\n\t"
    "ds_read_b64_tr_b16 v[182:183], v200 offset:2560\n\t"
    "s_waitcnt lgkmcnt(5)\n\t"
    "v_mfma_f32_32x32x16_bf16 v[160:175], v[140:143], v[188:191], v[144:159]\n\t"
    "ds_read_b64_tr_b16 v[184:185], v200 offset:4096\n\t"
    "ds_read_b64_tr_b16 v[186:187], v200 offset:4608\n\t"
    "s_add_u32 m0, s50, 40960\n\t"
    "s_add_u32 s46, s40, 442624\n\t"
    "s_addc_u32 s47, s41, 0\n\t"
    "global_load_lds_dwordx4 v250, s[46:47]\n\t"
    "s_sub_u32 s52, 0, s52\n\t"
    "s_mov_b32 s46, s50\n\t"
    "s_mov_b32 s50, s51\n\t"
    "s_mov_b32 s51, s46\n\t"
    "s_add_u32 s40, s40, 128\n\t"
    "s_addc_u32 s41, s41, 0\n\t"
    "s_lshr_b64 s[58:59], s[58:59], 8\n\t"
    "s_add_u32 s62, s62, 1\n\t"
    "s_add_u32 s63, s63, 64\n\t"
    "s_sub_u32 s54, s54, 1\n\t"
    "s_cmp_lg_u32 s54, 0\n\t"
    "s_cbranch_scc1 .Lk1v%=\n\t"
    ".Lk1vt%=:\n\t"
    "s_waitcnt lgkmcnt(4)\n\t"
    "v_mfma_f32_32x32x16_bf16 v[144:159], v[0:3], v[176:179], 0\n\t"
    "ds_read_b64_tr_b16 v[188:189], v201 offset:0\n\t"
    "ds_read_b64_tr_b16 v[190:191], v201 offset:512\n\t"
    "s_waitcnt lgkmcnt(4)\n\t"
    "v_mfma_f32_32x32x16_bf16 v[144:159], v[4:7], v[180:183], v[144:159]\n\t"
    "ds_read_b64_tr_b16 v[176:177], v201 offset:2048\n\t"
    "ds_read_b64_tr_b16 v[178:179], v201 offset:2560\n\t"
    "v_add_u32_e32 v218, s63, v228\n\t"
    "v_mul_u32_u24_e32 v219, 0xe39, v218\n\t"
    "v_lshl_add_u32 v218, v218, 4, v227\n\t"
    "v_lshrrev_b32_e32 v219, 16, v219\n\t"
    "v_mul_u32_u24_e32 v219, 159456, v219\n\t"
    "v_add_u32_e32 v218, v218, v219\n\t"
    "s_waitcnt lgkmcnt(4)\n\t"
    "v_mfma_f32_32x32x16_bf16 v[144:159], v[8:11], v[184:187], v[144:159]\n\t"
    "ds_read_b64_tr_b16 v[180:181], v201 offset:4096\n\t"
    "ds_read_b64_tr_b16 v[182:183], v201 offset:4608\n\t"
    "v_cvt_pk_bf16_f32 v212, v160, v161\n\t"
    "s_waitcnt lgkmcnt(4)\n\t"
    "v_mfma_f32_32x32x16_bf16 v[144:159], v[12:15], v[188:191], v[144:159]\n\t"
    "ds_read_b64_tr_b16 v[184:185], v202 offset:0\n\t"
    "ds_read_b64_tr_b16 v[186:187], v202 offset:512\n\t"
    "v_cvt_pk_bf16_f32 v213, v162, v163\n\t"
    "s_waitcnt lgkmcnt(4)\n\t"
    "v_mfma_f32_32x32x16_bf16 v[144:159], v[16:19], v[176:179], v[144:159]\n\t"
    "ds_read_b64_tr_b16 v[188:189], v202 offset:2048\n\t"
    "ds_read_b64_tr_b16 v[190:191], v202 offset:2560\n\t"
    "v_cvt_pk_bf16_f32 v214, v164, v165\n\t"
    "s_waitcnt lgkmcnt(4)\n\t"
    "v_mfma_f32_32x32x16_bf16 v[144:159], v[20:23], v[180:183], v[144:159]\n\t"
    "ds_read_b64_tr_b16 v[176:177], v202 offset:4096\n\t"
    "ds_read_b64_tr_b16 v[178:179], v202 offset:4608\n\t"
    "v_cvt_pk_bf16_f32 v215, v166, v167\n\t"
    "global_store_dwordx4 v218, v[212:215], s[44:45]\n\t"
    "s_waitcnt lgkmcnt(4)\n\t"
    "v_mfma_f32_32x32x16_bf16 v[144:159], v[24:27], v[184:187], v[144:159]\n\t"
    "ds_read_b64_tr_b16 v[180:181], v203 offset:0\n\t"
    "ds_read_b64_tr_b16 v[182:183], v203 offset:512\n\t"
    "s_waitcnt vmcnt(12)\n\t"
    "s_barrier\n\t"
    "v_cvt_pk_bf16_f32 v208, v168, v169\n\t"
    "s_waitcnt lgkmcnt(4)\n\t"
    "v_mfma_f32_32x32x16_bf16 v[144:159], v[28:31], v[188:191], v[144:159]\n\t"
    "ds_read_b64_tr_b16 v[184:185], v203 offset:2048\n\t"
    "ds_read_b64_tr_b16 v[186:187], v203 offset:2560\n\t"
    "s_add_u32 m0, s51, 49152\n\t"
    "s_add_u32 s46, s40, 128\n\t"
    "s_addc_u32 s47, s41, 0\n\t"
    "global_load_lds_dwordx4 v251, s[46:47]\n\t"
    "v_cvt_pk_bf16_f32 v209, v170, v171\n\t"
    "s_waitcnt lgkmcnt(4)\n\t"
    "v_mfma_f32_32x32x16_bf16 v[144:159], v[32:35], v[176:179], v[144:159]\n\t"
    "ds_read_b64_tr_b16 v[188:189], v203 offset:4096\n\t"
    "ds_read_b64_tr_b16 v[190:191], v203 offset:4608\n\t"
    "v_cvt_pk_bf16_f32 v210, v172, v173\n\t"
    "s_waitcnt lgkmcnt(4)\n\t"
    "v_mfma_f32_32x32x16_bf16 v[144:159], v[36:39], v[180:183], v[144:159]\n\t"
    "ds_read_b64_tr_b16 v[176:177], v200 offset:24576\n\t"
    "ds_read_b64_tr_b16 v[178:179], v200 offset:25088\n\t"
    "s_add_u32 m0, s51, 57344\n\t"
    "s_add_u32 s46, s40, 221312\n\t"
    "s_addc_u32 s47, s41, 0\n\t"
    "global_load_lds_dwordx4 v251, s[46:47]\n\t"
    "v_cvt_pk_bf16_f32 v211, v174, v175\n\t"
    "global_store_dwordx4 v218, v[208:211], s[44:45] offset:672\n\t"
    "s_waitcnt lgkmcnt(4)\n\t"
    "v_mfma_f32_32x32x16_bf16 v[144:159], v[40:43], v[184:187], v[144:159]\n\t"
    "ds_read_b64_tr_b16 v[180:181], v200 offset:26624\n\t"
    "ds_read_b64_tr_b16 v[182:183], v200 offset:27136\n\t"
    "s_waitcnt lgkmcnt(4)\n\t"
    "v_mfma_f32_32x32x16_bf16 v[144:159], v[44:47], v[188:191], v[144:159]\n\t"
    "ds_read_b64_tr_b16 v[184:185], v200 offset:28672\n\t"
    "ds_read_b64_tr_b16 v[186:187], v200 offset:29184\n\t"
    "s_add_u32 m0, s51, 65536\n\t"
    "s_add_u32 s46, s40, 442496\n\t"
    "s_addc_u32 s47, s41, 0\n\t"
    "global_load_lds_dwordx4 v251, s[46:47]\n\t"
    "s_waitcnt lgkmcnt(4)\n\t"
    "v_mfma_f32_32x32x16_bf16 v[144:159], v[48:51], v[176:179], v[144:159]\n\t"
    "ds_read_b64_tr_b16 v[188:189], v201 offset:24576\n\t"
    "ds_read_b64_tr_b16 v[190:191], v201 offset:25088\n\t"
    "s_waitcnt lgkmcnt(4)\n\t"
    "v_mfma_f32_32x32x16_bf16 v[144:159], v[52:55], v[180:183], v[144:159]\n\t"
    "ds_read_b64_tr_b16 v[176:177], v201 offset:26624\n\t"
    "ds_read_b64_tr_b16 v[178:179], v201 offset:27136\n\t"
    "s_waitcnt lgkmcnt(4)\n\t"
    "v_mfma_f32_32x32x16_bf16 v[144:159], v[56:59], v[184:187], v[144:159]\n\t"
    "ds_read_b64_tr_b16 v[180:181], v201 offset:28672\n\t"
    "ds_read_b64_tr_b16 v[182:183], v201 offset:29184\n\t"
    "s_waitcnt lgkmcnt(4)\n\t"
    "v_mfma_f32_32x32x16_bf16 v[144:159], v[60:63], v[188:191], v[144:159]\n\t"
    "ds_read_b64_tr_b16 v[184:185], v202 offset:24576\n\t"
    "ds_read_b64_tr_b16 v[186:187], v202 offset:25088\n\t"
    "s_waitcnt lgkmcnt(4)\n\t"
    "v_mfma_f32_32x32x16_bf16 v[144:159], v[64:67], v[176:179], v[144:159]\n\t"
    "ds_read_b64_tr_b16 v[188:189], v202 offset:26624\n\t"
    "ds_read_b64_tr_b16 v[190:191], v202 offset:27136\n\t"
    "s_waitcnt lgkmcnt(4)\n\t"
    "v_mfma_f32_32x32x16_bf16 v[144:159], v[68:71], v[180:183], v[144:159]\n\t"
    "ds_read_b64_tr_b16 v[176:177], v202 offset:28672\n\t"
    "ds_read_b64_tr_b16 v[178:179], v202 offset:29184\n\t"
    "s_waitcnt lgkmcnt(4)\n\t"
    "v_mfma_f32_32x32x16_bf16 v[144:159], v[72:75], v[184:187], v[144:159]\n\t"
    "ds_read_b64_tr_b16 v[180:181], v203 offset:24576\n\t"
    "ds_read_b64_tr_b16 v[182:183], v203 offset:25088\n\t"
    "s_waitcnt vmcnt(11)\n\t"
    "s_barrier\n\t"
    "s_waitcnt lgkmcnt(4)\n\t"
    "v_mfma_f32_32x32x16_bf16 v[144:159], v[76:79], v[188:191], v[144:159]\n\t"
    "ds_read_b64_tr_b16 v[184:185], v203 offset:26624\n\t"
    "ds_read_b64_tr_b16 v[186:187], v203 offset:27136\n\t"
    "s_waitcnt lgkmcnt(4)\n\t"
    "v_mfma_f32_32x32x16_bf16 v[144:159], v[80:83], v[176:179], v[144:159]\n\t"
    "ds_read_b64_tr_b16 v[188:189], v203 offset:28672\n\t"
    "ds_read_b64_tr_b16 v[190:191], v203 offset:29184\n\t"
    "s_waitcnt lgkmcnt(4)\n\t"
    "v_mfma_f32_32x32x16_bf16 v[144:159], v[84:87], v[180:183], v[144:159]\n\t"
    "ds_read_b64_tr_b16 v[176:177], v200 offset:49152\n\t"
    "ds_read_b64_tr_b16 v[178:179], v200 offset:49664\n\t"
    "s_waitcnt lgkmcnt(4)\n\t"
    "v_mfma_f32_32x32x16_bf16 v[144:159], v[88:91], v[184:187], v[144:159]\n\t"
    "ds_read_b64_tr_b16 v[180:181], v200 offset:51200\n\t"
    "ds_read_b64_tr_b16 v[182:183], v200 offset:51712\n\t"
    "s_waitcnt lgkmcnt(4)\n\t"
    "v_mfma_f32_32x32x16_bf16 v[144:159], v[92:95], v[188:191], v[144:159]\n\t"
    "ds_read_b64_tr_b16 v[184:185], v200 offset:53248\n\t"
    "ds_read_b64_tr_b16 v[186:187], v200 offset:53760\n\t"
    "s_waitcnt lgkmcnt(4)\n\t"
    "v_mfma_f32_32x32x16_bf16 v[144:159], v[96:99], v[176:179], v[144:159]\n\t"
    "ds_read_b64_tr_b16 v[188:189], v201 offset:49152\n\t"
    "ds_read_b64_tr_b16 v[190:191], v201 offset:49664\n\t"
    "v_add_u32_e32 v200, s52, v200\n\t"
    "s_waitcnt lgkmcnt(4)\n\t"
    "v_mfma_f32_32x32x16_bf16 v[144:159], v[100:103], v[180:183], v[144:159]\n\t"
    "ds_read_b64_tr_b16 v[176:177], v201 offset:51200\n\t"
    "ds_read_b64_tr_b16 v[178:179], v201 offset:51712\n\t"
    "s_waitcnt lgkmcnt(4)\n\t"
    "v_mfma_f32_32x32x16_bf16 v[144:159], v[104:107], v[184:187], v[144:159]\n\t"
    "ds_read_b64_tr_b16 v[180:181], v201 offset:53248\n\t"
    "ds_read_b64_tr_b16 v[182:183], v201 offset:53760\n\t"
    "s_waitcnt lgkmcnt(4)\n\t"
    "v_mfma_f32_32x32x16_bf16 v[144:159], v[108:111], v[188:191], v[144:159]\n\t"
    "ds_read_b64_tr_b16 v[184:185], v202 offset:49152\n\t"
    "ds_read_b64_tr_b16 v[186:187], v202 offset:49664\n\t"
    "v_add_u32_e32 v201, s52, v201\n\t"
    "s_waitcnt lgkmcnt(4)\n\t"
    "v_mfma_f32_32x32x16_bf16 v[144:159], v[112:115], v[176:179], v[144:159]\n\t"
    "ds_read_b64_tr_b16 v[188:189], v202 offset:51200\n\t"
    "ds_read_b64_tr_b16 v[190:191], v202 offset:51712\n\t"
    "s_waitcnt lgkmcnt(4)\n\t"
    "v_mfma_f32_32x32x16_bf16 v[144:159], v[116:119], v[180:183], v[144:159]\n\t"
    "ds_read_b64_tr_b16 v[176:177], v202 offset:53248\n\t"
    "ds_read_b64_tr_b16 v[178:179], v202 offset:53760\n\t"
    "s_waitcnt lgkmcnt(4)\n\t"
    "v_mfma_f32_32x32x16_bf16 v[144:159], v[120:123], v[184:187], v[144:159]\n\t"
    "ds_read_b64_tr_b16 v[180:181], v203 offset:49152\n\t"
    "ds_read_b64_tr_b16 v[182:183], v203 offset:49664\n\t"
    "s_waitcnt vmcnt(8)\n\t"
    "s_barrier\n\t"
    "v_add_u32_e32 v202, s52, v202\n\t"
    "v_add_u32_e32 v226, 32, v226\n\t"
    "s_waitcnt lgkmcnt(4)\n\t"
    "v_mfma_f32_32x32x16_bf16 v[144:159], v[124:127], v[188:191], v[144:159]\n\t"
    "ds_read_b64_tr_b16 v[184:185], v203 offset:51200\n\t"
    "ds_read_b64_tr_b16 v[186:187], v203 offset:51712\n\t"
    "s_waitcnt lgkmcnt(4)\n\t"
    "v_mfma_f32_32x32x16_bf16 v[144:159], v[128:131], v[176:179], v[144:159]\n\t"
    "ds_read_b64_tr_b16 v[188:189], v203 offset:53248\n\t"
    "ds_read_b64_tr_b16 v[190:191], v203 offset:53760\n\t"
    "ds_read_b128 v[208:211], v226\n\t"
    "s_waitcnt lgkmcnt(5)\n\t"
    "v_mfma_f32_32x32x16_bf16 v[144:159], v[132:135], v[180:183], v[144:159]\n\t"
    "ds_read_b64_tr_b16 v[176:177], v200 offset:0\n\t"
    "ds_read_b64_tr_b16 v[178:179], v200 offset:512\n\t"
    "v_add_u32_e32 v203, s52, v203\n\t"
    "s_waitcnt lgkmcnt(5)\n\t"
    "v_mfma_f32_32x32x16_bf16 v[144:159], v[136:139], v[184:187], v[144:159]\n\t"
    "ds_read_b64_tr_b16 v[180:181], v200 offset:2048\n\t"
    "ds_read_b64_tr_b16 v[182:183], v200 offset:2560\n\t"
    "s_waitcnt lgkmcnt(5)\n\t"
    "v_mfma_f32_32x32x16_bf16 v[160:175], v[140:143], v[188:191], v[144:159]\n\t"
    "ds_read_b64_tr_b16 v[184:185], v200 offset:4096\n\t"
    "ds_read_b64_tr_b16 v[186:187], v200 offset:4608\n\t"
    "s_sub_u32 s52, 0, s52\n\t"
    "s_mov_b32 s46, s50\n\t"
    "s_mov_b32 s50, s51\n\t"
    "s_mov_b32 s51, s46\n\t"
    "s_add_u32 s40, s40, 128\n\t"
    "s_addc_u32 s41, s41, 0\n\t"
    "s_lshr_b64 s[58:59], s[58:59], 8\n\t"
    "s_add_u32 s62, s62, 1\n\t"
    "s_add_u32 s63, s63, 64\n\t"
    "s_waitcnt lgkmcnt(4)\n\t"
    "v_mfma_f32_32x32x16_bf16 v[144:159], v[0:3], v[176:179], 0\n\t"
    "ds_read_b64_tr_b16 v[188:189], v201 offset:0\n\t"
    "ds_read_b64_tr_b16 v[190:191], v201 offset:512\n\t"
    "s_waitcnt lgkmcnt(4)\n\t"
    "v_mfma_f32_32x32x16_bf16 v[144:159], v[4:7], v[180:183], v[144:159]\n\t"
    "ds_read_b64_tr_b16 v[176:177], v201 offset:2048\n\t"
    "ds_read_b64_tr_b16 v[178:179], v201 offset:2560\n\t"
    "v_add_u32_e32 v218, s63, v228\n\t"
    "v_mul_u32_u24_e32 v219, 0xe39, v218\n\t"
    "v_lshl_add_u32 v218, v218, 4, v227\n\t"
    "v_lshrrev_b32_e32 v219, 16, v219\n\t"
    "v_mul_u32_u24_e32 v219, 159456, v219\n\t"
    "v_add_u32_e32 v218, v218, v219\n\t"
    "s_waitcnt lgkmcnt(4)\n\t"
    "v_mfma_f32_32x32x16_bf16 v[144:159], v[8:11], v[184:187], v[144:159]\n\t"
    "ds_read_b64_tr_b16 v[180:181], v201 offset:4096\n\t"
    "ds_read_b64_tr_b16 v[182:183], v201 offset:4608\n\t"
    "v_cvt_pk_bf16_f32 v212, v160, v161\n\t"
    "s_waitcnt lgkmcnt(4)\n\t"
    "v_mfma_f32_32x32x16_bf16 v[144:159], v[12:15], v[188:191], v[144:159]\n\t"
    "ds_read_b64_tr_b16 v[184:185], v202 offset:0\n\t"
    "ds_read_b64_tr_b16 v[186:187], v202 offset:512\n\t"
    "v_cvt_pk_bf16_f32 v213, v162, v163\n\t"
    "s_waitcnt lgkmcnt(4)\n\t"
    "v_mfma_f32_32x32x16_bf16 v[144:159], v[16:19], v[176:179], v[144:159]\n\t"
    "ds_read_b64_tr_b16 v[188:189], v202 offset:2048\n\t"
    "ds_read_b64_tr_b16 v[190:191], v202 offset:2560\n\t"
    "v_cvt_pk_bf16_f32 v214, v164, v165\n\t"
    "s_waitcnt lgkmcnt(4)\n\t"
    "v_mfma_f32_32x32x16_bf16 v[144:159], v[20:23], v[180:183], v[144:159]\n\t"
    "ds_read_b64_tr_b16 v[176:177], v202 offset:4096\n\t"
    "ds_read_b64_tr_b16 v[178:179], v202 offset:4608\n\t"
    "v_cvt_pk_bf16_f32 v215, v166, v167\n\t"
    "global_store_dwordx4 v218, v[212:215], s[44:45]\n\t"
    "s_waitcnt lgkmcnt(4)\n\t"
    "v_mfma_f32_32x32x16_bf16 v[144:159], v[24:27], v[184:187], v[144:159]\n\t"
    "ds_read_b64_tr_b16 v[180:181], v203 offset:0\n\t"
    "ds_read_b64_tr_b16 v[182:183], v203 offset:512\n\t"
    "s_waitcnt vmcnt(6)\n\t"
    "s_barrier\n\t"
    "v_cvt_pk_bf16_f32 v208, v168, v169\n\t"
    "s_waitcnt lgkmcnt(4)\n\t"
    "v_mfma_f32_32x32x16_bf16 v[144:159], v[28:31], v[188:191], v[144:159]\n\t"
    "ds_read_b64_tr_b16 v[184:185], v203 offset:2048\n\t"
    "ds_read_b64_tr_b16 v[186:187], v203 offset:2560\n\t"
    "v_cvt_pk_bf16_f32 v209, v170, v171\n\t"
    "s_waitcnt lgkmcnt(4)\n\t"
    "v_mfma_f32_32x32x16_bf16 v[144:159], v[32:35], v[176:179], v[144:159]\n\t"
    "ds_read_b64_tr_b16 v[188:189], v203 offset:4096\n\t"
    "ds_read_b64_tr_b16 v[190:191], v203 offset:4608\n\t"
    "v_cvt_pk_bf16_f32 v210, v172, v173\n\t"
    "s_waitcnt lgkmcnt(4)\n\t"
    "v_mfma_f32_32x32x16_bf16 v[144:159], v[36:39], v[180:183], v[144:159]\n\t"
    "ds_read_b64_tr_b16 v[176:177], v200 offset:24576\n\t"
    "ds_read_b64_tr_b16 v[178:179], v200 offset:25088\n\t"
    "v_cvt_pk_bf16_f32 v211, v174, v175\n\t"
    "global_store_dwordx4 v218, v[208:211], s[44:45] offset:672\n\t"
    "s_waitcnt lgkmcnt(4)\n\t"
    "v_mfma_f32_32x32x16_bf16 v[144:159], v[40:43], v[184:187], v[144:159]\n\t"
    "ds_read_b64_tr_b16 v[180:181], v200 offset:26624\n\t"
    "ds_read_b64_tr_b16 v[182:183], v200 offset:27136\n\t"
    "s_waitcnt lgkmcnt(4)\n\t"
    "v_mfma_f32_32x32x16_bf16 v[144:159], v[44:47], v[188:191], v[144:159]\n\t"
    "ds_read_b64_tr_b16 v[184:185], v200 offset:28672\n\t"
    "ds_read_b64_tr_b16 v[186:187], v200 offset:29184\n\t"
    "s_waitcnt lgkmcnt(4)\n\t"
    "v_mfma_f32_32x32x16_bf16 v[144:159], v[48:51], v[176:179], v[144:159]\n\t"
    "ds_read_b64_tr_b16 v[188:189], v201 offset:24576\n\t"
    "ds_read_b64_tr_b16 v[190:191], v201 offset:25088\n\t"
    "s_waitcnt lgkmcnt(4)\n\t"
    "v_mfma_f32_32x32x16_bf16 v[144:159], v[52:55], v[180:183], v[144:159]\n\t"
    "ds_read_b64_tr_b16 v[176:177], v201 offset:26624\n\t"
    "ds_read_b64_tr_b16 v[178:179], v201 offset:27136\n\t"
    "s_waitcnt lgkmcnt(4)\n\t"
    "v_mfma_f32_32x32x16_bf16 v[144:159], v[56:59], v[184:187], v[144:159]\n\t"
    "ds_read_b64_tr_b16 v[180:181], v201 offset:28672\n\t"
    "ds_read_b64_tr_b16 v[182:183], v201 offset:29184\n\t"
    "s_waitcnt lgkmcnt(4)\n\t"
    "v_mfma_f32_32x32x16_bf16 v[144:159], v[60:63], v[188:191], v[144:159]\n\t"
    "ds_read_b64_tr_b16 v[184:185], v202 offset:24576\n\t"
    "ds_read_b64_tr_b16 v[186:187], v202 offset:25088\n\t"
    "s_waitcnt lgkmcnt(4)\n\t"
    "v_mfma_f32_32x32x16_bf16 v[144:159], v[64:67], v[176:179], v[144:159]\n\t"
    "ds_read_b64_tr_b16 v[188:189], v202 offset:26624\n\t"
    "ds_read_b64_tr_b16 v[190:191], v202 offset:27136\n\t"
    "s_waitcnt lgkmcnt(4)\n\t"
    "v_mfma_f32_32x32x16_bf16 v[144:159], v[68:71], v[180:183], v[144:159]\n\t"
    "ds_read_b64_tr_b16 v[176:177], v202 offset:28672\n\t"
    "ds_read_b64_tr_b16 v[178:179], v202 offset:29184\n\t"
    "s_waitcnt lgkmcnt(4)\n\t"
    "v_mfma_f32_32x32x16_bf16 v[144:159], v[72:75], v[184:187], v[144:159]\n\t"
    "ds_read_b64_tr_b16 v[180:181], v203 offset:24576\n\t"
    "ds_read_b64_tr_b16 v[182:183], v203 offset:25088\n\t"
    "s_waitcnt vmcnt(2)\n\t"
    "s_barrier\n\t"
    "s_waitcnt lgkmcnt(4)\n\t"
    "v_mfma_f32_32x32x16_bf16 v[144:159], v[76:79], v[188:191], v[144:159]\n\t"
    "ds_read_b64_tr_b16 v[184:185], v203 offset:26624\n\t"
    "ds_read_b64_tr_b16 v[186:187], v203 offset:27136\n\t"
    "s_waitcnt lgkmcnt(4)\n\t"
    "v_mfma_f32_32x32x16_bf16 v[144:159], v[80:83], v[176:179], v[144:159]\n\t"
    "ds_read_b64_tr_b16 v[188:189], v203 offset:28672\n\t"
    "ds_read_b64_tr_b16 v[190:191], v203 offset:29184\n\t"
    "s_waitcnt lgkmcnt(4)\n\t"
    "v_mfma_f32_32x32x16_bf16 v[144:159], v[84:87], v[180:183], v[144:159]\n\t"
    "ds_read_b64_tr_b16 v[176:177], v200 offset:49152\n\t"
    "ds_read_b64_tr_b16 v[178:179], v200 offset:49664\n\t"
    "s_waitcnt lgkmcnt(4)\n\t"
    "v_mfma_f32_32x32x16_bf16 v[144:159], v[88:91], v[184:187], v[144:159]\n\t"
    "ds_read_b64_tr_b16 v[180:181], v200 offset:51200\n\t"
    "ds_read_b64_tr_b16 v[182:183], v200 offset:51712\n\t"
    "s_waitcnt lgkmcnt(4)\n\t"
    "v_mfma_f32_32x32x16_bf16 v[144:159], v[92:95], v[188:191], v[144:159]\n\t"
    "ds_read_b64_tr_b16 v[184:185], v200 offset:53248\n\t"
    "ds_read_b64_tr_b16 v[186:187], v200 offset:53760\n\t"
    "s_waitcnt lgkmcnt(4)\n\t"
    "v_mfma_f32_32x32x16_bf16 v[144:159], v[96:99], v[176:179], v[144:159]\n\t"
    "ds_read_b64_tr_b16 v[188:189], v201 offset:49152\n\t"
    "ds_read_b64_tr_b16 v[190:191], v201 offset:49664\n\t"
    "s_waitcnt lgkmcnt(4)\n\t"
    "v_mfma_f32_32x32x16_bf16 v[144:159], v[100:103], v[180:183], v[144:159]\n\t"
    "ds_read_b64_tr_b16 v[176:177], v201 offset:51200\n\t"
    "ds_read_b64_tr_b16 v[178:179], v201 offset:51712\n\t"
    "s_waitcnt lgkmcnt(4)\n\t"
    "v_mfma_f32_32x32x16_bf16 v[144:159], v[104:107], v[184:187], v[144:159]\n\t"
    "ds_read_b64_tr_b16 v[180:181], v201 offset:53248\n\t"
    "ds_read_b64_tr_b16 v[182:183], v201 offset:53760\n\t"
    "s_waitcnt lgkmcnt(4)\n\t"
    "v_mfma_f32_32x32x16_bf16 v[144:159], v[108:111], v[188:191], v[144:159]\n\t"
    "ds_read_b64_tr_b16 v[184:185], v202 offset:49152\n\t"
    "ds_read_b64_tr_b16 v[186:187], v202 offset:49664\n\t"
    "s_waitcnt lgkmcnt(4)\n\t"
    "v_mfma_f32_32x32x16_bf16 v[144:159], v[112:115], v[176:179], v[144:159]\n\t"
    "ds_read_b64_tr_b16 v[188:189], v202 offset:51200\n\t"
    "ds_read_b64_tr_b16 v[190:191], v202 offset:51712\n\t"
    "s_waitcnt lgkmcnt(4)\n\t"
    "v_mfma_f32_32x32x16_bf16 v[144:159], v[116:119], v[180:183], v[144:159]\n\t"
    "ds_read_b64_tr_b16 v[176:177], v202 offset:53248\n\t"
    "ds_read_b64_tr_b16 v[178:179], v202 offset:53760\n\t"
    "s_waitcnt lgkmcnt(4)\n\t"
    "v_mfma_f32_32x32x16_bf16 v[144:159], v[120:123], v[184:187], v[144:159]\n\t"
    "ds_read_b64_tr_b16 v[180:181], v203 offset:49152\n\t"
    "ds_read_b64_tr_b16 v[182:183], v203 offset:49664\n\t"
    "v_add_u32_e32 v226, 32, v226\n\t"
    "s_waitcnt lgkmcnt(4)\n\t"
    "v_mfma_f32_32x32x16_bf16 v[144:159], v[124:127], v[188:191], v[144:159]\n\t"
    "ds_read_b64_tr_b16 v[184:185], v203 offset:51200\n\t"
    "ds_read_b64_tr_b16 v[186:187], v203 offset:51712\n\t"
    "s_waitcnt lgkmcnt(4)\n\t"
    "v_mfma_f32_32x32x16_bf16 v[144:159], v[128:131], v[176:179], v[144:159]\n\t"
    "ds_read_b64_tr_b16 v[188:189], v203 offset:53248\n\t"
    "ds_read_b64_tr_b16 v[190:191], v203 offset:53760\n\t"
    "ds_read_b128 v[208:211], v226\n\t"
    "s_waitcnt lgkmcnt(5)\n\t"
    "v_mfma_f32_32x32x16_bf16 v[144:159], v[132:135], v[180:183], v[144:159]\n\t"
    "s_waitcnt lgkmcnt(3)\n\t"
    "v_mfma_f32_32x32x16_bf16 v[144:159], v[136:139], v[184:187], v[144:159]\n\t"
    "s_waitcnt lgkmcnt(1)\n\t"
    "v_mfma_f32_32x32x16_bf16 v[160:175], v[140:143], v[188:191], v[144:159]\n\t"
    "s_sub_u32 s52, 0, s52\n\t"
    "s_mov_b32 s46, s50\n\t"
    "s_mov_b32 s50, s51\n\t"
    "s_mov_b32 s51, s46\n\t"
    "s_add_u32 s40, s40, 128\n\t"
    "s_addc_u32 s41, s41, 0\n\t"
    "s_lshr_b64 s[58:59], s[58:59], 8\n\t"
    "s_add_u32 s62, s62, 1\n\t"
    "s_add_u32 s63, s63, 64\n\t"
    "v_lshlrev_b32_e32 v0, 2, v207\n\t"
    "global_load_dword v1, v0, s[38:39]\n\t"
    "s_nop 11\n\t"
    "v_add_u32_e32 v218, s63, v228\n\t"
    "v_mul_u32_u24_e32 v219, 0xe39, v218\n\t"
    "v_lshl_add_u32 v218, v218, 4, v227\n\t"
    "v_lshrrev_b32_e32 v219, 16, v219\n\t"
    "v_mul_u32_u24_e32 v219, 159456, v219\n\t"
    "v_add_u32_e32 v218, v218, v219\n\t"
    "v_cvt_pk_bf16_f32 v212, v160, v161\n\t"
    "v_cvt_pk_bf16_f32 v213, v162, v163\n\t"
    "v_cvt_pk_bf16_f32 v214, v164, v165\n\t"
    "v_cvt_pk_bf16_f32 v215, v166, v167\n\t"
    "global_store_dwordx4 v218, v[212:215], s[44:45]\n\t"
    "v_cvt_pk_bf16_f32 v208, v168, v169\n\t"
    "v_cvt_pk_bf16_f32 v209, v170, v171\n\t"
    "v_cvt_pk_bf16_f32 v210, v172, v173\n\t"
    "v_cvt_pk_bf16_f32 v211, v174, v175\n\t"
    "global_store_dwordx4 v218, v[208:211], s[44:45] offset:672\n\t"
    "s_waitcnt lgkmcnt(0)\n\t"
    "s_waitcnt vmcnt(2)\n\t"
    "s_branch .Lk1e%=\n\t"
    ".Lk1t0%=:\n\t"
    "s_bitcmp1_b32 s58, 0\n\t"
    "s_cselect_b32 s48, -1, 0\n\t"
    "s_cselect_b32 s49, 0, -1\n\t"
    "s_not_b64 exec, s[48:49]\n\t"
    "global_store_dwordx2 v218, v[216:217], s[44:45]\n\t"
    "s_mov_b64 exec, s[48:49]\n\t"
    "v_add_u32_e32 v218, v208, v225\n\t"
    "v_add_u32_e32 v219, s61, v218\n\t"
    "global_store_dword v218, v216, s[44:45]\n\t"
    "global_store_dword v219, v217, s[44:45]\n\t"
    "s_mov_b64 exec, -1\n\t"
    "s_branch .Lk1b0%=\n\t"
    ".Lk1t1%=:\n\t"
    "s_bitcmp1_b32 s58, 2\n\t"
    "s_cselect_b32 s48, -1, 0\n\t"
    "s_cselect_b32 s49, 0, -1\n\t"
    "s_not_b64 exec, s[48:49]\n\t"
    "global_store_dwordx2 v218, v[216:217], s[44:45]\n\t"
    "s_mov_b64 exec, s[48:49]\n\t"
    "v_add_u32_e32 v218, v209, v225\n\t"
    "v_add_u32_e32 v219, s61, v218\n\t"
    "global_store_dword v218, v216, s[44:45]\n\t"
    "global_store_dword v219, v217, s[44:45]\n\t"
    "s_mov_b64 exec, -1\n\t"
    "s_branch .Lk1b1%=\n\t"
    ".Lk1t2%=:\n\t"
    "s_bitcmp1_b32 s58, 4\n\t"
    "s_cselect_b32 s48, -1, 0\n\t"
    "s_cselect_b32 s49, 0, -1\n\t"
    "s_not_b64 exec, s[48:49]\n\t"
    "global_store_dwordx2 v218, v[216:217], s[44:45]\n\t"
    "s_mov_b64 exec, s[48:49]\n\t"
    "v_add_u32_e32 v218, v210, v225\n\t"
    "v_add_u32_e32 v219, s61, v218\n\t"
    "global_store_dword v218, v216, s[44:45]\n\t"
    "global_store_dword v219, v217, s[44:45]\n\t"
    "s_mov_b64 exec, -1\n\t"
    "s_branch .Lk1b2%=\n\t"
    ".Lk1t3%=:\n\t"
    "s_bitcmp1_b32 s58, 6\n\t"
    "s_cselect_b32 s48, -1, 0\n\t"
    "s_cselect_b32 s49, 0, -1\n\t"
    "s_not_b64 exec, s[48:49]\n\t"
    "global_store_dwordx2 v218, v[216:217], s[44:45]\n\t"
    "s_mov_b64 exec, s[48:49]\n\t"
    "v_add_u32_e32 v218, v211, v225\n\t"
    "v_add_u32_e32 v219, s61, v218\n\t"
    "global_store_dword v218, v216, s[44:45]\n\t"
    "global_store_dword v219, v217, s[44:45]\n\t"
    "s_mov_b64 exec, -1\n\t"
    "s_branch .Lk1b3%=\n\t"
    ".Lk1t4%=:\n\t"
    "s_bitcmp1_b32 s58, 0\n\t"
    "s_cselect_b32 s48, -1, 0\n\t"
    "s_cselect_b32 s49, 0, -1\n\t"
    "s_not_b64 exec, s[48:49]\n\t"
    "global_store_dwordx2 v218, v[216:217], s[44:45]\n\t"
    "s_mov_b64 exec, s[48:49]\n\t"
    "v_add_u32_e32 v218, v208, v225\n\t"
    "v_add_u32_e32 v219, s61, v218\n\t"
    "global_store_dword v218, v216, s[44:45]\n\t"
    "global_store_dword v219, v217, s[44:45]\n\t"
    "s_mov_b64 exec, -1\n\t"
    "s_branch .Lk1b4%=\n\t"
    ".Lk1t5%=:\n\t"
    "s_bitcmp1_b32 s58, 2\n\t"
    "s_cselect_b32 s48, -1, 0\n\t"
    "s_cselect_b32 s49, 0, -1\n\t"
    "s_not_b64 exec, s[48:49]\n\t"
    "global_store_dwordx2 v218, v[216:217], s[44:45]\n\t"
    "s_mov_b64 exec, s[48:49]\n\t"
    "v_add_u32_e32 v218, v209, v225\n\t"
    "v_add_u32_e32 v219, s61, v218\n\t"
    "global_store_dword v218, v216, s[44:45]\n\t"
    "global_store_dword v219, v217, s[44:45]\n\t"
    "s_mov_b64 exec, -1\n\t"
    "s_branch .Lk1b5%=\n\t"
    ".Lk1t6%=:\n\t"
    "s_bitcmp1_b32 s58, 4\n\t"
    "s_cselect_b32 s48, -1, 0\n\t"
    "s_cselect_b32 s49, 0, -1\n\t"
    "s_not_b64 exec, s[48:49]\n\t"
    "global_store_dwordx2 v218, v[216:217], s[44:45]\n\t"
    "s_mov_b64 exec, s[48:49]\n\t"
    "v_add_u32_e32 v218, v210, v225\n\t"
    "v_add_u32_e32 v219, s61, v218\n\t"
    "global_store_dword v218, v216, s[44:45]\n\t"
    "global_store_dword v219, v217, s[44:45]\n\t"
    "s_mov_b64 exec, -1\n\t"
    "s_branch .Lk1b6%=\n\t"
    ".Lk1t7%=:\n\t"
    "s_bitcmp1_b32 s58, 6\n\t"
    "s_cselect_b32 s48, -1, 0\n\t"
    "s_cselect_b32 s49, 0, -1\n\t"
    "s_not_b64 exec, s[48:49]\n\t"
    "global_store_dwordx2 v218, v[216:217], s[44:45]\n\t"
    "s_mov_b64 exec, s[48:49]\n\t"
    "v_add_u32_e32 v218, v211, v225\n\t"
    "v_add_u32_e32 v219, s61, v218\n\t"
    "global_store_dword v218, v216, s[44:45]\n\t"
    "global_store_dword v219, v217, s[44:45]\n\t"
    "s_mov_b64 exec, -1\n\t"
    "s_branch .Lk1b7%=\n\t"
    ".Lk1t8%=:\n\t"
    "s_bitcmp1_b32 s58, 0\n\t"
    "s_cselect_b32 s48, -1, 0\n\t"
    "s_cselect_b32 s49, 0, -1\n\t"
    "s_not_b64 exec, s[48:49]\n\t"
    "global_store_dwordx2 v218, v[216:217], s[44:45]\n\t"
    "s_mov_b64 exec, s[48:49]\n\t"
    "v_add_u32_e32 v218, v208, v225\n\t"
    "v_add_u32_e32 v219, s61, v218\n\t"
    "global_store_dword v218, v216, s[44:45]\n\t"
    "global_store_dword v219, v217, s[44:45]\n\t"
    "s_mov_b64 exec, -1\n\t"
    "s_branch .Lk1b8%=\n\t"
    ".Lk1t9%=:\n\t"
    "s_bitcmp1_b32 s58, 2\n\t"
    "s_cselect_b32 s48, -1, 0\n\t"
    "s_cselect_b32 s49, 0, -1\n\t"
    "s_not_b64 exec, s[48:49]\n\t"
    "global_store_dwordx2 v218, v[216:217], s[44:45]\n\t"
    "s_mov_b64 exec, s[48:49]\n\t"
    "v_add_u32_e32 v218, v209, v225\n\t"
    "v_add_u32_e32 v219, s61, v218\n\t"
    "global_store_dword v218, v216, s[44:45]\n\t"
    "global_store_dword v219, v217, s[44:45]\n\t"
    "s_mov_b64 exec, -1\n\t"
    "s_branch .Lk1b9%=\n\t"
    ".Lk1t10%=:\n\t"
    "s_bitcmp1_b32 s58, 4\n\t"
    "s_cselect_b32 s48, -1, 0\n\t"
    "s_cselect_b32 s49, 0, -1\n\t"
    "s_not_b64 exec, s[48:49]\n\t"
    "global_store_dwordx2 v218, v[216:217], s[44:45]\n\t"
    "s_mov_b64 exec, s[48:49]\n\t"
    "v_add_u32_e32 v218, v210, v225\n\t"
    "v_add_u32_e32 v219, s61, v218\n\t"
    "global_store_dword v218, v216, s[44:45]\n\t"
    "global_store_dword v219, v217, s[44:45]\n\t"
    "s_mov_b64 exec, -1\n\t"
    "s_branch .Lk1b10%=\n\t"
    ".Lk1t11%=:\n\t"
    "s_bitcmp1_b32 s58, 6\n\t"
    "s_cselect_b32 s48, -1, 0\n\t"
    "s_cselect_b32 s49, 0, -1\n\t"
    "s_not_b64 exec, s[48:49]\n\t"
    "global_store_dwordx2 v218, v[216:217], s[44:45]\n\t"
    "s_mov_b64 exec, s[48:49]\n\t"
    "v_add_u32_e32 v218, v211, v225\n\t"
    "v_add_u32_e32 v219, s61, v218\n\t"
    "global_store_dword v218, v216, s[44:45]\n\t"
    "global_store_dword v219, v217, s[44:45]\n\t"
    "s_mov_b64 exec, -1\n\t"
    "s_branch .Lk1b11%=\n\t"
    ".Lk1t12%=:\n\t"
    "s_bitcmp1_b32 s58, 0\n\t"
    "s_cselect_b32 s48, -1, 0\n\t"
    "s_cselect_b32 s49, 0, -1\n\t"
    "s_not_b64 exec, s[48:49]\n\t"
    "global_store_dwordx2 v218, v[216:217], s[44:45]\n\t"
    "s_mov_b64 exec, s[48:49]\n\t"
    "v_add_u32_e32 v218, v208, v225\n\t"
    "v_add_u32_e32 v219, s61, v218\n\t"
    "global_store_dword v218, v216, s[44:45]\n\t"
    "global_store_dword v219, v217, s[44:45]\n\t"
    "s_mov_b64 exec, -1\n\t"
    "s_branch .Lk1b12%=\n\t"
    ".Lk1t13%=:\n\t"
    "s_bitcmp1_b32 s58, 2\n\t"
    "s_cselect_b32 s48, -1, 0\n\t"
    "s_cselect_b32 s49, 0, -1\n\t"
    "s_not_b64 exec, s[48:49]\n\t"
    "global_store_dwordx2 v218, v[216:217], s[44:45]\n\t"
    "s_mov_b64 exec, s[48:49]\n\t"
    "v_add_u32_e32 v218, v209, v225\n\t"
    "v_add_u32_e32 v219, s61, v218\n\t"
    "global_store_dword v218, v216, s[44:45]\n\t"
    "global_store_dword v219, v217, s[44:45]\n\t"
    "s_mov_b64 exec, -1\n\t"
    "s_branch .Lk1b13%=\n\t"
    ".Lk1t14%=:\n\t"
    "s_bitcmp1_b32 s58, 4\n\t"
    "s_cselect_b32 s48, -1, 0\n\t"
    "s_cselect_b32 s49, 0, -1\n\t"
    "s_not_b64 exec, s[48:49]\n\t"
    "global_store_dwordx2 v218, v[216:217], s[44:45]\n\t"
    "s_mov_b64 exec, s[48:49]\n\t"
    "v_add_u32_e32 v218, v210, v225\n\t"
    "v_add_u32_e32 v219, s61, v218\n\t"
    "global_store_dword v218, v216, s[44:45]\n\t"
    "global_store_dword v219, v217, s[44:45]\n\t"
    "s_mov_b64 exec, -1\n\t"
    "s_branch .Lk1b14%=\n\t"
    ".Lk1t15%=:\n\t"
    "s_bitcmp1_b32 s58, 6\n\t"
    "s_cselect_b32 s48, -1, 0\n\t"
    "s_cselect_b32 s49, 0, -1\n\t"
    "s_not_b64 exec, s[48:49]\n\t"
    "global_store_dwordx2 v218, v[216:217], s[44:45]\n\t"
    "s_mov_b64 exec, s[48:49]\n\t"
    "v_add_u32_e32 v218, v211, v225\n\t"
    "v_add_u32_e32 v219, s61, v218\n\t"
    "global_store_dword v218, v216, s[44:45]\n\t"
    "global_store_dword v219, v217, s[44:45]\n\t"
    "s_mov_b64 exec, -1\n\t"
    "s_branch .Lk1b15%=\n\t"
    ".Lk1t16%=:\n\t"
    "s_bitcmp1_b32 s58, 0\n\t"
    "s_cselect_b32 s48, -1, 0\n\t"
    "s_cselect_b32 s49, 0, -1\n\t"
    "s_not_b64 exec, s[48:49]\n\t"
    "global_store_dwordx2 v218, v[216:217], s[44:45]\n\t"
    "s_mov_b64 exec, s[48:49]\n\t"
    "v_add_u32_e32 v218, v208, v225\n\t"
    "v_add_u32_e32 v219, s61, v218\n\t"
    "global_store_dword v218, v216, s[44:45]\n\t"
    "global_store_dword v219, v217, s[44:45]\n\t"
    "s_mov_b64 exec, -1\n\t"
    "s_branch .Lk1b16%=\n\t"
    ".Lk1t17%=:\n\t"
    "s_bitcmp1_b32 s58, 2\n\t"
    "s_cselect_b32 s48, -1, 0\n\t"
    "s_cselect_b32 s49, 0, -1\n\t"
    "s_not_b64 exec, s[48:49]\n\t"
    "global_store_dwordx2 v218, v[216:217], s[44:45]\n\t"
    "s_mov_b64 exec, s[48:49]\n\t"
    "v_add_u32_e32 v218, v209, v225\n\t"
    "v_add_u32_e32 v219, s61, v218\n\t"
    "global_store_dword v218, v216, s[44:45]\n\t"
    "global_store_dword v219, v217, s[44:45]\n\t"
    "s_mov_b64 exec, -1\n\t"
    "s_branch .Lk1b17%=\n\t"
    ".Lk1t18%=:\n\t"
    "s_bitcmp1_b32 s58, 4\n\t"
    "s_cselect_b32 s48, -1, 0\n\t"
    "s_cselect_b32 s49, 0, -1\n\t"
    "s_not_b64 exec, s[48:49]\n\t"
    "global_store_dwordx2 v218, v[216:217], s[44:45]\n\t"
    "s_mov_b64 exec, s[48:49]\n\t"
    "v_add_u32_e32 v218, v210, v225\n\t"
    "v_add_u32_e32 v219, s61, v218\n\t"
    "global_store_dword v218, v216, s[44:45]\n\t"
    "global_store_dword v219, v217, s[44:45]\n\t"
    "s_mov_b64 exec, -1\n\t"
    "s_branch .Lk1b18%=\n\t"
    ".Lk1t19%=:\n\t"
    "s_bitcmp1_b32 s58, 6\n\t"
    "s_cselect_b32 s48, -1, 0\n\t"
    "s_cselect_b32 s49, 0, -1\n\t"
    "s_not_b64 exec, s[48:49]\n\t"
    "global_store_dwordx2 v218, v[216:217], s[44:45]\n\t"
    "s_mov_b64 exec, s[48:49]\n\t"
    "v_add_u32_e32 v218, v211, v225\n\t"
    "v_add_u32_e32 v219, s61, v218\n\t"
    "global_store_dword v218, v216, s[44:45]\n\t"
    "global_store_dword v219, v217, s[44:45]\n\t"
    "s_mov_b64 exec, -1\n\t"
    "s_branch .Lk1b19%=\n\t"
    ".Lk1t20%=:\n\t"
    "s_bitcmp1_b32 s58, 0\n\t"
    "s_cselect_b32 s48, -1, 0\n\t"
    "s_cselect_b32 s49, 0, -1\n\t"
    "s_not_b64 exec, s[48:49]\n\t"
    "global_store_dwordx2 v218, v[216:217], s[44:45]\n\t"
    "s_mov_b64 exec, s[48:49]\n\t"
    "v_add_u32_e32 v218, v208, v225\n\t"
    "v_add_u32_e32 v219, s61, v218\n\t"
    "global_store_dword v218, v216, s[44:45]\n\t"
    "global_store_dword v219, v217, s[44:45]\n\t"
    "s_mov_b64 exec, -1\n\t"
    "s_branch .Lk1b20%=\n\t"
    ".Lk1t21%=:\n\t"
    "s_bitcmp1_b32 s58, 2\n\t"
    "s_cselect_b32 s48, -1, 0\n\t"
    "s_cselect_b32 s49, 0, -1\n\t"
    "s_not_b64 exec, s[48:49]\n\t"
    "global_store_dwordx2 v218, v[216:217], s[44:45]\n\t"
    "s_mov_b64 exec, s[48:49]\n\t"
    "v_add_u32_e32 v218, v209, v225\n\t"
    "v_add_u32_e32 v219, s61, v218\n\t"
    "global_store_dword v218, v216, s[44:45]\n\t"
    "global_store_dword v219, v217, s[44:45]\n\t"
    "s_mov_b64 exec, -1\n\t"
    "s_branch .Lk1b21%=\n\t"
    ".Lk1t22%=:\n\t"
    "s_bitcmp1_b32 s58, 4\n\t"
    "s_cselect_b32 s48, -1, 0\n\t"
    "s_cselect_b32 s49, 0, -1\n\t"
    "s_not_b64 exec, s[48:49]\n\t"
    "global_store_dwordx2 v218, v[216:217], s[44:45]\n\t"
    "s_mov_b64 exec, s[48:49]\n\t"
    "v_add_u32_e32 v218, v210, v225\n\t"
    "v_add_u32_e32 v219, s61, v218\n\t"
    "global_store_dword v218, v216, s[44:45]\n\t"
    "global_store_dword v219, v217, s[44:45]\n\t"
    "s_mov_b64 exec, -1\n\t"
    "s_branch .Lk1b22%=\n\t"
    ".Lk1t23%=:\n\t"
    "s_bitcmp1_b32 s58, 6\n\t"
    "s_cselect_b32 s48, -1, 0\n\t"
    "s_cselect_b32 s49, 0, -1\n\t"
    "s_not_b64 exec, s[48:49]\n\t"
    "global_store_dwordx2 v218, v[216:217], s[44:45]\n\t"
    "s_mov_b64 exec, s[48:49]\n\t"
    "v_add_u32_e32 v218, v211, v225\n\t"
    "v_add_u32_e32 v219, s61, v218\n\t"
    "global_store_dword v218, v216, s[44:45]\n\t"
    "global_store_dword v219, v217, s[44:45]\n\t"
    "s_mov_b64 exec, -1\n\t"
    "s_branch .Lk1b23%=\n\t"
    ".Lk1e%=:\n\t"
      : "+{v[160:175]}"(ad), "+{v[200:207]}"(vs), "+{v[224:231]}"(qv), "={v[232:239]}"(ga),
        "+{s[40:41]}"(wb), "+{s48}"(sex), "+{s50}"(smp), "+{s51}"(smo), "+{s52}"(sdl), "+{s[58:59]}"(tm), "+{s62}"(sgs), "+{s63}"(scol) K1_TSOUT
      : "{v[248:251]}"(wvk), "{s[42:43]}"(xb), "{s[36:37]}"(xc), "{s[38:39]}"(pfa), "{s[44:45]}"(ws), "{s56}"(grp)
      : K1_CLOB, "v208", "v209", "v210", "v211", "v212", "v213", "v214", "v215", "v216", "v217", "v218", "v219",
        "v240", "v241", "v242", "v243", "v244", "v245", "v246", "v247", "s60", "s61");
    if (grp == 3) {
      const unsigned ln = vs[7] >> 4, t2 = wid * 64 + ln;
      const float gv = addx32(ga[0] + ga[4]);
      if (ln < 32) ((float*)(ws + WS_G))[wn * S + rb * 128 + wm * 32 + ln] = gv;
      u32x4* vf = (u32x4*)(ws + WS_KV + (size_t)(t2 >> 4) * KV_HEAD + rb * KV_GRP + ((t2 >> 2) & 3) * KV_T + 1024 + (t2 & 3) * 336 + 288);
      vf[0] = u32x4{0x3f803f80u, 0x3f803f80u, 0x3f803f80u, 0x3f803f80u};
      vf[1] = u32x4{0u, 0u, 0u, 0u};
      vf[2] = u32x4{0u, 0u, 0u, 0u};
      if (t2 < (SY_F + 2048) / 256) *(u32x4*)(ws + WS_SY + rb * ((SY_F + 2048) / 16) + t2 * 16) = u32x4{0u, 0u, 0u, 0u};
    }
  }
  TS(0, 6);
}

__global__ __launch_bounds__(512, 2) void k1_kernel(const uint16_t* __restrict__ x, const uint16_t* __restrict__ wqkv,
                                                    const uint16_t* __restrict__ w1, const uint16_t* __restrict__ b1,
                                                    const uint16_t* __restrict__ gate_w, char* __restrict__ ws) {
  extern __shared__ __attribute__((aligned(16))) char smem[];
  const int b = blockIdx.x, xcd = b & 7, idx = b >> 3;
  const int rb = (xcd >> 1) * 4 + (idx & 3), gi = idx >> 2, rot = ((idx & 3) + gi) % 3;
  if (gi < 6) k1_body<true>(x, w1, b1, gate_w, ws, smem, (xcd & 1) * 6 + gi, rb, rot);
  else k1_body<false>(x, wqkv, b1, gate_w, ws, smem, (xcd & 1) * 2 + gi - 6, rb, rot);
  asm volatile("s_mov_b64 exec, 0\n\t"
               "s_cmp_eq_u32 %0, 0\n\ts_cbranch_scc1 .Lpf_s0\n\ts_cmp_eq_u32 %0, 1\n\ts_cbranch_scc1 .Lpf_s1\n\t"
               "s_cmp_eq_u32 %0, 2\n\ts_cbranch_scc1 .Lpf_s2\n\ts_cmp_eq_u32 %0, 3\n\ts_cbranch_scc1 .Lpf_s3\n\t"
               "s_cmp_eq_u32 %0, 4\n\ts_cbranch_scc1 .Lpf_s4\n\ts_cmp_eq_u32 %0, 5\n\ts_cbranch_scc1 .Lpf_s5\n\t"
               "s_cmp_eq_u32 %0, 6\n\ts_cbranch_scc1 .Lpf_s6\n\ts_branch .Lpf_s7"
               :: "s"(__builtin_amdgcn_readfirstlane(threadIdx.x >> 6)) : "scc", "memory");
}

#define WOJ 6
#define AT_ALIGN ".p2align 6\n\t"
#define AT_POLL 8
constexpr int AT_DG = 2 * KV_GRP;
constexpr int AG_LDS = 81920;
constexpr int G2_CH = 18432;
constexpr float AT_THR = 16.0f;
DI float swapmax32(float v) {
  auto rr = __builtin_amdgcn_permlane32_swap(__float_as_uint(v), __float_as_uint(v), false, false);
  return __builtin_fmaxf(__uint_as_float(rr[0]), __uint_as_float(rr[1]));
}
DI float rowmax16(const f32x16& s) {
  float mx = __builtin_fmaxf(__builtin_fmaxf(s[0], s[1]), s[2]);
#pragma unroll
  for (int r = 3; r < 15; r += 2) mx = __builtin_fmaxf(__builtin_fmaxf(mx, s[r]), s[r + 1]);
  return __builtin_fmaxf(mx, s[15]);
}
#define GLOAD(dst, p) asm volatile("global_load_dwordx4 %0, %1, off" : "=v"(dst) : "v"(p) : "memory")

struct Cand { int base; unsigned mask; };
DI unsigned atom_add(const void* p, unsigned v) {
  unsigned r;
  asm volatile("global_atomic_add %0, %1, %2, off sc0\n\ts_waitcnt vmcnt(0)" : "=&v"(r) : "v"(p), "v"(v) : "memory");
  return r;
}
DI Cand attn_role(const uint16_t* __restrict__ wo, char* __restrict__ ws, char* smem, int b, int wid) {
  const int head = (b & 7) * 4 + ((b >> 3) & 3), qc = b >> 5;
  const unsigned lds0 = (unsigned)(uintptr_t)(LAS char*)smem;
  volatile LAS unsigned* flag = (volatile LAS unsigned*)((LAS char*)smem + 4 * AT_DG);
  TS(1, 0);
  if (wid == 4) {
    __builtin_amdgcn_s_setprio(3);
    const int lane = lane_id();
    const char* kv = ws + WS_KV + (size_t)head * KV_HEAD;
    const unsigned vo = lane * 16;
    auto issue_dg = [&](int g, int slot) {
      const char* src = kv + g * AT_DG;
      const unsigned dst = lds0 + slot * AT_DG;
#pragma unroll
      for (int p = 0; p < 19; ++p) glds16s(src + p * 1024, vo, dst + p * 1024);
      if (lane < 32) glds16s(src + 19456, vo, dst + 19456);
    };
    LTS_DECL; LTS(0);
    PAD();
    issue_dg(0, 0);
    PAD();
    issue_dg(1, 1);
    PAD();
    issue_dg(2, 2);
    SEG(7);
    flag[lane] = 0u;
    asm volatile("s_waitcnt vmcnt(50) lgkmcnt(0)\n\ts_barrier" ::: "memory");
    asm volatile("s_waitcnt vmcnt(40)" ::: "memory");
    flag[32 + (lane & 3)] = 1u;
    issue_dg(3, 3);
    PAD();
    asm volatile("s_waitcnt vmcnt(40)" ::: "memory");
    flag[32 + (lane & 3)] = 2u;
    LTS(1);
    asm volatile("s_waitcnt vmcnt(20)" ::: "memory");
    flag[32 + (lane & 3)] = 3u;
    LTS(2);
    unsigned ptd = 0u;
#pragma unroll 1
    for (unsigned j = 4; j < 9; ++j) {
      asm volatile("s_waitcnt vmcnt(0)" : "+v"(ptd) :: "memory");
      flag[32 + (lane & 3)] = j < 8 ? j : 9u;
      LTS(j - 1);
      if (j == 8) break;
      while (!__all(flag[16 + (lane & 3)] >= j - 3)) __builtin_amdgcn_s_sleep(AT_POLL);
      issue_dg(j, j & 3);
      if (j == WOJ) {
#pragma unroll
        for (int i = 0; i < 3; ++i)
          asm volatile("global_load_dword %0, %1, %2" : "+v"(ptd) : "v"(((b >> 3) * 162 + (i * 64 + lane < 162 ? i * 64 + lane : 161)) * 128), "s"(wo) : "memory");
      }
    }
    asm volatile("s_waitcnt vmcnt(0) lgkmcnt(0)\n\ts_barrier" ::: "memory");
    LTS(9); LTS_OUT(0);
    PAD();
    const unsigned fl = __builtin_amdgcn_readfirstlane(*flag);
    if (__builtin_expect(fl != 0u, 0)) {
#pragma unroll 1
      for (int g = 0; g < 8; ++g) {
        asm volatile("s_waitcnt lgkmcnt(0)\n\ts_barrier" ::: "memory");
        issue_dg(g, 0);
        asm volatile("s_waitcnt vmcnt(0)\n\ts_barrier" ::: "memory");
      }
    }
    return Cand{0, 0u};
  }
  PAD();
  const int lane = lane_id();
  const int hh = lane >> 5, l31 = lane & 31;
  const int qtA = qc * 8 + wid * 2;
  bf16x8 q1A = *(const bf16x8*)(ws + WS_QF + ((size_t)(head * 64 + qtA) * 64 + lane) * 16);
  bf16x8 q1B = *(const bf16x8*)(ws + WS_QF + ((size_t)(head * 64 + qtA + 1) * 64 + lane) * 16);
  const unsigned q2vA = *(const unsigned*)(ws + WS_Q2 + ((size_t)head * S + qtA * 32 + l31) * 4);
  const unsigned q2vB = *(const unsigned*)(ws + WS_Q2 + ((size_t)head * S + qtA * 32 + 32 + l31) * 4);
  const float glg = ((const float*)(ws + WS_G))[qtA * 32 + lane] + ((const float*)(ws + WS_G))[S + qtA * 32 + lane];
  float gt = 1.0f / (1.0f + __expf(-glg));
  const int dmap = l31 < 19 ? l31 : ((l31 >= 20 && l31 <= 27) ? 20 : 19);
  lcp bK = (lcp)smem + lane * 16, bK2 = (lcp)smem + (hh ? 1024 + 18 * 16 : KV_K2 + l31 * 4), bV = (lcp)smem + 1024 + hh * 336 + dmap * 16;
  const f32x16 zero16 = {};
  f32x16 oA = {}, oB = {};
  u32x4 q2A = {hh ? 0u : q2vA, 0u, 0u, 0u}, q2B = {hh ? 0u : q2vB, 0u, 0u, 0u};
#define LDS_B32(p) (*(const LAS unsigned*)(p))
  asm volatile("s_waitcnt lgkmcnt(0)\n\ts_barrier" : "+v"(gt) :: "memory");
  TS(1, 1);
  PAD();
  {
    bf16x8 kf = LDS_B128(bK);
    u32x4 k2f = {LDS_B32(bK2), 0u, 0u, 0u};
    bf16x8 v0 = LDS_B128(bV), v1 = v0;
    f32x16 s = MFMA32(kf, q1A, zero16);
    s = MFMA32(BC8(k2f), BC8(q2A), s);
    f32x16 t = MFMA32(kf, q1B, zero16);
    t = MFMA32(BC8(k2f), BC8(q2B), t);
    const unsigned nA = cvtpk(-swapmax32(rowmax16(s)), 0.f);
    const unsigned nB = cvtpk(-swapmax32(rowmax16(t)), 0.f);
    if (hh) { q2A[0] = nA; q2B[0] = nB; }
    f32x16 sA = MFMA32(kf, q1A, zero16);
    sA = MFMA32(BC8(k2f), BC8(q2A), sA);
    u32x4 p1 = {0u, 0u, 0u, 0u};
    lcp aK = bK, aK2 = bK2, aV = bV;
    unsigned it, tmp;
    SEG(5);
    asm volatile(
    "s_mov_b32 %[it], 0\n\t"
    "s_nop 12\n\t"
    AT_ALIGN
    "1:\n\t"
    "v_mfma_f32_32x32x16_bf16 v[48:63], v[72:75], v[96:99], 0\n\t"
    "ds_read_b128 v[72:75], v107 offset:2496\n\t"
    "v_exp_f32_e32 v32, v32\n\t"
    "v_exp_f32_e32 v33, v33\n\t"
    "v_exp_f32_e32 v34, v34\n\t"
    "v_exp_f32_e32 v35, v35\n\t"
    "v_cvt_pk_bf16_f32 v64, v32, v33\n\t"
    "v_cvt_pk_bf16_f32 v65, v34, v35\n\t"
    "v_mfma_f32_32x32x16_bf16 v[16:31], v[84:87], v[68:71], v[16:31]\n\t"
    "ds_read_b128 v[84:87], v109 offset:672\n\t"
    "v_exp_f32_e32 v36, v36\n\t"
    "v_exp_f32_e32 v37, v37\n\t"
    "v_exp_f32_e32 v38, v38\n\t"
    "v_exp_f32_e32 v39, v39\n\t"
    "v_cvt_pk_bf16_f32 v66, v36, v37\n\t"
    "v_cvt_pk_bf16_f32 v67, v38, v39\n\t"
    "v_mfma_f32_32x32x16_bf16 v[48:63], v[76:79], v[100:103], v[48:63]\n\t"
    "ds_read_b32 v76, v108 offset:2496\n\t"
    "v_exp_f32_e32 v40, v40\n\t"
    "s_waitcnt lgkmcnt(2)\n\t"
    "v_exp_f32_e64 v41, v41\n\t"
    "v_exp_f32_e32 v42, v42\n\t"
    "v_exp_f32_e32 v43, v43\n\t"
    "v_cvt_pk_bf16_f32 v68, v40, v41\n\t"
    "v_cvt_pk_bf16_f32 v69, v42, v43\n\t"
    "v_mfma_f32_32x32x16_bf16 v[0:15], v[80:83], v[64:67], v[0:15]\n\t"
    "s_add_i32 %[it], %[it], 1\n\t"
    "s_and_b32 %[tmp], %[it], 3\n\t"
    "s_mul_i32 %[tmp], %[tmp], 19968\n\t"
    "v_exp_f32_e32 v44, v44\n\t"
    "v_exp_f32_e32 v45, v45\n\t"
    "v_exp_f32_e32 v46, v46\n\t"
    "v_exp_f32_e32 v47, v47\n\t"
    "v_cvt_pk_bf16_f32 v70, v44, v45\n\t"
    "v_cvt_pk_bf16_f32 v71, v46, v47\n\t"
    "v_mfma_f32_32x32x16_bf16 v[32:47], v[72:75], v[88:91], 0\n\t"
    "v_exp_f32_e32 v48, v48\n\t"
    "s_waitcnt lgkmcnt(1)\n\t"
    "v_exp_f32_e64 v49, v49\n\t"
    "v_exp_f32_e32 v50, v50\n\t"
    "v_exp_f32_e32 v51, v51\n\t"
    "v_cvt_pk_bf16_f32 v64, v48, v49\n\t"
    "v_cvt_pk_bf16_f32 v65, v50, v51\n\t"
    "v_mfma_f32_32x32x16_bf16 v[0:15], v[84:87], v[68:71], v[0:15]\n\t"
    "v_exp_f32_e32 v52, v52\n\t"
    "s_waitcnt lgkmcnt(0)\n\t"
    "v_exp_f32_e64 v53, v53\n\t"
    "v_exp_f32_e32 v54, v54\n\t"
    "v_exp_f32_e32 v55, v55\n\t"
    "v_cvt_pk_bf16_f32 v66, v52, v53\n\t"
    "v_cvt_pk_bf16_f32 v67, v54, v55\n\t"
    "v_mfma_f32_32x32x16_bf16 v[32:47], v[76:79], v[92:95], v[32:47]\n\t"
    "v_exp_f32_e32 v56, v56\n\t"
    "v_exp_f32_e32 v57, v57\n\t"
    "v_exp_f32_e32 v58, v58\n\t"
    "v_exp_f32_e32 v59, v59\n\t"
    "v_cvt_pk_bf16_f32 v68, v56, v57\n\t"
    "v_cvt_pk_bf16_f32 v69, v58, v59\n\t"
    "v_mfma_f32_32x32x16_bf16 v[16:31], v[80:83], v[64:67], v[16:31]\n\t"
    "ds_read_b128 v[80:83], v109 offset:2496\n\t"
    "v_exp_f32_e32 v60, v60\n\t"
    "v_exp_f32_e32 v61, v61\n\t"
    "v_exp_f32_e32 v62, v62\n\t"
    "v_exp_f32_e32 v63, v63\n\t"
    "v_cvt_pk_bf16_f32 v70, v60, v61\n\t"
    "v_cvt_pk_bf16_f32 v71, v62, v63\n\t"
    "v_mfma_f32_32x32x16_bf16 v[48:63], v[72:75], v[96:99], 0\n\t"
    "77:\n\t"
    "s_cbranch_execz 77f\n\t"
    "ds_read_b128 v[72:75], v107 offset:4992\n\t"
    "v_exp_f32_e32 v32, v32\n\t"
    "v_exp_f32_e32 v33, v33\n\t"
    "v_exp_f32_e32 v34, v34\n\t"
    "v_exp_f32_e32 v35, v35\n\t"
    "v_cvt_pk_bf16_f32 v64, v32, v33\n\t"
    "v_cvt_pk_bf16_f32 v65, v34, v35\n\t"
    "v_mfma_f32_32x32x16_bf16 v[16:31], v[84:87], v[68:71], v[16:31]\n\t"
    "ds_read_b128 v[84:87], v109 offset:3168\n\t"
    "v_exp_f32_e32 v36, v36\n\t"
    "v_exp_f32_e32 v37, v37\n\t"
    "v_exp_f32_e32 v38, v38\n\t"
    "v_exp_f32_e32 v39, v39\n\t"
    "v_cvt_pk_bf16_f32 v66, v36, v37\n\t"
    "v_cvt_pk_bf16_f32 v67, v38, v39\n\t"
    "v_mfma_f32_32x32x16_bf16 v[48:63], v[76:79], v[100:103], v[48:63]\n\t"
    "ds_read_b32 v76, v108 offset:4992\n\t"
    "v_exp_f32_e32 v40, v40\n\t"
    "s_waitcnt lgkmcnt(2)\n\t"
    "v_exp_f32_e64 v41, v41\n\t"
    "v_exp_f32_e32 v42, v42\n\t"
    "v_exp_f32_e32 v43, v43\n\t"
    "v_cvt_pk_bf16_f32 v68, v40, v41\n\t"
    "v_cvt_pk_bf16_f32 v69, v42, v43\n\t"
    "v_mfma_f32_32x32x16_bf16 v[0:15], v[80:83], v[64:67], v[0:15]\n\t"
    "v_add_u32_e32 v110, %[tmp], v104\n\t"
    "v_add_u32_e32 v111, %[tmp], v105\n\t"
    "v_exp_f32_e32 v44, v44\n\t"
    "v_exp_f32_e32 v45, v45\n\t"
    "v_exp_f32_e32 v46, v46\n\t"
    "v_exp_f32_e32 v47, v47\n\t"
    "v_cvt_pk_bf16_f32 v70, v44, v45\n\t"
    "v_cvt_pk_bf16_f32 v71, v46, v47\n\t"
    "v_mfma_f32_32x32x16_bf16 v[32:47], v[72:75], v[88:91], 0\n\t"
    "v_exp_f32_e32 v48, v48\n\t"
    "s_waitcnt lgkmcnt(1)\n\t"
    "v_exp_f32_e64 v49, v49\n\t"
    "v_exp_f32_e32 v50, v50\n\t"
    "v_exp_f32_e32 v51, v51\n\t"
    "v_cvt_pk_bf16_f32 v64, v48, v49\n\t"
    "v_cvt_pk_bf16_f32 v65, v50, v51\n\t"
    "v_mfma_f32_32x32x16_bf16 v[0:15], v[84:87], v[68:71], v[0:15]\n\t"
    "v_exp_f32_e32 v52, v52\n\t"
    "s_waitcnt lgkmcnt(0)\n\t"
    "v_exp_f32_e64 v53, v53\n\t"
    "v_exp_f32_e32 v54, v54\n\t"
    "v_exp_f32_e32 v55, v55\n\t"
    "v_cvt_pk_bf16_f32 v66, v52, v53\n\t"
    "v_cvt_pk_bf16_f32 v67, v54, v55\n\t"
    "v_mfma_f32_32x32x16_bf16 v[32:47], v[76:79], v[92:95], v[32:47]\n\t"
    "v_add_u32_e64 v112, %[tmp], v106\n\t"
    "v_exp_f32_e32 v56, v56\n\t"
    "v_exp_f32_e32 v57, v57\n\t"
    "v_exp_f32_e32 v58, v58\n\t"
    "v_exp_f32_e32 v59, v59\n\t"
    "v_cvt_pk_bf16_f32 v68, v56, v57\n\t"
    "v_cvt_pk_bf16_f32 v69, v58, v59\n\t"
    "v_mfma_f32_32x32x16_bf16 v[16:31], v[80:83], v[64:67], v[16:31]\n\t"
    "ds_read_b128 v[80:83], v109 offset:4992\n\t"
    "ds_read_b32 v114, v113 offset:64\n\t"
    "v_exp_f32_e32 v60, v60\n\t"
    "v_exp_f32_e32 v61, v61\n\t"
    "v_exp_f32_e32 v62, v62\n\t"
    "v_exp_f32_e32 v63, v63\n\t"
    "v_cvt_pk_bf16_f32 v70, v60, v61\n\t"
    "v_cvt_pk_bf16_f32 v71, v62, v63\n\t"
    "v_mfma_f32_32x32x16_bf16 v[48:63], v[72:75], v[96:99], 0\n\t"
    "ds_read_b128 v[72:75], v107 offset:7488\n\t"
    "v_exp_f32_e32 v32, v32\n\t"
    "v_exp_f32_e32 v33, v33\n\t"
    "v_exp_f32_e32 v34, v34\n\t"
    "v_exp_f32_e32 v35, v35\n\t"
    "v_cvt_pk_bf16_f32 v64, v32, v33\n\t"
    "v_cvt_pk_bf16_f32 v65, v34, v35\n\t"
    "v_mfma_f32_32x32x16_bf16 v[16:31], v[84:87], v[68:71], v[16:31]\n\t"
    "ds_read_b128 v[84:87], v109 offset:5664\n\t"
    "v_exp_f32_e32 v36, v36\n\t"
    "v_exp_f32_e32 v37, v37\n\t"
    "v_exp_f32_e32 v38, v38\n\t"
    "v_exp_f32_e32 v39, v39\n\t"
    "v_cvt_pk_bf16_f32 v66, v36, v37\n\t"
    "v_cvt_pk_bf16_f32 v67, v38, v39\n\t"
    "v_mfma_f32_32x32x16_bf16 v[48:63], v[76:79], v[100:103], v[48:63]\n\t"
    "ds_read_b32 v76, v108 offset:7488\n\t"
    "v_exp_f32_e32 v40, v40\n\t"
    "s_waitcnt lgkmcnt(2)\n\t"
    "v_exp_f32_e64 v41, v41\n\t"
    "v_exp_f32_e32 v42, v42\n\t"
    "v_exp_f32_e32 v43, v43\n\t"
    "v_cvt_pk_bf16_f32 v68, v40, v41\n\t"
    "v_cvt_pk_bf16_f32 v69, v42, v43\n\t"
    "v_mfma_f32_32x32x16_bf16 v[0:15], v[80:83], v[64:67], v[0:15]\n\t"
    "v_cmp_le_u32_e32 vcc, %[it], v114\n\t"
    "v_exp_f32_e32 v44, v44\n\t"
    "v_exp_f32_e32 v45, v45\n\t"
    "v_exp_f32_e32 v46, v46\n\t"
    "s_cbranch_vccz .Law2%=\n\t"
    ".Lab2%=:\n\t"
    "v_exp_f32_e32 v47, v47\n\t"
    "v_cvt_pk_bf16_f32 v70, v44, v45\n\t"
    "v_cvt_pk_bf16_f32 v71, v46, v47\n\t"
    "v_mfma_f32_32x32x16_bf16 v[32:47], v[72:75], v[88:91], 0\n\t"
    "v_exp_f32_e32 v48, v48\n\t"
    "s_waitcnt lgkmcnt(1)\n\t"
    "v_exp_f32_e64 v49, v49\n\t"
    "v_exp_f32_e32 v50, v50\n\t"
    "v_exp_f32_e32 v51, v51\n\t"
    "v_cvt_pk_bf16_f32 v64, v48, v49\n\t"
    "v_cvt_pk_bf16_f32 v65, v50, v51\n\t"
    "v_mfma_f32_32x32x16_bf16 v[0:15], v[84:87], v[68:71], v[0:15]\n\t"
    "v_exp_f32_e32 v52, v52\n\t"
    "s_waitcnt lgkmcnt(0)\n\t"
    "v_exp_f32_e64 v53, v53\n\t"
    "v_exp_f32_e32 v54, v54\n\t"
    "v_exp_f32_e32 v55, v55\n\t"
    "v_cvt_pk_bf16_f32 v66, v52, v53\n\t"
    "v_cvt_pk_bf16_f32 v67, v54, v55\n\t"
    "v_mfma_f32_32x32x16_bf16 v[32:47], v[76:79], v[92:95], v[32:47]\n\t"
    "v_exp_f32_e32 v56, v56\n\t"
    "v_exp_f32_e32 v57, v57\n\t"
    "v_exp_f32_e32 v58, v58\n\t"
    "v_exp_f32_e32 v59, v59\n\t"
    "v_cvt_pk_bf16_f32 v68, v56, v57\n\t"
    "v_cvt_pk_bf16_f32 v69, v58, v59\n\t"
    "v_mfma_f32_32x32x16_bf16 v[16:31], v[80:83], v[64:67], v[16:31]\n\t"
    "ds_read_b128 v[80:83], v109 offset:7488\n\t"
    "v_exp_f32_e32 v60, v60\n\t"
    "v_exp_f32_e32 v61, v61\n\t"
    "v_exp_f32_e32 v62, v62\n\t"
    "v_exp_f32_e32 v63, v63\n\t"
    "v_cvt_pk_bf16_f32 v70, v60, v61\n\t"
    "v_cvt_pk_bf16_f32 v71, v62, v63\n\t"
    "v_mfma_f32_32x32x16_bf16 v[48:63], v[72:75], v[96:99], 0\n\t"
    "77:\n\t"
    "s_cbranch_execz 77f\n\t"
    "ds_read_b128 v[72:75], v107 offset:9984\n\t"
    "v_exp_f32_e32 v32, v32\n\t"
    "v_exp_f32_e32 v33, v33\n\t"
    "v_exp_f32_e32 v34, v34\n\t"
    "v_exp_f32_e32 v35, v35\n\t"
    "v_cvt_pk_bf16_f32 v64, v32, v33\n\t"
    "v_cvt_pk_bf16_f32 v65, v34, v35\n\t"
    "v_mfma_f32_32x32x16_bf16 v[16:31], v[84:87], v[68:71], v[16:31]\n\t"
    "ds_read_b128 v[84:87], v109 offset:8160\n\t"
    "v_exp_f32_e32 v36, v36\n\t"
    "v_exp_f32_e32 v37, v37\n\t"
    "v_exp_f32_e32 v38, v38\n\t"
    "v_exp_f32_e32 v39, v39\n\t"
    "v_cvt_pk_bf16_f32 v66, v36, v37\n\t"
    "v_cvt_pk_bf16_f32 v67, v38, v39\n\t"
    "v_mfma_f32_32x32x16_bf16 v[48:63], v[76:79], v[100:103], v[48:63]\n\t"
    "ds_read_b32 v76, v108 offset:9984\n\t"
    "v_exp_f32_e32 v40, v40\n\t"
    "s_waitcnt lgkmcnt(2)\n\t"
    "v_exp_f32_e64 v41, v41\n\t"
    "v_exp_f32_e32 v42, v42\n\t"
    "v_exp_f32_e32 v43, v43\n\t"
    "v_cvt_pk_bf16_f32 v68, v40, v41\n\t"
    "v_cvt_pk_bf16_f32 v69, v42, v43\n\t"
    "v_mfma_f32_32x32x16_bf16 v[0:15], v[80:83], v[64:67], v[0:15]\n\t"
    "v_exp_f32_e32 v44, v44\n\t"
    "v_exp_f32_e32 v45, v45\n\t"
    "v_exp_f32_e32 v46, v46\n\t"
    "v_exp_f32_e32 v47, v47\n\t"
    "v_cvt_pk_bf16_f32 v70, v44, v45\n\t"
    "v_cvt_pk_bf16_f32 v71, v46, v47\n\t"
    "v_mfma_f32_32x32x16_bf16 v[32:47], v[72:75], v[88:91], 0\n\t"
    "v_exp_f32_e32 v48, v48\n\t"
    "s_waitcnt lgkmcnt(1)\n\t"
    "v_exp_f32_e64 v49, v49\n\t"
    "v_exp_f32_e32 v50, v50\n\t"
    "v_exp_f32_e32 v51, v51\n\t"
    "v_cvt_pk_bf16_f32 v64, v48, v49\n\t"
    "v_cvt_pk_bf16_f32 v65, v50, v51\n\t"
    "v_mfma_f32_32x32x16_bf16 v[0:15], v[84:87], v[68:71], v[0:15]\n\t"
    "v_exp_f32_e32 v52, v52\n\t"
    "s_waitcnt lgkmcnt(0)\n\t"
    "v_exp_f32_e64 v53, v53\n\t"
    "v_exp_f32_e32 v54, v54\n\t"
    "v_exp_f32_e32 v55, v55\n\t"
    "v_cvt_pk_bf16_f32 v66, v52, v53\n\t"
    "v_cvt_pk_bf16_f32 v67, v54, v55\n\t"
    "v_mfma_f32_32x32x16_bf16 v[32:47], v[76:79], v[92:95], v[32:47]\n\t"
    "v_exp_f32_e32 v56, v56\n\t"
    "v_exp_f32_e32 v57, v57\n\t"
    "v_exp_f32_e32 v58, v58\n\t"
    "v_exp_f32_e32 v59, v59\n\t"
    "v_cvt_pk_bf16_f32 v68, v56, v57\n\t"
    "v_cvt_pk_bf16_f32 v69, v58, v59\n\t"
    "v_mfma_f32_32x32x16_bf16 v[16:31], v[80:83], v[64:67], v[16:31]\n\t"
    "ds_read_b128 v[80:83], v109 offset:9984\n\t"
    "v_exp_f32_e32 v60, v60\n\t"
    "v_exp_f32_e32 v61, v61\n\t"
    "v_exp_f32_e32 v62, v62\n\t"
    "v_exp_f32_e32 v63, v63\n\t"
    "v_cvt_pk_bf16_f32 v70, v60, v61\n\t"
    "v_cvt_pk_bf16_f32 v71, v62, v63\n\t"
    "v_mfma_f32_32x32x16_bf16 v[48:63], v[72:75], v[96:99], 0\n\t"
    "ds_read_b128 v[72:75], v107 offset:12480\n\t"
    "v_exp_f32_e32 v32, v32\n\t"
    "v_exp_f32_e32 v33, v33\n\t"
    "v_exp_f32_e32 v34, v34\n\t"
    "v_exp_f32_e32 v35, v35\n\t"
    "v_cvt_pk_bf16_f32 v64, v32, v33\n\t"
    "v_cvt_pk_bf16_f32 v65, v34, v35\n\t"
    "v_mfma_f32_32x32x16_bf16 v[16:31], v[84:87], v[68:71], v[16:31]\n\t"
    "ds_read_b128 v[84:87], v109 offset:10656\n\t"
    "v_exp_f32_e32 v36, v36\n\t"
    "v_exp_f32_e32 v37, v37\n\t"
    "v_exp_f32_e32 v38, v38\n\t"
    "v_exp_f32_e32 v39, v39\n\t"
    "v_cvt_pk_bf16_f32 v66, v36, v37\n\t"
    "v_cvt_pk_bf16_f32 v67, v38, v39\n\t"
    "v_mfma_f32_32x32x16_bf16 v[48:63], v[76:79], v[100:103], v[48:63]\n\t"
    "ds_read_b32 v76, v108 offset:12480\n\t"
    "v_exp_f32_e32 v40, v40\n\t"
    "s_waitcnt lgkmcnt(2)\n\t"
    "v_exp_f32_e64 v41, v41\n\t"
    "v_exp_f32_e32 v42, v42\n\t"
    "v_exp_f32_e32 v43, v43\n\t"
    "v_cvt_pk_bf16_f32 v68, v40, v41\n\t"
    "v_cvt_pk_bf16_f32 v69, v42, v43\n\t"
    "v_mfma_f32_32x32x16_bf16 v[0:15], v[80:83], v[64:67], v[0:15]\n\t"
    "v_exp_f32_e32 v44, v44\n\t"
    "v_exp_f32_e32 v45, v45\n\t"
    "v_exp_f32_e32 v46, v46\n\t"
    "v_exp_f32_e32 v47, v47\n\t"
    "v_cvt_pk_bf16_f32 v70, v44, v45\n\t"
    "v_cvt_pk_bf16_f32 v71, v46, v47\n\t"
    "v_mfma_f32_32x32x16_bf16 v[32:47], v[72:75], v[88:91], 0\n\t"
    "v_exp_f32_e32 v48, v48\n\t"
    "s_waitcnt lgkmcnt(1)\n\t"
    "v_exp_f32_e64 v49, v49\n\t"
    "v_exp_f32_e32 v50, v50\n\t"
    "v_exp_f32_e32 v51, v51\n\t"
    "v_cvt_pk_bf16_f32 v64, v48, v49\n\t"
    "v_cvt_pk_bf16_f32 v65, v50, v51\n\t"
    "v_mfma_f32_32x32x16_bf16 v[0:15], v[84:87], v[68:71], v[0:15]\n\t"
    "v_exp_f32_e32 v52, v52\n\t"
    "s_waitcnt lgkmcnt(0)\n\t"
    "v_exp_f32_e64 v53, v53\n\t"
    "v_exp_f32_e32 v54, v54\n\t"
    "v_exp_f32_e32 v55, v55\n\t"
    "v_cvt_pk_bf16_f32 v66, v52, v53\n\t"
    "v_cvt_pk_bf16_f32 v67, v54, v55\n\t"
    "v_mfma_f32_32x32x16_bf16 v[32:47], v[76:79], v[92:95], v[32:47]\n\t"
    "v_exp_f32_e32 v56, v56\n\t"
    "v_exp_f32_e32 v57, v57\n\t"
    "v_exp_f32_e32 v58, v58\n\t"
    "v_exp_f32_e32 v59, v59\n\t"
    "v_cvt_pk_bf16_f32 v68, v56, v57\n\t"
    "v_cvt_pk_bf16_f32 v69, v58, v59\n\t"
    "v_mfma_f32_32x32x16_bf16 v[16:31], v[80:83], v[64:67], v[16:31]\n\t"
    "ds_read_b128 v[80:83], v109 offset:12480\n\t"
    "v_exp_f32_e32 v60, v60\n\t"
    "v_exp_f32_e32 v61, v61\n\t"
    "v_exp_f32_e32 v62, v62\n\t"
    "v_exp_f32_e32 v63, v63\n\t"
    "v_cvt_pk_bf16_f32 v70, v60, v61\n\t"
    "v_cvt_pk_bf16_f32 v71, v62, v63\n\t"
    "v_mfma_f32_32x32x16_bf16 v[48:63], v[72:75], v[96:99], 0\n\t"
    "77:\n\t"
    "s_cbranch_execz 77f\n\t"
    "ds_read_b128 v[72:75], v107 offset:14976\n\t"
    "v_exp_f32_e32 v32, v32\n\t"
    "v_exp_f32_e32 v33, v33\n\t"
    "v_exp_f32_e32 v34, v34\n\t"
    "v_exp_f32_e32 v35, v35\n\t"
    "v_cvt_pk_bf16_f32 v64, v32, v33\n\t"
    "v_cvt_pk_bf16_f32 v65, v34, v35\n\t"
    "v_mfma_f32_32x32x16_bf16 v[16:31], v[84:87], v[68:71], v[16:31]\n\t"
    "ds_read_b128 v[84:87], v109 offset:13152\n\t"
    "v_exp_f32_e32 v36, v36\n\t"
    "v_exp_f32_e32 v37, v37\n\t"
    "v_exp_f32_e32 v38, v38\n\t"
    "v_exp_f32_e32 v39, v39\n\t"
    "v_cvt_pk_bf16_f32 v66, v36, v37\n\t"
    "v_cvt_pk_bf16_f32 v67, v38, v39\n\t"
    "v_mfma_f32_32x32x16_bf16 v[48:63], v[76:79], v[100:103], v[48:63]\n\t"
    "ds_read_b32 v76, v108 offset:14976\n\t"
    "v_exp_f32_e32 v40, v40\n\t"
    "s_waitcnt lgkmcnt(2)\n\t"
    "v_exp_f32_e64 v41, v41\n\t"
    "v_exp_f32_e32 v42, v42\n\t"
    "v_exp_f32_e32 v43, v43\n\t"
    "v_cvt_pk_bf16_f32 v68, v40, v41\n\t"
    "v_cvt_pk_bf16_f32 v69, v42, v43\n\t"
    "v_mfma_f32_32x32x16_bf16 v[0:15], v[80:83], v[64:67], v[0:15]\n\t"
    "v_exp_f32_e32 v44, v44\n\t"
    "v_exp_f32_e32 v45, v45\n\t"
    "v_exp_f32_e32 v46, v46\n\t"
    "v_exp_f32_e32 v47, v47\n\t"
    "v_cvt_pk_bf16_f32 v70, v44, v45\n\t"
    "v_cvt_pk_bf16_f32 v71, v46, v47\n\t"
    "v_mfma_f32_32x32x16_bf16 v[32:47], v[72:75], v[88:91], 0\n\t"
    "v_exp_f32_e32 v48, v48\n\t"
    "s_waitcnt lgkmcnt(1)\n\t"
    "v_exp_f32_e64 v49, v49\n\t"
    "v_exp_f32_e32 v50, v50\n\t"
    "v_exp_f32_e32 v51, v51\n\t"
    "v_cvt_pk_bf16_f32 v64, v48, v49\n\t"
    "v_cvt_pk_bf16_f32 v65, v50, v51\n\t"
    "v_mfma_f32_32x32x16_bf16 v[0:15], v[84:87], v[68:71], v[0:15]\n\t"
    "v_exp_f32_e32 v52, v52\n\t"
    "s_waitcnt lgkmcnt(0)\n\t"
    "v_exp_f32_e64 v53, v53\n\t"
    "v_exp_f32_e32 v54, v54\n\t"
    "v_exp_f32_e32 v55, v55\n\t"
    "v_cvt_pk_bf16_f32 v66, v52, v53\n\t"
    "v_cvt_pk_bf16_f32 v67, v54, v55\n\t"
    "v_mfma_f32_32x32x16_bf16 v[32:47], v[76:79], v[92:95], v[32:47]\n\t"
    "v_exp_f32_e32 v56, v56\n\t"
    "v_exp_f32_e32 v57, v57\n\t"
    "v_exp_f32_e32 v58, v58\n\t"
    "v_exp_f32_e32 v59, v59\n\t"
    "v_cvt_pk_bf16_f32 v68, v56, v57\n\t"
    "v_cvt_pk_bf16_f32 v69, v58, v59\n\t"
    "v_mfma_f32_32x32x16_bf16 v[16:31], v[80:83], v[64:67], v[16:31]\n\t"
    "ds_read_b128 v[80:83], v109 offset:14976\n\t"
    "ds_read_b32 v114, v113 offset:64\n\t"
    "v_exp_f32_e32 v60, v60\n\t"
    "v_exp_f32_e32 v61, v61\n\t"
    "v_exp_f32_e32 v62, v62\n\t"
    "v_exp_f32_e32 v63, v63\n\t"
    "v_cvt_pk_bf16_f32 v70, v60, v61\n\t"
    "v_cvt_pk_bf16_f32 v71, v62, v63\n\t"
    "v_mfma_f32_32x32x16_bf16 v[48:63], v[72:75], v[96:99], 0\n\t"
    "ds_read_b128 v[72:75], v107 offset:17472\n\t"
    "v_exp_f32_e32 v32, v32\n\t"
    "v_exp_f32_e32 v33, v33\n\t"
    "v_exp_f32_e32 v34, v34\n\t"
    "v_exp_f32_e32 v35, v35\n\t"
    "v_cvt_pk_bf16_f32 v64, v32, v33\n\t"
    "v_cvt_pk_bf16_f32 v65, v34, v35\n\t"
    "v_mfma_f32_32x32x16_bf16 v[16:31], v[84:87], v[68:71], v[16:31]\n\t"
    "ds_read_b128 v[84:87], v109 offset:15648\n\t"
    "v_exp_f32_e32 v36, v36\n\t"
    "v_exp_f32_e32 v37, v37\n\t"
    "v_exp_f32_e32 v38, v38\n\t"
    "v_exp_f32_e32 v39, v39\n\t"
    "v_cvt_pk_bf16_f32 v66, v36, v37\n\t"
    "v_cvt_pk_bf16_f32 v67, v38, v39\n\t"
    "v_mfma_f32_32x32x16_bf16 v[48:63], v[76:79], v[100:103], v[48:63]\n\t"
    "ds_read_b32 v76, v108 offset:17472\n\t"
    "v_exp_f32_e32 v40, v40\n\t"
    "s_waitcnt lgkmcnt(2)\n\t"
    "v_exp_f32_e64 v41, v41\n\t"
    "v_exp_f32_e32 v42, v42\n\t"
    "v_exp_f32_e32 v43, v43\n\t"
    "v_cvt_pk_bf16_f32 v68, v40, v41\n\t"
    "v_cvt_pk_bf16_f32 v69, v42, v43\n\t"
    "v_mfma_f32_32x32x16_bf16 v[0:15], v[80:83], v[64:67], v[0:15]\n\t"
    "v_cmp_lt_u32_e32 vcc, %[it], v114\n\t"
    "v_exp_f32_e32 v44, v44\n\t"
    "v_exp_f32_e32 v45, v45\n\t"
    "v_exp_f32_e32 v46, v46\n\t"
    "s_cbranch_vccz .Law6%=\n\t"
    ".Lab6%=:\n\t"
    "v_exp_f32_e32 v47, v47\n\t"
    "v_cvt_pk_bf16_f32 v70, v44, v45\n\t"
    "v_cvt_pk_bf16_f32 v71, v46, v47\n\t"
    "v_mfma_f32_32x32x16_bf16 v[32:47], v[72:75], v[88:91], 0\n\t"
    "v_exp_f32_e32 v48, v48\n\t"
    "s_waitcnt lgkmcnt(1)\n\t"
    "v_exp_f32_e64 v49, v49\n\t"
    "v_exp_f32_e32 v50, v50\n\t"
    "v_exp_f32_e32 v51, v51\n\t"
    "v_cvt_pk_bf16_f32 v64, v48, v49\n\t"
    "v_cvt_pk_bf16_f32 v65, v50, v51\n\t"
    "v_mfma_f32_32x32x16_bf16 v[0:15], v[84:87], v[68:71], v[0:15]\n\t"
    "v_exp_f32_e32 v52, v52\n\t"
    "s_waitcnt lgkmcnt(0)\n\t"
    "v_exp_f32_e64 v53, v53\n\t"
    "v_exp_f32_e32 v54, v54\n\t"
    "v_exp_f32_e32 v55, v55\n\t"
    "v_cvt_pk_bf16_f32 v66, v52, v53\n\t"
    "v_cvt_pk_bf16_f32 v67, v54, v55\n\t"
    "v_mfma_f32_32x32x16_bf16 v[32:47], v[76:79], v[92:95], v[32:47]\n\t"
    "v_exp_f32_e32 v56, v56\n\t"
    "v_exp_f32_e32 v57, v57\n\t"
    "v_exp_f32_e32 v58, v58\n\t"
    "v_exp_f32_e32 v59, v59\n\t"
    "v_cvt_pk_bf16_f32 v68, v56, v57\n\t"
    "v_cvt_pk_bf16_f32 v69, v58, v59\n\t"
    "v_mfma_f32_32x32x16_bf16 v[16:31], v[80:83], v[64:67], v[16:31]\n\t"
    "ds_read_b128 v[80:83], v109 offset:17472\n\t"
    "v_exp_f32_e32 v60, v60\n\t"
    "v_exp_f32_e32 v61, v61\n\t"
    "v_exp_f32_e32 v62, v62\n\t"
    "v_exp_f32_e32 v63, v63\n\t"
    "v_cvt_pk_bf16_f32 v70, v60, v61\n\t"
    "v_cvt_pk_bf16_f32 v71, v62, v63\n\t"
    "v_mfma_f32_32x32x16_bf16 v[48:63], v[72:75], v[96:99], 0\n\t"
    "77:\n\t"
    "s_cbranch_execz 77f\n\t"
    "ds_read_b128 v[72:75], v110 offset:0\n\t"
    "v_exp_f32_e32 v32, v32\n\t"
    "v_exp_f32_e32 v33, v33\n\t"
    "v_exp_f32_e32 v34, v34\n\t"
    "v_exp_f32_e32 v35, v35\n\t"
    "v_cvt_pk_bf16_f32 v64, v32, v33\n\t"
    "v_cvt_pk_bf16_f32 v65, v34, v35\n\t"
    "v_mfma_f32_32x32x16_bf16 v[16:31], v[84:87], v[68:71], v[16:31]\n\t"
    "ds_read_b128 v[84:87], v109 offset:18144\n\t"
    "v_exp_f32_e32 v36, v36\n\t"
    "v_exp_f32_e32 v37, v37\n\t"
    "v_exp_f32_e32 v38, v38\n\t"
    "v_exp_f32_e32 v39, v39\n\t"
    "v_cvt_pk_bf16_f32 v66, v36, v37\n\t"
    "v_cvt_pk_bf16_f32 v67, v38, v39\n\t"
    "v_mfma_f32_32x32x16_bf16 v[48:63], v[76:79], v[100:103], v[48:63]\n\t"
    "ds_read_b32 v76, v111 offset:0\n\t"
    "v_exp_f32_e32 v40, v40\n\t"
    "s_waitcnt lgkmcnt(2)\n\t"
    "v_exp_f32_e64 v41, v41\n\t"
    "v_exp_f32_e32 v42, v42\n\t"
    "v_exp_f32_e32 v43, v43\n\t"
    "v_cvt_pk_bf16_f32 v68, v40, v41\n\t"
    "v_cvt_pk_bf16_f32 v69, v42, v43\n\t"
    "v_mfma_f32_32x32x16_bf16 v[0:15], v[80:83], v[64:67], v[0:15]\n\t"
    "v_exp_f32_e32 v44, v44\n\t"
    "v_exp_f32_e32 v45, v45\n\t"
    "v_exp_f32_e32 v46, v46\n\t"
    "v_exp_f32_e32 v47, v47\n\t"
    "v_cvt_pk_bf16_f32 v70, v44, v45\n\t"
    "v_cvt_pk_bf16_f32 v71, v46, v47\n\t"
    "v_mfma_f32_32x32x16_bf16 v[32:47], v[72:75], v[88:91], 0\n\t"
    "v_exp_f32_e32 v48, v48\n\t"
    "s_waitcnt lgkmcnt(1)\n\t"
    "v_exp_f32_e64 v49, v49\n\t"
    "v_exp_f32_e32 v50, v50\n\t"
    "v_exp_f32_e32 v51, v51\n\t"
    "v_cvt_pk_bf16_f32 v64, v48, v49\n\t"
    "v_cvt_pk_bf16_f32 v65, v50, v51\n\t"
    "v_mfma_f32_32x32x16_bf16 v[0:15], v[84:87], v[68:71], v[0:15]\n\t"
    "v_exp_f32_e32 v52, v52\n\t"
    "s_waitcnt lgkmcnt(0)\n\t"
    "v_exp_f32_e64 v53, v53\n\t"
    "v_exp_f32_e32 v54, v54\n\t"
    "v_exp_f32_e32 v55, v55\n\t"
    "v_cvt_pk_bf16_f32 v66, v52, v53\n\t"
    "v_cvt_pk_bf16_f32 v67, v54, v55\n\t"
    "v_mfma_f32_32x32x16_bf16 v[32:47], v[76:79], v[92:95], v[32:47]\n\t"
    "v_mov_b32_e64 v114, %[it]\n\t"
    "v_exp_f32_e32 v56, v56\n\t"
    "v_exp_f32_e32 v57, v57\n\t"
    "v_exp_f32_e32 v58, v58\n\t"
    "v_exp_f32_e32 v59, v59\n\t"
    "v_cvt_pk_bf16_f32 v68, v56, v57\n\t"
    "v_cvt_pk_bf16_f32 v69, v58, v59\n\t"
    "v_mfma_f32_32x32x16_bf16 v[16:31], v[80:83], v[64:67], v[16:31]\n\t"
    "ds_read_b128 v[80:83], v112 offset:0\n\t"
    "ds_write_b32 v113, v114\n\t"
    "v_mov_b32_e32 v107, v110\n\t"
    "v_mov_b32_e32 v108, v111\n\t"
    "v_mov_b32_e32 v109, v112\n\t"
    "s_cmp_lt_u32 %[it], 8\n\t"
    "v_exp_f32_e32 v60, v60\n\t"
    "v_exp_f32_e32 v61, v61\n\t"
    "v_exp_f32_e32 v62, v62\n\t"
    "v_exp_f32_e32 v63, v63\n\t"
    "v_cvt_pk_bf16_f32 v70, v60, v61\n\t"
    "v_cvt_pk_bf16_f32 v71, v62, v63\n\t"
    "s_cbranch_scc1 1b\n\t"
    "s_branch .Lae%=\n\t"
    ".Law2%=:\n\t"
    "s_sleep 1\n\t"
    "ds_read_b32 v114, v113 offset:64\n\t"
    "s_waitcnt lgkmcnt(0)\n\t"
    "v_cmp_le_u32_e32 vcc, %[it], v114\n\t"
    "s_cbranch_vccz .Law2%=\n\t"
    "s_branch .Lab2%=\n\t"
    ".Law6%=:\n\t"
    "s_sleep 1\n\t"
    "ds_read_b32 v114, v113 offset:64\n\t"
    "s_waitcnt lgkmcnt(0)\n\t"
    "v_cmp_lt_u32_e32 vcc, %[it], v114\n\t"
    "s_cbranch_vccz .Law6%=\n\t"
    "s_branch .Lab6%=\n\t"
    ".Lae%=:\n\t"
    "v_mfma_f32_32x32x16_bf16 v[16:31], v[84:87], v[68:71], v[16:31]\n\t"
    "s_waitcnt lgkmcnt(0)\n\t"
    "s_nop 12\n\t"
      : "+{v[0:15]}"(oA), "+{v[16:31]}"(oB), "+{v[32:47]}"(sA), "+{v[68:71]}"(p1), "+{v[72:75]}"(kf), "+{v[76:79]}"(k2f),
        "+{v[80:83]}"(v0), "+{v[84:87]}"(v1), "+{v107}"(aK), "+{v108}"(aK2), "+{v109}"(aV), [it] "=&s"(it), [tmp] "=&s"(tmp),
        "+{v[88:91]}"(q1A), "+{v[92:95]}"(q2A), "+{v[96:99]}"(q1B), "+{v[100:103]}"(q2B), "+{v104}"(bK), "+{v105}"(bK2), "+{v106}"(bV)
      : "{v113}"(lds0 + 4 * AT_DG + 64 + wid * 4)
      : "v114", "vcc", "v48", "v49", "v50", "v51", "v52", "v53", "v54", "v55", "v56", "v57", "v58", "v59", "v60", "v61", "v62", "v63",
        "v64", "v65", "v66", "v67", "v110", "v111", "v112", "scc", "memory");
  }
  TS(1, 2);
  SEG(6);
  auto ostore = [&](const f32x16& o, int qt, float gq) {
    const auto ls = __builtin_amdgcn_permlane32_swap(__float_as_uint(o[10]), __float_as_uint(o[10]), false, false);
    const float rl = gq * __builtin_amdgcn_rcpf(__uint_as_float(ls[0]));
    char* ob = ws + WS_OT + (size_t)qt * (36 * 1024);
    const u32x4 v = {cvtpk(o[0] * rl, o[1] * rl), cvtpk(o[2] * rl, o[3] * rl), cvtpk(o[4] * rl, o[5] * rl), cvtpk(o[6] * rl, o[7] * rl)};
    asm volatile("global_store_dwordx4 %0, %1, %2 sc1\n\ts_nop 1" :: "v"(head * 1024 + lane * 16), "v"(v), "s"(ob) : "memory");
    return cvtpk(o[8] * rl, o[9] * rl);
  };
  const auto gs = __builtin_amdgcn_permlane32_swap(__float_as_uint(gt), __float_as_uint(gt), false, false);
  const unsigned inc = 1u << (8 * ((head >> 2) & 3));
  unsigned old = 0u;
  bool pub = false;
#pragma unroll 1
  for (unsigned fl = 0u;;) {
    if (__builtin_expect(fl != 0u, 0)) {
      float mA = __uint_as_float(q2A[1]), mB = mA;
#pragma unroll
      for (int r = 0; r < 16; ++r) { oA[r] = __uint_as_float(q2A[1]); oB[r] = oA[r]; }
      if (hh) { q2A[0] = q2A[1]; q2B[0] = q2A[1]; }
      auto tile = [&](const bf16x8& ks, const u32x4& k2s, const bf16x8& va, const bf16x8& vb, const bf16x8& q1, const u32x4& q2,
                      float& m, f32x16& o, bool first) {
        f32x16 s = MFMA32(ks, q1, zero16);
        s = MFMA32(BC8(k2s), BC8(q2), s);
        const float mx = rowmax16(s) - m;
        if (first || __any(mx > AT_THR)) {
          float dl = swapmax32(mx);
          if (!first) dl = __builtin_fmaxf(dl, 0.f);
          m += dl;
          if (!first) {
            const float f = EX2(-dl);
#pragma unroll
            for (int r = 0; r < 16; ++r) o[r] *= f;
          }
        }
        u32x4 pa, pb;
#pragma unroll
        for (int r = 0; r < 4; ++r) {
          pa[r] = cvtpk(EX2(s[2 * r] - m), EX2(s[2 * r + 1] - m));
          pb[r] = cvtpk(EX2(s[8 + 2 * r] - m), EX2(s[8 + 2 * r + 1] - m));
        }
        o = MFMA32(va, BC8(pa), o);
        o = MFMA32(vb, BC8(pb), o);
      };
#pragma unroll 1
      for (int g = 0; g < 8; ++g) {
        asm volatile("s_waitcnt lgkmcnt(0)\n\ts_barrier" ::: "memory");
        asm volatile("s_waitcnt vmcnt(0)\n\ts_barrier" ::: "memory");
#pragma unroll 1
        for (int ti = 0; ti < 8; ++ti) {
          const bf16x8 ks = LDS_B128(bK + ti * KV_T);
          const u32x4 k2s = {LDS_B32(bK2 + ti * KV_T), q2A[1], q2A[2], q2A[3]};
          const bf16x8 va = LDS_B128(bV + ti * KV_T);
          const bf16x8 vb = LDS_B128(bV + ti * KV_T + 672);
          const bool first = (g == 0 && ti == 0);
          tile(ks, k2s, va, vb, q1A, q2A, mA, oA, first);
          tile(ks, k2s, va, vb, q1B, q2B, mB, oB, first);
        }
      }
    }
    PAD();
    if (pub) break;
    f32x2 nf = {0.f, 0.f};
#pragma unroll
    for (int r = 0; r < 10; r += 2) {
      asm("v_pk_fma_f32 %0, %1, 0, %0" : "+v"(nf) : "v"(f32x2{oA[r], oA[r + 1]}));
      asm("v_pk_fma_f32 %0, %1, 0, %0" : "+v"(nf) : "v"(f32x2{oB[r], oB[r + 1]}));
    }
    const bool bad = (!(oA[10] < 1.8446744073709552e19f && oB[10] < 1.8446744073709552e19f) && !hh) || !(nf[0] == nf[1]);
    pub = fl != 0u || !__any(bad);
    if (pub) {
      const unsigned vA = ostore(oA, qtA, __uint_as_float(gs[0])), vB = ostore(oB, qtA + 1, __uint_as_float(gs[1]));
      const auto v2 = __builtin_amdgcn_permlane32_swap(vA, vB, false, false);
      asm volatile("global_store_dword %0, %1, %2 sc1" :: "v"(hh * (36 * 1024) + l31 * 4), "v"(v2[0]),
                   "s"(ws + WS_OT + (size_t)qtA * (36 * 1024) + 32768 + head * 128) : "memory");
    }
    asm volatile("s_waitcnt vmcnt(0)" ::: "memory");
    if (lane == 0) {
      if (pub) asm volatile("global_atomic_add %0, %1, %2, %3 sc0" : "+v"(old)
                            : "v"(0u), "v"(inc), "s"(ws + WS_SY + ((qc * 4 + wid) * 2 + (head >> 4)) * 128) : "memory");
      else *flag = 1u;
    }
    TS(1, 3);
    if (fl != 0u) break;
    asm volatile("s_waitcnt lgkmcnt(0)\n\ts_barrier" ::: "memory");
    fl = __builtin_amdgcn_readfirstlane(*flag);
    if (__builtin_expect(fl == 0u, 1)) break;
  }
  asm volatile("s_waitcnt vmcnt(0)" : "+v"(old) :: "memory");
  return Cand{(qc * 4 + wid) * 8, __builtin_amdgcn_readfirstlane(old) + inc == SY_ALL ? 0xffu : 0u};
}

constexpr int TL_P2 = 65536;
constexpr int TL_G = 73728;
constexpr int TL_B2 = TL_G + 512;
constexpr int TL_ST = TL_B2 + 512;
constexpr int TL_US = 1056;
#define SK 0
#define G2_PE 99
#define G2_PO 99
#define TL_PS 1
#define TL_PA 1
#define TL_RDY 99u
#define SPIN_MAX 4000
DI Cand g2_role(const uint16_t* __restrict__ w2, const uint16_t* __restrict__ wo, const uint16_t* __restrict__ b2,
                char* __restrict__ ws, uint16_t* __restrict__ out, char* smem, int g, int wid) {
  const int xcd = g & 7, idx = g >> 3;
  const int kq = xcd & 3, rb = (xcd >> 2) * 8 + (idx & 7), nt = idx >> 3;
  const int nch = (kq & 1) ? 18 - SK : 18 + SK, s0 = kq * 72 + (kq & 1) * (4 * SK);
  const int m64 = rb * 2 + (kq >> 1), sl = 2 * nt + (kq & 1);
  const unsigned lds0 = (unsigned)(uintptr_t)(LAS char*)smem;
  volatile LAS unsigned* stw = (volatile LAS unsigned*)((LAS char*)smem + TL_ST);
  unsigned cmask = 0u;
  TS(2, 0);
  if (wid == 4) {
    SEG(3);
    __builtin_amdgcn_s_setprio(3);
    const int lane = lane_id();
    unsigned vo[9];
#pragma unroll
    for (int p = 0; p < 9; ++p) {
      const int o = p * 64 + lane, row = o / 18, pc = o - row * 18;
      vo[p] = row * (D * 2) + (sl ^ (pc < 16 ? pc >> 3 : pc & 1)) * 144 + (pc < 16 ? pc & 7 : 8) * 16;
    }
    const char* src0 = (const char*)w2 + (size_t)s0 * 16 * (D * 2);
    auto issue_chunk = [&](int c) {
      const unsigned dst = lds0 + ((c + SK) & 3) * G2_CH;
#pragma unroll
      for (int s = 0; s < 2; ++s) {
        const char* src = src0 + (size_t)(c * 2 + s) * (32 * D * 2);
#pragma unroll
        for (int p = 0; p < 9; ++p) glds16s(src, vo[p], dst + s * 9216 + p * 1024);
      }
    };
    const char* wos = (const char*)wo + sl * 144;
    const unsigned vw = (lane >> 2) * (D * 2) + (lane & 3) * 16, vw2 = ((lane >> 4) * 18 + (lane & 15)) * (D * 2) + 128;
    unsigned vwa = vw, vwb = vw2;
    asm volatile("s_sleep %11" : "+v"(vo[0]), "+v"(vo[1]), "+v"(vo[2]), "+v"(vo[3]), "+v"(vo[4]), "+v"(vo[5]), "+v"(vo[6]), "+v"(vo[7]),
                 "+v"(vo[8]), "+v"(vwa), "+v"(vwb) : "n"(G2_DELAY));
    auto issue_wo = [&](int st0, int st1) {
#pragma unroll 1
      for (int st = st0; st < st1; ++st) {
        glds16s(wos + st * (18 * D * 2), vwa, lds0 + st * 2048);
        glds16s(wos + st * (18 * D * 2) + 64, vwa, lds0 + st * 2048 + 1024);
      }
    };
    LTS_DECL; LTS(0);
    PAD();
    issue_chunk(0);
    float ga[4];
    unsigned bv[2];
    {
      const char* gp = ws + WS_G + rb * 512;
      asm volatile("global_load_dword %0, %1, %2" : "=v"(ga[0]) : "v"(lane * 4), "s"(gp) : "memory");
      asm volatile("global_load_dword %0, %1, %2 offset:256" : "=v"(ga[1]) : "v"(lane * 4), "s"(gp) : "memory");
      asm volatile("global_load_dword %0, %1, %2" : "=v"(ga[2]) : "v"(lane * 4), "s"(gp + S * 4) : "memory");
      asm volatile("global_load_dword %0, %1, %2 offset:256" : "=v"(ga[3]) : "v"(lane * 4), "s"(gp + S * 4) : "memory");
      asm volatile("global_load_ushort %0, %1, %2" : "=v"(bv[0]) : "v"(lane * 2), "s"(b2 + sl * 72) : "memory");
      asm volatile("global_load_ushort %0, %1, %2 offset:128" : "=v"(bv[1]) : "v"((lane & 7) * 2), "s"(b2 + sl * 72) : "memory");
    }
    PAD();
    issue_chunk(1);
    PAD();
    issue_chunk(2);
    PAD();
#pragma unroll 1
    for (int c = 0; c < nch - 3; ++c) {
      asm volatile("s_waitcnt vmcnt(36)" ::: "memory");
      BAR();
      LTS(c + 19 - nch);
      issue_chunk(c + 3);
    }
    asm volatile("s_waitcnt vmcnt(36)\n\ts_barrier" ::: "memory");
    LTS(16);
    SEG(4);
    issue_wo(18, 27);
    asm volatile("s_waitcnt vmcnt(36)\n\ts_barrier"
                 : "+v"(ga[0]), "+v"(ga[1]), "+v"(ga[2]), "+v"(ga[3]), "+v"(bv[0]), "+v"(bv[1]) :: "memory");
    LTS(17);
    issue_wo(27, 32);
    *(LAS float*)((lcp)smem + TL_G + lane * 4) = 1.0f / (1.0f + __expf(ga[0] + ga[2]));
    *(LAS float*)((lcp)smem + TL_G + 256 + lane * 4) = 1.0f / (1.0f + __expf(ga[1] + ga[3]));
    *(LAS unsigned*)((lcp)smem + TL_B2 + lane * 4) = bv[0] << 16;
    *(LAS unsigned*)((lcp)smem + TL_B2 + 256 + lane * 4) = bv[1] << 16;
    stw[3] = 0u;
    asm volatile("s_waitcnt vmcnt(28) lgkmcnt(0)\n\ts_barrier" ::: "memory");
    LTS(18);
    PAD();
    issue_wo(0, 9);
#pragma unroll 1
    for (int p = 0; p < 8; ++p) glds16s(wos + p * (72 * D * 2), vwb, lds0 + TL_P2 + p * 1024);
    BAR();
    LTS(19);
    const char* cb = ws + WS_SY + SY_B + (m64 * 4 + nt) * 128;
    issue_wo(9, 18);
    unsigned ok = 0u;
#pragma unroll 1
    for (int it = 0; it < SPIN_MAX; ++it) {
      unsigned v;
      asm volatile("global_load_dword %0, %1, %2 sc1\n\ts_waitcnt vmcnt(0)" : "=v"(v) : "v"(0u), "s"(cb) : "memory");
      if (__builtin_amdgcn_readfirstlane(v) >= 8u) { ok = 1u; break; }
      __builtin_amdgcn_s_sleep(TL_PS);
    }
    asm volatile("s_waitcnt vmcnt(0)" ::: "memory");
    stw[0] = ok;
    asm volatile("s_waitcnt lgkmcnt(0)\n\ts_barrier" ::: "memory");
    LTS(20);
    PAD();
    const char* ca = ws + WS_SY + m64 * 256;
#pragma unroll 1
    for (int ph = 1; ph < 3 && ok == 1u; ++ph) {
      ok = 0u;
#pragma unroll 1
      for (int it = 0; it < SPIN_MAX; ++it) {
        unsigned v;
        asm volatile("global_load_dword %0, %1, %2 sc1\n\ts_waitcnt vmcnt(0)" : "=v"(v) : "v"((lane & 1) * 128), "s"(ca) : "memory");
        if (__all(v == SY_ALL)) { ok = 2u; break; }
        if (ph == 1 && __all((v & 0x00ff00ffu) == 0x00040004u && stw[3] >= TL_RDY)) { ok = 1u; break; }
        __builtin_amdgcn_s_sleep(TL_PA);
      }
      stw[ph] = ok;
      asm volatile("s_waitcnt lgkmcnt(0)\n\ts_barrier" ::: "memory");
    }
    LTS(21);
    LTS_OUT(1);
    if (!ok) {
      BAR();
      if (lane == 0) atom_add(ws + WS_SY + SY_F + (m64 * 8 + sl) * 4, 1u);
      return Cand{m64 * 8 + sl, 1u};
    }
    BAR();
    BAR();
  } else {
  __builtin_amdgcn_s_setprio(3);
  const int m32 = rb * 4 + wid;
  const char* asrc = ws + WS_HT + (size_t)m32 * HT_ST + s0 * 1024;
#define GLOADS(dst, vo, sp) asm volatile("global_load_dwordx4 %0, %1, %2" : "=v"(dst) : "v"(vo), "s"(sp) : "memory")
#define GLOADC(dst, vo, sp) asm volatile("global_load_dwordx4 %0, %1, %2 sc1" : "=v"(dst) : "v"(vo), "s"(sp) : "memory")
  const int m = wid & 1, kh = wid >> 1;
  const bool own = kh == (kq >> 1);
  f32x16 a0, a1;
  f32x4 a2;
  {
  const int lane = lane_id();
  const unsigned avo = lane * 16;
  const int i16 = lane & 15, G = lane >> 4;
#define PK8(a, r) u32x4{cvtpk(a[r], a[r + 1]), cvtpk(a[r + 2], a[r + 3]), cvtpk(a[r + 4], a[r + 5]), cvtpk(a[r + 6], a[r + 7])}
#define PK4(a, r) u32x4{cvtpk(a[r], a[r + 1]), cvtpk(a[r + 2], a[r + 3]), 0u, 0u}
#define GSTC(vo, v, sp) asm volatile("global_store_dwordx4 %0, %1, %2 sc1\n\ts_nop 1" :: "v"(vo), "v"(v), "s"(sp) : "memory")
  unsigned sb8 = lds0 + (SK & 2) * G2_CH + (16 * (G >> 1) + 4 * (G & 1) + (i16 >> 2)) * 288 + (i16 & 3) * 8;
  unsigned sb = lds0 + (SK & 2) * G2_CH + (16 * (G >> 1) + 4 * (G & 1) + (i16 >> 2)) * 288 + (i16 & 1) * 16 + (i16 & 2) * 4;
  f32x16 acc[4];
  f32x4 r0, r1;
  {
    const char* hp = asrc + 4096;
    int sdl = (2 - 2 * (SK & 2)) * G2_CH;
    asm volatile(
    "v_mov_b64_e32 v[0:1], 0\n\t"
    "v_mov_b64_e32 v[2:3], 0\n\t"
    "v_mov_b64_e32 v[4:5], 0\n\t"
    "v_mov_b64_e32 v[6:7], 0\n\t"
    "v_mov_b64_e32 v[8:9], 0\n\t"
    "v_mov_b64_e32 v[10:11], 0\n\t"
    "v_mov_b64_e32 v[12:13], 0\n\t"
    "v_mov_b64_e32 v[14:15], 0\n\t"
    "v_mov_b64_e32 v[16:17], 0\n\t"
    "v_mov_b64_e32 v[18:19], 0\n\t"
    "v_mov_b64_e32 v[20:21], 0\n\t"
    "v_mov_b64_e32 v[22:23], 0\n\t"
    "v_mov_b64_e32 v[24:25], 0\n\t"
    "v_mov_b64_e32 v[26:27], 0\n\t"
    "v_mov_b64_e32 v[28:29], 0\n\t"
    "v_mov_b64_e32 v[30:31], 0\n\t"
    "v_mov_b64_e32 v[32:33], 0\n\t"
    "v_mov_b64_e32 v[34:35], 0\n\t"
    "v_mov_b64_e32 v[36:37], 0\n\t"
    "v_mov_b64_e32 v[38:39], 0\n\t"
    "v_mov_b64_e32 v[40:41], 0\n\t"
    "v_mov_b64_e32 v[42:43], 0\n\t"
    "v_mov_b64_e32 v[44:45], 0\n\t"
    "v_mov_b64_e32 v[46:47], 0\n\t"
    "v_mov_b64_e32 v[48:49], 0\n\t"
    "v_mov_b64_e32 v[50:51], 0\n\t"
    "v_mov_b64_e32 v[52:53], 0\n\t"
    "v_mov_b64_e32 v[54:55], 0\n\t"
    "v_mov_b64_e32 v[56:57], 0\n\t"
    "v_mov_b64_e32 v[58:59], 0\n\t"
    "v_mov_b64_e32 v[60:61], 0\n\t"
    "v_mov_b64_e32 v[62:63], 0\n\t"
    "v_mov_b64_e32 v[64:65], 0\n\t"
    "v_mov_b64_e32 v[66:67], 0\n\t"
    "v_mov_b64_e32 v[68:69], 0\n\t"
    "v_mov_b64_e32 v[70:71], 0\n\t"
    "s_sleep %[dly]\n\t"
    "global_load_dwordx4 v[72:75], v122, s[40:41] offset:-4096\n\t"
    "global_load_dwordx4 v[76:79], v122, s[40:41] offset:-3840\n\t"
    "global_load_dwordx4 v[80:83], v122, s[40:41] offset:-2048\n\t"
    "global_load_dwordx4 v[84:87], v122, s[40:41] offset:-1792\n\t"
    "global_load_dwordx4 v[88:91], v122, s[40:41] offset:0\n\t"
    "global_load_dwordx4 v[92:95], v122, s[40:41] offset:256\n\t"
    "global_load_dwordx4 v[96:99], v122, s[40:41] offset:2048\n\t"
    "global_load_dwordx4 v[100:103], v122, s[40:41] offset:2304\n\t"
    "s_mov_b64 vcc, 0\n\t"
    "s_mov_b32 s42, 0\n\t"
    "s_add_u32 s40, s40, 0x1000\n\t"
    "s_addc_u32 s41, s41, 0\n\t"
    "s_setprio 0\n\t"
    "1:\n\t"
    "s_waitcnt lgkmcnt(0)\n\t"
    "s_barrier\n\t"
    "77:\n\t"
    "s_cbranch_execz 77f\n\t"
    "ds_read_b64_tr_b16 v[104:105], v120 offset:0\n\t"
    "ds_read_b64_tr_b16 v[106:107], v120 offset:2304\n\t"
    "ds_read_b64_tr_b16 v[108:109], v120 offset:32\n\t"
    "ds_read_b64_tr_b16 v[110:111], v120 offset:2336\n\t"
    "ds_read_b64_tr_b16 v[112:113], v120 offset:64\n\t"
    "ds_read_b64_tr_b16 v[114:115], v120 offset:2368\n\t"
    "ds_read_b64_tr_b16 v[116:117], v120 offset:96\n\t"
    "ds_read_b64_tr_b16 v[118:119], v120 offset:2400\n\t"
    "s_waitcnt vmcnt(6) lgkmcnt(6)\n\t"
    "v_mfma_f32_16x16x32_bf16 v[0:3], v[104:107], v[72:75], v[0:3]\n\t"
    "v_mfma_f32_16x16x32_bf16 v[4:7], v[104:107], v[76:79], v[4:7]\n\t"
    "ds_read_b64_tr_b16 v[104:105], v120 offset:128\n\t"
    "ds_read_b64_tr_b16 v[106:107], v120 offset:2432\n\t"
    "s_waitcnt lgkmcnt(6)\n\t"
    "v_mfma_f32_16x16x32_bf16 v[8:11], v[108:111], v[72:75], v[8:11]\n\t"
    "v_mfma_f32_16x16x32_bf16 v[12:15], v[108:111], v[76:79], v[12:15]\n\t"
    "ds_read_b64_tr_b16 v[108:109], v120 offset:160\n\t"
    "ds_read_b64_tr_b16 v[110:111], v120 offset:2464\n\t"
    "s_waitcnt lgkmcnt(6)\n\t"
    "v_mfma_f32_16x16x32_bf16 v[16:19], v[112:115], v[72:75], v[16:19]\n\t"
    "v_mfma_f32_16x16x32_bf16 v[20:23], v[112:115], v[76:79], v[20:23]\n\t"
    "ds_read_b64_tr_b16 v[112:113], v120 offset:192\n\t"
    "ds_read_b64_tr_b16 v[114:115], v120 offset:2496\n\t"
    "s_waitcnt lgkmcnt(6)\n\t"
    "v_mfma_f32_16x16x32_bf16 v[24:27], v[116:119], v[72:75], v[24:27]\n\t"
    "v_mfma_f32_16x16x32_bf16 v[28:31], v[116:119], v[76:79], v[28:31]\n\t"
    "ds_read_b64_tr_b16 v[116:117], v120 offset:224\n\t"
    "ds_read_b64_tr_b16 v[118:119], v120 offset:2528\n\t"
    "s_waitcnt lgkmcnt(6)\n\t"
    "v_mfma_f32_16x16x32_bf16 v[32:35], v[104:107], v[72:75], v[32:35]\n\t"
    "v_mfma_f32_16x16x32_bf16 v[36:39], v[104:107], v[76:79], v[36:39]\n\t"
    "ds_read_b64_tr_b16 v[104:105], v121 offset:256\n\t"
    "ds_read_b64_tr_b16 v[106:107], v121 offset:2560\n\t"
    "s_waitcnt lgkmcnt(6)\n\t"
    "v_mfma_f32_16x16x32_bf16 v[40:43], v[108:111], v[72:75], v[40:43]\n\t"
    "v_mfma_f32_16x16x32_bf16 v[44:47], v[108:111], v[76:79], v[44:47]\n\t"
    "ds_read_b64_tr_b16 v[108:109], v120 offset:9216\n\t"
    "ds_read_b64_tr_b16 v[110:111], v120 offset:11520\n\t"
    "s_waitcnt lgkmcnt(6)\n\t"
    "v_mfma_f32_16x16x32_bf16 v[48:51], v[112:115], v[72:75], v[48:51]\n\t"
    "v_mfma_f32_16x16x32_bf16 v[52:55], v[112:115], v[76:79], v[52:55]\n\t"
    "ds_read_b64_tr_b16 v[112:113], v120 offset:9248\n\t"
    "ds_read_b64_tr_b16 v[114:115], v120 offset:11552\n\t"
    "s_waitcnt lgkmcnt(6)\n\t"
    "v_mfma_f32_16x16x32_bf16 v[56:59], v[116:119], v[72:75], v[56:59]\n\t"
    "v_mfma_f32_16x16x32_bf16 v[60:63], v[116:119], v[76:79], v[60:63]\n\t"
    "ds_read_b64_tr_b16 v[116:117], v120 offset:9280\n\t"
    "ds_read_b64_tr_b16 v[118:119], v120 offset:11584\n\t"
    "s_waitcnt lgkmcnt(6)\n\t"
    "v_mfma_f32_16x16x32_bf16 v[64:67], v[104:107], v[72:75], v[64:67]\n\t"
    "v_mfma_f32_16x16x32_bf16 v[68:71], v[104:107], v[76:79], v[68:71]\n\t"
    "s_cbranch_vccnz .Lg2s0%=\n\t"
    "global_load_dwordx4 v[72:75], v122, s[40:41] offset:0\n\t"
    "global_load_dwordx4 v[76:79], v122, s[40:41] offset:256\n\t"
    ".Lg2s0%=:\n\t"
    "77:\n\t"
    "s_cbranch_execz 77f\n\t"
    "ds_read_b64_tr_b16 v[104:105], v120 offset:9312\n\t"
    "ds_read_b64_tr_b16 v[106:107], v120 offset:11616\n\t"
    "s_cbranch_vccnz .Lg2w1%=\n\t"
    ".Lg2r1%=:\n\t"
    "s_waitcnt vmcnt(6) lgkmcnt(6)\n\t"
    "v_mfma_f32_16x16x32_bf16 v[0:3], v[108:111], v[80:83], v[0:3]\n\t"
    "v_mfma_f32_16x16x32_bf16 v[4:7], v[108:111], v[84:87], v[4:7]\n\t"
    "ds_read_b64_tr_b16 v[108:109], v120 offset:9344\n\t"
    "ds_read_b64_tr_b16 v[110:111], v120 offset:11648\n\t"
    "s_waitcnt lgkmcnt(6)\n\t"
    "v_mfma_f32_16x16x32_bf16 v[8:11], v[112:115], v[80:83], v[8:11]\n\t"
    "v_mfma_f32_16x16x32_bf16 v[12:15], v[112:115], v[84:87], v[12:15]\n\t"
    "ds_read_b64_tr_b16 v[112:113], v120 offset:9376\n\t"
    "ds_read_b64_tr_b16 v[114:115], v120 offset:11680\n\t"
    "s_waitcnt lgkmcnt(6)\n\t"
    "v_mfma_f32_16x16x32_bf16 v[16:19], v[116:119], v[80:83], v[16:19]\n\t"
    "v_mfma_f32_16x16x32_bf16 v[20:23], v[116:119], v[84:87], v[20:23]\n\t"
    "ds_read_b64_tr_b16 v[116:117], v120 offset:9408\n\t"
    "ds_read_b64_tr_b16 v[118:119], v120 offset:11712\n\t"
    "s_waitcnt lgkmcnt(6)\n\t"
    "v_mfma_f32_16x16x32_bf16 v[24:27], v[104:107], v[80:83], v[24:27]\n\t"
    "v_mfma_f32_16x16x32_bf16 v[28:31], v[104:107], v[84:87], v[28:31]\n\t"
    "ds_read_b64_tr_b16 v[104:105], v120 offset:9440\n\t"
    "ds_read_b64_tr_b16 v[106:107], v120 offset:11744\n\t"
    "s_waitcnt lgkmcnt(6)\n\t"
    "v_mfma_f32_16x16x32_bf16 v[32:35], v[108:111], v[80:83], v[32:35]\n\t"
    "v_mfma_f32_16x16x32_bf16 v[36:39], v[108:111], v[84:87], v[36:39]\n\t"
    "ds_read_b64_tr_b16 v[108:109], v121 offset:9472\n\t"
    "ds_read_b64_tr_b16 v[110:111], v121 offset:11776\n\t"
    "s_waitcnt lgkmcnt(6)\n\t"
    "v_mfma_f32_16x16x32_bf16 v[40:43], v[112:115], v[80:83], v[40:43]\n\t"
    "v_mfma_f32_16x16x32_bf16 v[44:47], v[112:115], v[84:87], v[44:47]\n\t"
    "s_waitcnt lgkmcnt(4)\n\t"
    "v_mfma_f32_16x16x32_bf16 v[48:51], v[116:119], v[80:83], v[48:51]\n\t"
    "v_mfma_f32_16x16x32_bf16 v[52:55], v[116:119], v[84:87], v[52:55]\n\t"
    "s_waitcnt lgkmcnt(2)\n\t"
    "v_mfma_f32_16x16x32_bf16 v[56:59], v[104:107], v[80:83], v[56:59]\n\t"
    "v_mfma_f32_16x16x32_bf16 v[60:63], v[104:107], v[84:87], v[60:63]\n\t"
    "s_waitcnt lgkmcnt(0)\n\t"
    "v_mfma_f32_16x16x32_bf16 v[64:67], v[108:111], v[80:83], v[64:67]\n\t"
    "v_mfma_f32_16x16x32_bf16 v[68:71], v[108:111], v[84:87], v[68:71]\n\t"
    "s_cbranch_vccnz .Lg2s1%=\n\t"
    "global_load_dwordx4 v[80:83], v122, s[40:41] offset:2048\n\t"
    "global_load_dwordx4 v[84:87], v122, s[40:41] offset:2304\n\t"
    ".Lg2s1%=:\n\t"
    "s_add_u32 s40, s40, 0x1000\n\t"
    "s_addc_u32 s41, s41, 0\n\t"
    "s_waitcnt lgkmcnt(0)\n\t"
    "s_barrier\n\t"
    "77:\n\t"
    "s_cbranch_execz 77f\n\t"
    "ds_read_b64_tr_b16 v[104:105], v120 offset:18432\n\t"
    "ds_read_b64_tr_b16 v[106:107], v120 offset:20736\n\t"
    "ds_read_b64_tr_b16 v[108:109], v120 offset:18464\n\t"
    "ds_read_b64_tr_b16 v[110:111], v120 offset:20768\n\t"
    "ds_read_b64_tr_b16 v[112:113], v120 offset:18496\n\t"
    "ds_read_b64_tr_b16 v[114:115], v120 offset:20800\n\t"
    "ds_read_b64_tr_b16 v[116:117], v120 offset:18528\n\t"
    "ds_read_b64_tr_b16 v[118:119], v120 offset:20832\n\t"
    "s_cbranch_vccnz .Lg2w2%=\n\t"
    ".Lg2r2%=:\n\t"
    "s_waitcnt vmcnt(6) lgkmcnt(6)\n\t"
    "v_mfma_f32_16x16x32_bf16 v[0:3], v[104:107], v[88:91], v[0:3]\n\t"
    "v_mfma_f32_16x16x32_bf16 v[4:7], v[104:107], v[92:95], v[4:7]\n\t"
    "ds_read_b64_tr_b16 v[104:105], v120 offset:18560\n\t"
    "ds_read_b64_tr_b16 v[106:107], v120 offset:20864\n\t"
    "s_waitcnt lgkmcnt(6)\n\t"
    "v_mfma_f32_16x16x32_bf16 v[8:11], v[108:111], v[88:91], v[8:11]\n\t"
    "v_mfma_f32_16x16x32_bf16 v[12:15], v[108:111], v[92:95], v[12:15]\n\t"
    "ds_read_b64_tr_b16 v[108:109], v120 offset:18592\n\t"
    "ds_read_b64_tr_b16 v[110:111], v120 offset:20896\n\t"
    "s_waitcnt lgkmcnt(6)\n\t"
    "v_mfma_f32_16x16x32_bf16 v[16:19], v[112:115], v[88:91], v[16:19]\n\t"
    "v_mfma_f32_16x16x32_bf16 v[20:23], v[112:115], v[92:95], v[20:23]\n\t"
    "ds_read_b64_tr_b16 v[112:113], v120 offset:18624\n\t"
    "ds_read_b64_tr_b16 v[114:115], v120 offset:20928\n\t"
    "s_waitcnt lgkmcnt(6)\n\t"
    "v_mfma_f32_16x16x32_bf16 v[24:27], v[116:119], v[88:91], v[24:27]\n\t"
    "v_mfma_f32_16x16x32_bf16 v[28:31], v[116:119], v[92:95], v[28:31]\n\t"
    "ds_read_b64_tr_b16 v[116:117], v120 offset:18656\n\t"
    "ds_read_b64_tr_b16 v[118:119], v120 offset:20960\n\t"
    "s_waitcnt lgkmcnt(6)\n\t"
    "v_mfma_f32_16x16x32_bf16 v[32:35], v[104:107], v[88:91], v[32:35]\n\t"
    "v_mfma_f32_16x16x32_bf16 v[36:39], v[104:107], v[92:95], v[36:39]\n\t"
    "ds_read_b64_tr_b16 v[104:105], v121 offset:18688\n\t"
    "ds_read_b64_tr_b16 v[106:107], v121 offset:20992\n\t"
    "s_waitcnt lgkmcnt(6)\n\t"
    "v_mfma_f32_16x16x32_bf16 v[40:43], v[108:111], v[88:91], v[40:43]\n\t"
    "v_mfma_f32_16x16x32_bf16 v[44:47], v[108:111], v[92:95], v[44:47]\n\t"
    "ds_read_b64_tr_b16 v[108:109], v120 offset:27648\n\t"
    "ds_read_b64_tr_b16 v[110:111], v120 offset:29952\n\t"
    "s_waitcnt lgkmcnt(6)\n\t"
    "v_mfma_f32_16x16x32_bf16 v[48:51], v[112:115], v[88:91], v[48:51]\n\t"
    "v_mfma_f32_16x16x32_bf16 v[52:55], v[112:115], v[92:95], v[52:55]\n\t"
    "ds_read_b64_tr_b16 v[112:113], v120 offset:27680\n\t"
    "ds_read_b64_tr_b16 v[114:115], v120 offset:29984\n\t"
    "s_waitcnt lgkmcnt(6)\n\t"
    "v_mfma_f32_16x16x32_bf16 v[56:59], v[116:119], v[88:91], v[56:59]\n\t"
    "v_mfma_f32_16x16x32_bf16 v[60:63], v[116:119], v[92:95], v[60:63]\n\t"
    "ds_read_b64_tr_b16 v[116:117], v120 offset:27712\n\t"
    "ds_read_b64_tr_b16 v[118:119], v120 offset:30016\n\t"
    "s_waitcnt lgkmcnt(6)\n\t"
    "v_mfma_f32_16x16x32_bf16 v[64:67], v[104:107], v[88:91], v[64:67]\n\t"
    "v_mfma_f32_16x16x32_bf16 v[68:71], v[104:107], v[92:95], v[68:71]\n\t"
    "s_cbranch_vccnz .Lg2s2%=\n\t"
    "global_load_dwordx4 v[88:91], v122, s[40:41] offset:0\n\t"
    "global_load_dwordx4 v[92:95], v122, s[40:41] offset:256\n\t"
    ".Lg2s2%=:\n\t"
    "77:\n\t"
    "s_cbranch_execz 77f\n\t"
    "ds_read_b64_tr_b16 v[104:105], v120 offset:27744\n\t"
    "ds_read_b64_tr_b16 v[106:107], v120 offset:30048\n\t"
    "s_cbranch_vccnz .Lg2w3%=\n\t"
    ".Lg2r3%=:\n\t"
    "s_waitcnt vmcnt(6) lgkmcnt(6)\n\t"
    "v_mfma_f32_16x16x32_bf16 v[0:3], v[108:111], v[96:99], v[0:3]\n\t"
    "v_mfma_f32_16x16x32_bf16 v[4:7], v[108:111], v[100:103], v[4:7]\n\t"
    "ds_read_b64_tr_b16 v[108:109], v120 offset:27776\n\t"
    "ds_read_b64_tr_b16 v[110:111], v120 offset:30080\n\t"
    "s_waitcnt lgkmcnt(6)\n\t"
    "v_mfma_f32_16x16x32_bf16 v[8:11], v[112:115], v[96:99], v[8:11]\n\t"
    "v_mfma_f32_16x16x32_bf16 v[12:15], v[112:115], v[100:103], v[12:15]\n\t"
    "ds_read_b64_tr_b16 v[112:113], v120 offset:27808\n\t"
    "ds_read_b64_tr_b16 v[114:115], v120 offset:30112\n\t"
    "s_waitcnt lgkmcnt(6)\n\t"
    "v_mfma_f32_16x16x32_bf16 v[16:19], v[116:119], v[96:99], v[16:19]\n\t"
    "v_mfma_f32_16x16x32_bf16 v[20:23], v[116:119], v[100:103], v[20:23]\n\t"
    "ds_read_b64_tr_b16 v[116:117], v120 offset:27840\n\t"
    "ds_read_b64_tr_b16 v[118:119], v120 offset:30144\n\t"
    "s_waitcnt lgkmcnt(6)\n\t"
    "v_mfma_f32_16x16x32_bf16 v[24:27], v[104:107], v[96:99], v[24:27]\n\t"
    "v_mfma_f32_16x16x32_bf16 v[28:31], v[104:107], v[100:103], v[28:31]\n\t"
    "ds_read_b64_tr_b16 v[104:105], v120 offset:27872\n\t"
    "ds_read_b64_tr_b16 v[106:107], v120 offset:30176\n\t"
    "s_waitcnt lgkmcnt(6)\n\t"
    "v_mfma_f32_16x16x32_bf16 v[32:35], v[108:111], v[96:99], v[32:35]\n\t"
    "v_mfma_f32_16x16x32_bf16 v[36:39], v[108:111], v[100:103], v[36:39]\n\t"
    "ds_read_b64_tr_b16 v[108:109], v121 offset:27904\n\t"
    "ds_read_b64_tr_b16 v[110:111], v121 offset:30208\n\t"
    "s_waitcnt lgkmcnt(6)\n\t"
    "v_mfma_f32_16x16x32_bf16 v[40:43], v[112:115], v[96:99], v[40:43]\n\t"
    "v_mfma_f32_16x16x32_bf16 v[44:47], v[112:115], v[100:103], v[44:47]\n\t"
    "s_waitcnt lgkmcnt(4)\n\t"
    "s_setprio 3\n\t"
    "v_add_u32_e32 v120, s43, v120\n\t"
    "v_add_u32_e32 v121, s43, v121\n\t"
    "s_setprio 0\n\t"
    "s_sub_i32 s43, 0, s43\n\t"
    "s_add_i32 s42, s42, 1\n\t"
    "s_cmp_lt_u32 s42, %[pi]\n\t"
    "s_cbranch_scc1 .Lg2p%=\n\t"
    "s_setprio 1\n\t"
    ".Lg2p%=:\n\t"
    "v_mfma_f32_16x16x32_bf16 v[48:51], v[116:119], v[96:99], v[48:51]\n\t"
    "v_mfma_f32_16x16x32_bf16 v[52:55], v[116:119], v[100:103], v[52:55]\n\t"
    "s_waitcnt lgkmcnt(2)\n\t"
    "v_mfma_f32_16x16x32_bf16 v[56:59], v[104:107], v[96:99], v[56:59]\n\t"
    "v_mfma_f32_16x16x32_bf16 v[60:63], v[104:107], v[100:103], v[60:63]\n\t"
    "s_waitcnt lgkmcnt(0)\n\t"
    "v_mfma_f32_16x16x32_bf16 v[64:67], v[108:111], v[96:99], v[64:67]\n\t"
    "v_mfma_f32_16x16x32_bf16 v[68:71], v[108:111], v[100:103], v[68:71]\n\t"
    "s_cbranch_vccnz .Lg2s3%=\n\t"
    "global_load_dwordx4 v[96:99], v122, s[40:41] offset:2048\n\t"
    "global_load_dwordx4 v[100:103], v122, s[40:41] offset:2304\n\t"
    "s_add_u32 s40, s40, 0x1000\n\t"
    "s_addc_u32 s41, s41, 0\n\t"
    "s_cmp_eq_u32 s42, %[nit]\n\t"
    "s_cselect_b64 vcc, -1, 0\n\t"
    "s_branch 1b\n\t"
    ".Lg2w1%=:\n\t"
    "s_waitcnt vmcnt(4)\n\t"
    "s_branch .Lg2r1%=\n\t"
    ".Lg2w2%=:\n\t"
    "s_waitcnt vmcnt(2)\n\t"
    "s_branch .Lg2r2%=\n\t"
    ".Lg2w3%=:\n\t"
    "s_waitcnt vmcnt(0)\n\t"
    "s_branch .Lg2r3%=\n\t"
    ".Lg2s3%=:\n\t"
    "s_setprio 3\n\t"
    "s_nop 7\n\t"
      : "={v[0:15]}"(acc[0]), "={v[16:31]}"(acc[1]), "={v[32:47]}"(acc[2]), "={v[48:63]}"(acc[3]), "={v[64:67]}"(r0), "={v[68:71]}"(r1),
        "+{v120}"(sb), "+{v121}"(sb8), "+{s[40:41]}"(hp), "+{s43}"(sdl)
      : "{v122}"(G * 512 + i16 * 16), [dly] "n"(G2_DELAYC), [nit] "s"(nch / 2 - 1), [pi] "s"((kq & 1) ? G2_PO : G2_PE)
      : "v72", "v73", "v74", "v75", "v76", "v77", "v78", "v79", VCL10(8), VCL10(9), VCL10(10), VCL10(11), "s42", "vcc", "scc", "memory");
  }
  TS(2, 1);
  BAR();
  SEG(1);
    const float og0 = *(const LAS float*)((lcp)smem + TL_G + wid * 128 + i16 * 4);
    const float og1 = *(const LAS float*)((lcp)smem + TL_G + wid * 128 + 64 + i16 * 4);
    const unsigned slo = (G & 1) * 32 + (G >> 1) * 16 + i16;
    const char* bp = ws + WS_BP + (size_t)((kq * 64 + m32) * 8) * 5120;
#pragma unroll
    for (int j = 3; j >= 0; --j)
#pragma unroll
      for (int r = 0; r < 16; ++r) acc[j][r] *= (r & 4) ? og1 : og0;
#pragma unroll
    for (int r = 0; r < 4; ++r) {
      const auto sw = __builtin_amdgcn_permlane32_swap(__float_as_uint(r0[r] * og0), __float_as_uint(r1[r] * og1), false, false);
      r0[r] = __uint_as_float(sw[0]); r1[r] = __uint_as_float(sw[1]);
    }
    auto pk8s = [&](const f32x16& a, int r) {
      const auto s0 = __builtin_amdgcn_permlane16_swap(cvtpk(a[r], a[r + 1]), cvtpk(a[r + 4], a[r + 5]), false, false);
      const auto s1 = __builtin_amdgcn_permlane16_swap(cvtpk(a[r + 2], a[r + 3]), cvtpk(a[r + 6], a[r + 7]), false, false);
      return u32x4{s0[0], s1[0], s0[1], s1[1]};
    };
#pragma unroll
    for (int pr = 0; pr < 4; ++pr) GSTC(avo, pk8s(acc[2 + (pr >> 1)], 8 * (pr & 1)), bp + (sl ^ 1) * 5120 + pr * 1024);
    GSTC(slo * 16, PK4(r1, 0), bp + (sl ^ 1) * 5120 + 4096);
    PAD();
    if (!own) {
#pragma unroll
      for (int pr = 0; pr < 4; ++pr) GSTC(avo, pk8s(acc[pr >> 1], 8 * (pr & 1)), bp + sl * 5120 + pr * 1024);
      GSTC(slo * 16, PK4(r0, 0), bp + sl * 5120 + 4096);
      acc[0] = f32x16{}; acc[1] = f32x16{}; r0 = f32x4{};
    } else {
#pragma unroll
      for (int j = 0; j < 16; ++j) {
        const int r = (j & 3) + 2 * (j & 4);
        const auto sw = __builtin_amdgcn_permlane16_swap(__float_as_uint(acc[j >> 3][r]), __float_as_uint(acc[j >> 3][r + 4]), false, false);
        acc[j >> 3][r] = __uint_as_float(sw[0]); acc[j >> 3][r + 4] = __uint_as_float(sw[1]);
      }
    }
    a0 = acc[0]; a1 = acc[1]; a2 = r0;
  }
  asm volatile("s_waitcnt vmcnt(0)" ::: "memory");
  const int ln = lane_id();
  unsigned old = 0u;
  if (ln == 0) asm volatile("global_atomic_add %0, %1, %2, %3 sc0" : "+v"(old)
                            : "v"(0u), "v"(1u), "s"(ws + WS_SY + SY_B + ((m32 >> 1) * 4 + nt) * 128) : "memory");
  TS(2, 2);
  PAD();
  const int i16 = ln & 15, G = ln >> 4;
  const unsigned avo = ln * 16;
  const unsigned pmask = (i16 >> 3) == (G & 1) ? 0xffffffffu : 0u;
  u32x4 rw[2][3];
#pragma unroll
  for (int r = 0; r < 2; ++r)
#pragma unroll
    for (int pn = 0; pn < 3; ++pn)
#pragma unroll
      for (int jj = 0; jj < 4; ++jj) {
        const uint16_t* wp = wo + (size_t)(288 * kh + 72 * r + 18 * jj + 16) * D + 72 * sl + 32 * pn;
        const int vi = (G >> 1) * (144 * D) + (pn < 2 ? (ln & 31) : (i16 & 7));
        const unsigned v = (unsigned)wp[vi] | ((unsigned)wp[vi + D] << 16);
        rw[r][pn][jj] = pn < 2 ? v : (v & pmask);
      }
  PAD();
  u32x4 ie, io, i6;
#pragma unroll
  for (int j = 0; j < 4; ++j) {
    const int d = (ln & 31) - 4 * (G >> 1) - 2 * j - 4 * (j >> 1), e = i16 - 8 * (G & 1) - 4 * (G >> 1) - 2 * j;
    ie[j] = d == 0 ? 0x3f80u : d == 1 ? 0x3f800000u : 0u;
    io[j] = d == 16 ? 0x3f80u : d == 17 ? 0x3f800000u : 0u;
    i6[j] = j >= 2 ? 0u : e == 0 ? 0x3f80u : e == 1 ? 0x3f800000u : 0u;
  }
  asm volatile("s_waitcnt vmcnt(0)" : "+v"(old) :: "memory");
  if (__builtin_amdgcn_readfirstlane(old) == 7u) cmask = 3u << (8 * kh);
  asm volatile("77:\n\ts_cbranch_execz 77f\n\ts_barrier"
               : "+{v[36:39]}"(rw[0][0]), "+{v[40:43]}"(rw[0][1]), "+{v[44:47]}"(rw[0][2]), "+{v[48:51]}"(rw[1][0]),
                 "+{v[52:55]}"(rw[1][1]), "+{v[56:59]}"(rw[1][2]), "+{v[100:103]}"(ie), "+{v[104:107]}"(io), "+{v[108:111]}"(i6),
                 "+{v[0:15]}"(a0), "+{v[16:31]}"(a1), "+{v[32:35]}"(a2) :: "memory");
  const char* pp = ws + WS_BP + (size_t)((m64 * 2 + m) * 8 + sl) * 5120;
  constexpr size_t KQS = (size_t)64 * 8 * 5120;
  bool ok = __builtin_amdgcn_readfirstlane(stw[0]) != 0u;
  TS(2, 3);
  if (ok) {
    asm volatile(
      "s_cmp_eq_u32 %[kh], %[kq2]\n\t"
      "s_cbranch_scc1 .Lpa%=\n\t"
      "global_load_dwordx4 v[80:83], v124, %[py] offset:-2048 sc1\n\t"
      "global_load_dwordx4 v[84:87], v124, %[py] offset:-1024 sc1\n\t"
      "global_load_dwordx4 v[88:91], v124, %[py] offset:0 sc1\n\t"
      "global_load_dwordx4 v[92:95], v124, %[py] offset:1024 sc1\n\t"
      "global_load_dwordx4 v[96:99], v124, %[py] offset:2048 sc1\n\t"
      ".Lpa%=:\n\t"
      "global_load_dwordx4 v[60:63], v124, %[px] offset:-2048 sc1\n\t"
      "global_load_dwordx4 v[64:67], v124, %[px] offset:-1024 sc1\n\t"
      "global_load_dwordx4 v[68:71], v124, %[px] offset:0 sc1\n\t"
      "global_load_dwordx4 v[72:75], v124, %[px] offset:1024 sc1\n\t"
      "global_load_dwordx4 v[76:79], v124, %[px] offset:2048 sc1\n\t"
      "s_cbranch_scc1 .Lpb%=\n\t"
      "s_waitcnt vmcnt(5)\n\t"
      "v_mfma_f32_32x32x16_bf16 v[0:15], v[100:103], v[80:83], v[0:15]\n\t"
      "v_mfma_f32_32x32x16_bf16 v[16:31], v[100:103], v[88:91], v[16:31]\n\t"
      "v_mfma_f32_16x16x32_bf16 v[32:35], v[108:111], v[96:99], v[32:35]\n\t"
      "v_mfma_f32_32x32x16_bf16 v[0:15], v[104:107], v[84:87], v[0:15]\n\t"
      "v_mfma_f32_32x32x16_bf16 v[16:31], v[104:107], v[92:95], v[16:31]\n\t"
      ".Lpb%=:\n\t"
      "s_waitcnt vmcnt(0)\n\t"
      "v_mfma_f32_32x32x16_bf16 v[0:15], v[100:103], v[60:63], v[0:15]\n\t"
      "v_mfma_f32_32x32x16_bf16 v[16:31], v[100:103], v[68:71], v[16:31]\n\t"
      "v_mfma_f32_16x16x32_bf16 v[32:35], v[108:111], v[76:79], v[32:35]\n\t"
      "v_mfma_f32_32x32x16_bf16 v[0:15], v[104:107], v[64:67], v[0:15]\n\t"
      "v_mfma_f32_32x32x16_bf16 v[16:31], v[104:107], v[72:75], v[16:31]\n\t"
      "s_nop 7\n\t"
      "s_nop 7\n\t"
      : "+{v[0:15]}"(a0), "+{v[16:31]}"(a1), "+{v[32:35]}"(a2)
      : "{v[100:103]}"(ie), "{v[104:107]}"(io), "{v[108:111]}"(i6), "{v124}"(avo), [kh] "s"(kh), [kq2] "s"(kq >> 1),
        [px] "s"(pp + ((kq + (own ? 3 : 1)) & 3) * KQS + 2048), [py] "s"(pp + ((kq + 2) & 3) * KQS + 2048)
      : VCL10(6), VCL10(7), VCL10(8), VCL10(9), "scc", "memory");
  }
  const char* om = ws + WS_OT + (size_t)(m64 * 2 + m) * (36 * 1024) + kh * 16384;
  const char* orr = ws + WS_OT + (size_t)(m64 * 2 + m) * (36 * 1024) + (32 + 2 * kh) * 1024;
  unsigned t0p = lds0 + kh * 32768 + (4 * (G >> 1) + (i16 >> 2)) * 64 + (16 * (G & 1) + 4 * (i16 & 3)) * 2;
  unsigned t2p = lds0 + TL_P2 + kh * 4096 + (4 * (G >> 1) + (i16 >> 2)) * 16 + 8 * (i16 & 1);
  unsigned md = 0u;
  if (ok) {
    if (ln == 0) __hip_atomic_fetch_add((LAS unsigned*)stw + 3, 1u, __ATOMIC_RELAXED, __HIP_MEMORY_SCOPE_WORKGROUP);
    asm volatile("s_barrier" : "+v"(a0), "+v"(a1), "+v"(a2), "+v"(t0p), "+v"(t2p), "+s"(om), "+s"(orr) :: "memory");
    md = __builtin_amdgcn_readfirstlane(stw[1]);
  }
  TS(2, 4);
  SEG(2);
  if (__builtin_expect(md != 0u, 1)) {
    asm volatile(
    "global_load_dword v60, v127, s[42:43] offset:0 sc1\n\t"
    "global_load_dword v61, v127, s[42:43] offset:128 sc1\n\t"
    "global_load_dword v62, v127, s[42:43] offset:256 sc1\n\t"
    "global_load_dword v63, v127, s[42:43] offset:384 sc1\n\t"
    "global_load_dwordx4 v[68:71], v124, s[40:41] offset:-4096 sc1\n\t"
    "global_load_dwordx4 v[72:75], v124, s[40:41] offset:-3072 sc1\n\t"
    "global_load_dwordx4 v[76:79], v124, s[40:41] offset:-2048 sc1\n\t"
    "global_load_dwordx4 v[80:83], v124, s[40:41] offset:-1024 sc1\n\t"
    "global_load_dwordx4 v[84:87], v124, s[46:47] offset:-4096 sc1\n\t"
    "global_load_dwordx4 v[88:91], v124, s[46:47] offset:-3072 sc1\n\t"
    "global_load_dwordx4 v[92:95], v124, s[46:47] offset:-2048 sc1\n\t"
    "global_load_dwordx4 v[96:99], v124, s[46:47] offset:-1024 sc1\n\t"
    "ds_read_b64_tr_b16 v[108:109], v125 offset:0\n\t"
    "ds_read_b64_tr_b16 v[110:111], v125 offset:512\n\t"
    "ds_read_b64_tr_b16 v[112:113], v125 offset:1024\n\t"
    "ds_read_b64_tr_b16 v[114:115], v125 offset:1536\n\t"
    "ds_read_b64_tr_b16 v[116:117], v126 offset:0\n\t"
    "ds_read_b64_tr_b16 v[118:119], v126 offset:128\n\t"
    "s_cmp_eq_u32 %[md], 2\n\t"
    "s_cbranch_scc0 .Lts%=\n\t"
    "global_load_dword v100, v127, s[42:43] offset:512 sc1\n\t"
    "global_load_dword v101, v127, s[42:43] offset:640 sc1\n\t"
    "global_load_dword v102, v127, s[42:43] offset:768 sc1\n\t"
    "global_load_dword v103, v127, s[42:43] offset:896 sc1\n\t"
    "global_load_dwordx4 v[104:107], v124, s[40:41] offset:0 sc1\n\t"
    "s_waitcnt vmcnt(13)\n\t"
    "v_mfma_f32_32x32x16_bf16 v[0:15], v[36:39], v[60:63], v[0:15]\n\t"
    "v_mfma_f32_32x32x16_bf16 v[16:31], v[40:43], v[60:63], v[16:31]\n\t"
    "v_mfma_f32_16x16x32_bf16 v[32:35], v[44:47], v[60:63], v[32:35]\n\t"
    "s_nop 3\n\t"
    "global_load_dwordx4 v[36:39], v124, s[40:41] offset:1024 sc1\n\t"
    "global_load_dwordx4 v[40:43], v124, s[40:41] offset:2048 sc1\n\t"
    "global_load_dwordx4 v[44:47], v124, s[40:41] offset:3072 sc1\n\t"
    "ds_read_b64_tr_b16 v[60:61], v125 offset:2048\n\t"
    "ds_read_b64_tr_b16 v[62:63], v125 offset:2560\n\t"
    "ds_read_b64_tr_b16 v[64:65], v125 offset:3072\n\t"
    "ds_read_b64_tr_b16 v[66:67], v125 offset:3584\n\t"
    "ds_read_b64_tr_b16 v[120:121], v126 offset:256\n\t"
    "ds_read_b64_tr_b16 v[122:123], v126 offset:384\n\t"
    "s_waitcnt lgkmcnt(6)\n\t"
    "v_cndmask_b32_e64 v116, 0, v116, s[44:45]\n\t"
    "v_cndmask_b32_e64 v117, 0, v117, s[44:45]\n\t"
    "v_cndmask_b32_e64 v118, 0, v118, s[44:45]\n\t"
    "v_cndmask_b32_e64 v119, 0, v119, s[44:45]\n\t"
    "s_waitcnt vmcnt(15)\n\t"
    "v_mfma_f32_32x32x16_bf16 v[0:15], v[108:111], v[68:71], v[0:15]\n\t"
    "v_mfma_f32_32x32x16_bf16 v[16:31], v[112:115], v[68:71], v[16:31]\n\t"
    "v_mfma_f32_16x16x32_bf16 v[32:35], v[116:119], v[68:71], v[32:35]\n\t"
    "ds_read_b64_tr_b16 v[108:109], v125 offset:4096\n\t"
    "ds_read_b64_tr_b16 v[110:111], v125 offset:4608\n\t"
    "ds_read_b64_tr_b16 v[112:113], v125 offset:5120\n\t"
    "ds_read_b64_tr_b16 v[114:115], v125 offset:5632\n\t"
    "ds_read_b64_tr_b16 v[116:117], v126 offset:512\n\t"
    "ds_read_b64_tr_b16 v[118:119], v126 offset:640\n\t"
    "s_waitcnt lgkmcnt(6)\n\t"
    "v_cndmask_b32_e64 v120, 0, v120, s[44:45]\n\t"
    "v_cndmask_b32_e64 v121, 0, v121, s[44:45]\n\t"
    "v_cndmask_b32_e64 v122, 0, v122, s[44:45]\n\t"
    "v_cndmask_b32_e64 v123, 0, v123, s[44:45]\n\t"
    "s_waitcnt vmcnt(14)\n\t"
    "v_mfma_f32_32x32x16_bf16 v[0:15], v[60:63], v[72:75], v[0:15]\n\t"
    "v_mfma_f32_32x32x16_bf16 v[16:31], v[64:67], v[72:75], v[16:31]\n\t"
    "v_mfma_f32_16x16x32_bf16 v[32:35], v[120:123], v[72:75], v[32:35]\n\t"
    "global_load_dwordx4 v[68:71], v124, s[46:47] offset:0 sc1\n\t"
    "ds_read_b64_tr_b16 v[60:61], v125 offset:6144\n\t"
    "ds_read_b64_tr_b16 v[62:63], v125 offset:6656\n\t"
    "ds_read_b64_tr_b16 v[64:65], v125 offset:7168\n\t"
    "ds_read_b64_tr_b16 v[66:67], v125 offset:7680\n\t"
    "ds_read_b64_tr_b16 v[120:121], v126 offset:768\n\t"
    "ds_read_b64_tr_b16 v[122:123], v126 offset:896\n\t"
    "s_waitcnt lgkmcnt(6)\n\t"
    "77:\n\t"
    "s_cbranch_execz 77f\n\t"
    "v_cndmask_b32_e64 v116, 0, v116, s[44:45]\n\t"
    "v_cndmask_b32_e64 v117, 0, v117, s[44:45]\n\t"
    "v_cndmask_b32_e64 v118, 0, v118, s[44:45]\n\t"
    "v_cndmask_b32_e64 v119, 0, v119, s[44:45]\n\t"
    "s_waitcnt vmcnt(14)\n\t"
    "v_mfma_f32_32x32x16_bf16 v[0:15], v[108:111], v[76:79], v[0:15]\n\t"
    "v_mfma_f32_32x32x16_bf16 v[16:31], v[112:115], v[76:79], v[16:31]\n\t"
    "v_mfma_f32_16x16x32_bf16 v[32:35], v[116:119], v[76:79], v[32:35]\n\t"
    "global_load_dwordx4 v[72:75], v124, s[46:47] offset:1024 sc1\n\t"
    "ds_read_b64_tr_b16 v[108:109], v125 offset:16384\n\t"
    "ds_read_b64_tr_b16 v[110:111], v125 offset:16896\n\t"
    "ds_read_b64_tr_b16 v[112:113], v125 offset:17408\n\t"
    "ds_read_b64_tr_b16 v[114:115], v125 offset:17920\n\t"
    "ds_read_b64_tr_b16 v[116:117], v126 offset:2048\n\t"
    "ds_read_b64_tr_b16 v[118:119], v126 offset:2176\n\t"
    "s_waitcnt lgkmcnt(6)\n\t"
    "v_cndmask_b32_e64 v120, 0, v120, s[44:45]\n\t"
    "v_cndmask_b32_e64 v121, 0, v121, s[44:45]\n\t"
    "v_cndmask_b32_e64 v122, 0, v122, s[44:45]\n\t"
    "v_cndmask_b32_e64 v123, 0, v123, s[44:45]\n\t"
    "s_waitcnt vmcnt(14)\n\t"
    "v_mfma_f32_32x32x16_bf16 v[0:15], v[60:63], v[80:83], v[0:15]\n\t"
    "v_mfma_f32_32x32x16_bf16 v[16:31], v[64:67], v[80:83], v[16:31]\n\t"
    "v_mfma_f32_16x16x32_bf16 v[32:35], v[120:123], v[80:83], v[32:35]\n\t"
    "global_load_dwordx4 v[76:79], v124, s[46:47] offset:2048 sc1\n\t"
    "ds_read_b64_tr_b16 v[60:61], v125 offset:18432\n\t"
    "ds_read_b64_tr_b16 v[62:63], v125 offset:18944\n\t"
    "ds_read_b64_tr_b16 v[64:65], v125 offset:19456\n\t"
    "ds_read_b64_tr_b16 v[66:67], v125 offset:19968\n\t"
    "ds_read_b64_tr_b16 v[120:121], v126 offset:2304\n\t"
    "ds_read_b64_tr_b16 v[122:123], v126 offset:2432\n\t"
    "s_waitcnt lgkmcnt(6)\n\t"
    "v_cndmask_b32_e64 v116, 0, v116, s[44:45]\n\t"
    "v_cndmask_b32_e64 v117, 0, v117, s[44:45]\n\t"
    "v_cndmask_b32_e64 v118, 0, v118, s[44:45]\n\t"
    "v_cndmask_b32_e64 v119, 0, v119, s[44:45]\n\t"
    "s_waitcnt vmcnt(14)\n\t"
    "v_mfma_f32_32x32x16_bf16 v[0:15], v[108:111], v[84:87], v[0:15]\n\t"
    "v_mfma_f32_32x32x16_bf16 v[16:31], v[112:115], v[84:87], v[16:31]\n\t"
    "v_mfma_f32_16x16x32_bf16 v[32:35], v[116:119], v[84:87], v[32:35]\n\t"
    "global_load_dwordx4 v[80:83], v124, s[46:47] offset:3072 sc1\n\t"
    "ds_read_b64_tr_b16 v[108:109], v125 offset:20480\n\t"
    "ds_read_b64_tr_b16 v[110:111], v125 offset:20992\n\t"
    "ds_read_b64_tr_b16 v[112:113], v125 offset:21504\n\t"
    "ds_read_b64_tr_b16 v[114:115], v125 offset:22016\n\t"
    "ds_read_b64_tr_b16 v[116:117], v126 offset:2560\n\t"
    "ds_read_b64_tr_b16 v[118:119], v126 offset:2688\n\t"
    "s_waitcnt lgkmcnt(6)\n\t"
    "v_cndmask_b32_e64 v120, 0, v120, s[44:45]\n\t"
    "v_cndmask_b32_e64 v121, 0, v121, s[44:45]\n\t"
    "v_cndmask_b32_e64 v122, 0, v122, s[44:45]\n\t"
    "v_cndmask_b32_e64 v123, 0, v123, s[44:45]\n\t"
    "s_waitcnt vmcnt(14)\n\t"
    "v_mfma_f32_32x32x16_bf16 v[0:15], v[60:63], v[88:91], v[0:15]\n\t"
    "v_mfma_f32_32x32x16_bf16 v[16:31], v[64:67], v[88:91], v[16:31]\n\t"
    "v_mfma_f32_16x16x32_bf16 v[32:35], v[120:123], v[88:91], v[32:35]\n\t"
    "ds_read_b64_tr_b16 v[60:61], v125 offset:22528\n\t"
    "ds_read_b64_tr_b16 v[62:63], v125 offset:23040\n\t"
    "ds_read_b64_tr_b16 v[64:65], v125 offset:23552\n\t"
    "ds_read_b64_tr_b16 v[66:67], v125 offset:24064\n\t"
    "ds_read_b64_tr_b16 v[120:121], v126 offset:2816\n\t"
    "ds_read_b64_tr_b16 v[122:123], v126 offset:2944\n\t"
    "s_waitcnt lgkmcnt(6)\n\t"
    "77:\n\t"
    "s_cbranch_execz 77f\n\t"
    "v_cndmask_b32_e64 v116, 0, v116, s[44:45]\n\t"
    "v_cndmask_b32_e64 v117, 0, v117, s[44:45]\n\t"
    "v_cndmask_b32_e64 v118, 0, v118, s[44:45]\n\t"
    "v_cndmask_b32_e64 v119, 0, v119, s[44:45]\n\t"
    "s_waitcnt vmcnt(13)\n\t"
    "v_mfma_f32_32x32x16_bf16 v[0:15], v[108:111], v[92:95], v[0:15]\n\t"
    "v_mfma_f32_32x32x16_bf16 v[16:31], v[112:115], v[92:95], v[16:31]\n\t"
    "v_mfma_f32_16x16x32_bf16 v[32:35], v[116:119], v[92:95], v[32:35]\n\t"
    "ds_read_b64_tr_b16 v[108:109], v125 offset:8192\n\t"
    "ds_read_b64_tr_b16 v[110:111], v125 offset:8704\n\t"
    "ds_read_b64_tr_b16 v[112:113], v125 offset:9216\n\t"
    "ds_read_b64_tr_b16 v[114:115], v125 offset:9728\n\t"
    "ds_read_b64_tr_b16 v[116:117], v126 offset:1024\n\t"
    "ds_read_b64_tr_b16 v[118:119], v126 offset:1152\n\t"
    "s_waitcnt lgkmcnt(6)\n\t"
    "v_cndmask_b32_e64 v120, 0, v120, s[44:45]\n\t"
    "v_cndmask_b32_e64 v121, 0, v121, s[44:45]\n\t"
    "v_cndmask_b32_e64 v122, 0, v122, s[44:45]\n\t"
    "v_cndmask_b32_e64 v123, 0, v123, s[44:45]\n\t"
    "s_waitcnt vmcnt(12)\n\t"
    "v_mfma_f32_32x32x16_bf16 v[0:15], v[60:63], v[96:99], v[0:15]\n\t"
    "v_mfma_f32_32x32x16_bf16 v[16:31], v[64:67], v[96:99], v[16:31]\n\t"
    "v_mfma_f32_16x16x32_bf16 v[32:35], v[120:123], v[96:99], v[32:35]\n\t"
    "s_branch .Ltj%=\n\t"
    ".Lts%=:\n\t"
    "77:\n\t"
    "s_cbranch_execz 77f\n\t"
    "s_waitcnt vmcnt(8)\n\t"
    "v_mfma_f32_32x32x16_bf16 v[0:15], v[36:39], v[60:63], v[0:15]\n\t"
    "v_mfma_f32_32x32x16_bf16 v[16:31], v[40:43], v[60:63], v[16:31]\n\t"
    "v_mfma_f32_16x16x32_bf16 v[32:35], v[44:47], v[60:63], v[32:35]\n\t"
    "ds_read_b64_tr_b16 v[60:61], v125 offset:2048\n\t"
    "ds_read_b64_tr_b16 v[62:63], v125 offset:2560\n\t"
    "ds_read_b64_tr_b16 v[64:65], v125 offset:3072\n\t"
    "ds_read_b64_tr_b16 v[66:67], v125 offset:3584\n\t"
    "ds_read_b64_tr_b16 v[120:121], v126 offset:256\n\t"
    "ds_read_b64_tr_b16 v[122:123], v126 offset:384\n\t"
    "s_waitcnt lgkmcnt(6)\n\t"
    "v_cndmask_b32_e64 v116, 0, v116, s[44:45]\n\t"
    "v_cndmask_b32_e64 v117, 0, v117, s[44:45]\n\t"
    "v_cndmask_b32_e64 v118, 0, v118, s[44:45]\n\t"
    "v_cndmask_b32_e64 v119, 0, v119, s[44:45]\n\t"
    "s_waitcnt vmcnt(7)\n\t"
    "v_mfma_f32_32x32x16_bf16 v[0:15], v[108:111], v[68:71], v[0:15]\n\t"
    "v_mfma_f32_32x32x16_bf16 v[16:31], v[112:115], v[68:71], v[16:31]\n\t"
    "v_mfma_f32_16x16x32_bf16 v[32:35], v[116:119], v[68:71], v[32:35]\n\t"
    "ds_read_b64_tr_b16 v[108:109], v125 offset:4096\n\t"
    "ds_read_b64_tr_b16 v[110:111], v125 offset:4608\n\t"
    "ds_read_b64_tr_b16 v[112:113], v125 offset:5120\n\t"
    "ds_read_b64_tr_b16 v[114:115], v125 offset:5632\n\t"
    "ds_read_b64_tr_b16 v[116:117], v126 offset:512\n\t"
    "ds_read_b64_tr_b16 v[118:119], v126 offset:640\n\t"
    "s_waitcnt lgkmcnt(6)\n\t"
    "v_cndmask_b32_e64 v120, 0, v120, s[44:45]\n\t"
    "v_cndmask_b32_e64 v121, 0, v121, s[44:45]\n\t"
    "v_cndmask_b32_e64 v122, 0, v122, s[44:45]\n\t"
    "v_cndmask_b32_e64 v123, 0, v123, s[44:45]\n\t"
    "s_waitcnt vmcnt(6)\n\t"
    "v_mfma_f32_32x32x16_bf16 v[0:15], v[60:63], v[72:75], v[0:15]\n\t"
    "v_mfma_f32_32x32x16_bf16 v[16:31], v[64:67], v[72:75], v[16:31]\n\t"
    "v_mfma_f32_16x16x32_bf16 v[32:35], v[120:123], v[72:75], v[32:35]\n\t"
    "ds_read_b64_tr_b16 v[60:61], v125 offset:6144\n\t"
    "ds_read_b64_tr_b16 v[62:63], v125 offset:6656\n\t"
    "ds_read_b64_tr_b16 v[64:65], v125 offset:7168\n\t"
    "ds_read_b64_tr_b16 v[66:67], v125 offset:7680\n\t"
    "ds_read_b64_tr_b16 v[120:121], v126 offset:768\n\t"
    "ds_read_b64_tr_b16 v[122:123], v126 offset:896\n\t"
    "s_waitcnt lgkmcnt(6)\n\t"
    "77:\n\t"
    "s_cbranch_execz 77f\n\t"
    "v_cndmask_b32_e64 v116, 0, v116, s[44:45]\n\t"
    "v_cndmask_b32_e64 v117, 0, v117, s[44:45]\n\t"
    "v_cndmask_b32_e64 v118, 0, v118, s[44:45]\n\t"
    "v_cndmask_b32_e64 v119, 0, v119, s[44:45]\n\t"
    "s_waitcnt vmcnt(5)\n\t"
    "v_mfma_f32_32x32x16_bf16 v[0:15], v[108:111], v[76:79], v[0:15]\n\t"
    "v_mfma_f32_32x32x16_bf16 v[16:31], v[112:115], v[76:79], v[16:31]\n\t"
    "v_mfma_f32_16x16x32_bf16 v[32:35], v[116:119], v[76:79], v[32:35]\n\t"
    "ds_read_b64_tr_b16 v[108:109], v125 offset:16384\n\t"
    "ds_read_b64_tr_b16 v[110:111], v125 offset:16896\n\t"
    "ds_read_b64_tr_b16 v[112:113], v125 offset:17408\n\t"
    "ds_read_b64_tr_b16 v[114:115], v125 offset:17920\n\t"
    "ds_read_b64_tr_b16 v[116:117], v126 offset:2048\n\t"
    "ds_read_b64_tr_b16 v[118:119], v126 offset:2176\n\t"
    "s_waitcnt lgkmcnt(6)\n\t"
    "v_cndmask_b32_e64 v120, 0, v120, s[44:45]\n\t"
    "v_cndmask_b32_e64 v121, 0, v121, s[44:45]\n\t"
    "v_cndmask_b32_e64 v122, 0, v122, s[44:45]\n\t"
    "v_cndmask_b32_e64 v123, 0, v123, s[44:45]\n\t"
    "s_waitcnt vmcnt(4)\n\t"
    "v_mfma_f32_32x32x16_bf16 v[0:15], v[60:63], v[80:83], v[0:15]\n\t"
    "v_mfma_f32_32x32x16_bf16 v[16:31], v[64:67], v[80:83], v[16:31]\n\t"
    "v_mfma_f32_16x16x32_bf16 v[32:35], v[120:123], v[80:83], v[32:35]\n\t"
    "ds_read_b64_tr_b16 v[60:61], v125 offset:18432\n\t"
    "ds_read_b64_tr_b16 v[62:63], v125 offset:18944\n\t"
    "ds_read_b64_tr_b16 v[64:65], v125 offset:19456\n\t"
    "ds_read_b64_tr_b16 v[66:67], v125 offset:19968\n\t"
    "ds_read_b64_tr_b16 v[120:121], v126 offset:2304\n\t"
    "ds_read_b64_tr_b16 v[122:123], v126 offset:2432\n\t"
    "s_waitcnt lgkmcnt(6)\n\t"
    "v_cndmask_b32_e64 v116, 0, v116, s[44:45]\n\t"
    "v_cndmask_b32_e64 v117, 0, v117, s[44:45]\n\t"
    "v_cndmask_b32_e64 v118, 0, v118, s[44:45]\n\t"
    "v_cndmask_b32_e64 v119, 0, v119, s[44:45]\n\t"
    "s_waitcnt vmcnt(3)\n\t"
    "v_mfma_f32_32x32x16_bf16 v[0:15], v[108:111], v[84:87], v[0:15]\n\t"
    "v_mfma_f32_32x32x16_bf16 v[16:31], v[112:115], v[84:87], v[16:31]\n\t"
    "v_mfma_f32_16x16x32_bf16 v[32:35], v[116:119], v[84:87], v[32:35]\n\t"
    "ds_read_b64_tr_b16 v[108:109], v125 offset:20480\n\t"
    "ds_read_b64_tr_b16 v[110:111], v125 offset:20992\n\t"
    "ds_read_b64_tr_b16 v[112:113], v125 offset:21504\n\t"
    "ds_read_b64_tr_b16 v[114:115], v125 offset:22016\n\t"
    "ds_read_b64_tr_b16 v[116:117], v126 offset:2560\n\t"
    "ds_read_b64_tr_b16 v[118:119], v126 offset:2688\n\t"
    "s_waitcnt lgkmcnt(6)\n\t"
    "v_cndmask_b32_e64 v120, 0, v120, s[44:45]\n\t"
    "v_cndmask_b32_e64 v121, 0, v121, s[44:45]\n\t"
    "v_cndmask_b32_e64 v122, 0, v122, s[44:45]\n\t"
    "v_cndmask_b32_e64 v123, 0, v123, s[44:45]\n\t"
    "s_waitcnt vmcnt(2)\n\t"
    "v_mfma_f32_32x32x16_bf16 v[0:15], v[60:63], v[88:91], v[0:15]\n\t"
    "v_mfma_f32_32x32x16_bf16 v[16:31], v[64:67], v[88:91], v[16:31]\n\t"
    "v_mfma_f32_16x16x32_bf16 v[32:35], v[120:123], v[88:91], v[32:35]\n\t"
    "ds_read_b64_tr_b16 v[60:61], v125 offset:22528\n\t"
    "ds_read_b64_tr_b16 v[62:63], v125 offset:23040\n\t"
    "ds_read_b64_tr_b16 v[64:65], v125 offset:23552\n\t"
    "ds_read_b64_tr_b16 v[66:67], v125 offset:24064\n\t"
    "ds_read_b64_tr_b16 v[120:121], v126 offset:2816\n\t"
    "ds_read_b64_tr_b16 v[122:123], v126 offset:2944\n\t"
    "s_waitcnt lgkmcnt(6)\n\t"
    "77:\n\t"
    "s_cbranch_execz 77f\n\t"
    "v_cndmask_b32_e64 v116, 0, v116, s[44:45]\n\t"
    "v_cndmask_b32_e64 v117, 0, v117, s[44:45]\n\t"
    "v_cndmask_b32_e64 v118, 0, v118, s[44:45]\n\t"
    "v_cndmask_b32_e64 v119, 0, v119, s[44:45]\n\t"
    "s_waitcnt vmcnt(1)\n\t"
    "v_mfma_f32_32x32x16_bf16 v[0:15], v[108:111], v[92:95], v[0:15]\n\t"
    "v_mfma_f32_32x32x16_bf16 v[16:31], v[112:115], v[92:95], v[16:31]\n\t"
    "v_mfma_f32_16x16x32_bf16 v[32:35], v[116:119], v[92:95], v[32:35]\n\t"
    "ds_read_b64_tr_b16 v[108:109], v125 offset:8192\n\t"
    "ds_read_b64_tr_b16 v[110:111], v125 offset:8704\n\t"
    "ds_read_b64_tr_b16 v[112:113], v125 offset:9216\n\t"
    "ds_read_b64_tr_b16 v[114:115], v125 offset:9728\n\t"
    "ds_read_b64_tr_b16 v[116:117], v126 offset:1024\n\t"
    "ds_read_b64_tr_b16 v[118:119], v126 offset:1152\n\t"
    "s_waitcnt lgkmcnt(6)\n\t"
    "v_cndmask_b32_e64 v120, 0, v120, s[44:45]\n\t"
    "v_cndmask_b32_e64 v121, 0, v121, s[44:45]\n\t"
    "v_cndmask_b32_e64 v122, 0, v122, s[44:45]\n\t"
    "v_cndmask_b32_e64 v123, 0, v123, s[44:45]\n\t"
    "s_waitcnt vmcnt(0)\n\t"
    "v_mfma_f32_32x32x16_bf16 v[0:15], v[60:63], v[96:99], v[0:15]\n\t"
    "v_mfma_f32_32x32x16_bf16 v[16:31], v[64:67], v[96:99], v[16:31]\n\t"
    "v_mfma_f32_16x16x32_bf16 v[32:35], v[120:123], v[96:99], v[32:35]\n\t"
    "v_mov_b32_e32 v68, %[stw]\n\t"
    "s_barrier\n\t"
    "ds_read_b32 v68, v68 offset:8\n\t"
    "s_waitcnt lgkmcnt(0)\n\t"
    "v_readfirstlane_b32 %[md], v68\n\t"
    "s_cmp_eq_u32 %[md], 0\n\t"
    "s_cbranch_scc1 .Ltz%=\n\t"
    "global_load_dword v100, v127, s[42:43] offset:512 sc1\n\t"
    "global_load_dword v101, v127, s[42:43] offset:640 sc1\n\t"
    "global_load_dword v102, v127, s[42:43] offset:768 sc1\n\t"
    "global_load_dword v103, v127, s[42:43] offset:896 sc1\n\t"
    "global_load_dwordx4 v[104:107], v124, s[40:41] offset:0 sc1\n\t"
    "global_load_dwordx4 v[36:39], v124, s[40:41] offset:1024 sc1\n\t"
    "global_load_dwordx4 v[40:43], v124, s[40:41] offset:2048 sc1\n\t"
    "global_load_dwordx4 v[44:47], v124, s[40:41] offset:3072 sc1\n\t"
    "global_load_dwordx4 v[68:71], v124, s[46:47] offset:0 sc1\n\t"
    "global_load_dwordx4 v[72:75], v124, s[46:47] offset:1024 sc1\n\t"
    "global_load_dwordx4 v[76:79], v124, s[46:47] offset:2048 sc1\n\t"
    "global_load_dwordx4 v[80:83], v124, s[46:47] offset:3072 sc1\n\t"
    ".Ltj%=:\n\t"
    "s_waitcnt vmcnt(8)\n\t"
    "v_mfma_f32_32x32x16_bf16 v[0:15], v[48:51], v[100:103], v[0:15]\n\t"
    "v_mfma_f32_32x32x16_bf16 v[16:31], v[52:55], v[100:103], v[16:31]\n\t"
    "v_mfma_f32_16x16x32_bf16 v[32:35], v[56:59], v[100:103], v[32:35]\n\t"
    "ds_read_b64_tr_b16 v[60:61], v125 offset:10240\n\t"
    "ds_read_b64_tr_b16 v[62:63], v125 offset:10752\n\t"
    "ds_read_b64_tr_b16 v[64:65], v125 offset:11264\n\t"
    "ds_read_b64_tr_b16 v[66:67], v125 offset:11776\n\t"
    "ds_read_b64_tr_b16 v[120:121], v126 offset:1280\n\t"
    "ds_read_b64_tr_b16 v[122:123], v126 offset:1408\n\t"
    "s_waitcnt lgkmcnt(6)\n\t"
    "v_cndmask_b32_e64 v116, 0, v116, s[44:45]\n\t"
    "v_cndmask_b32_e64 v117, 0, v117, s[44:45]\n\t"
    "v_cndmask_b32_e64 v118, 0, v118, s[44:45]\n\t"
    "v_cndmask_b32_e64 v119, 0, v119, s[44:45]\n\t"
    "s_waitcnt vmcnt(7)\n\t"
    "v_mfma_f32_32x32x16_bf16 v[0:15], v[108:111], v[104:107], v[0:15]\n\t"
    "v_mfma_f32_32x32x16_bf16 v[16:31], v[112:115], v[104:107], v[16:31]\n\t"
    "v_mfma_f32_16x16x32_bf16 v[32:35], v[116:119], v[104:107], v[32:35]\n\t"
    "ds_read_b64_tr_b16 v[108:109], v125 offset:12288\n\t"
    "ds_read_b64_tr_b16 v[110:111], v125 offset:12800\n\t"
    "ds_read_b64_tr_b16 v[112:113], v125 offset:13312\n\t"
    "ds_read_b64_tr_b16 v[114:115], v125 offset:13824\n\t"
    "ds_read_b64_tr_b16 v[116:117], v126 offset:1536\n\t"
    "ds_read_b64_tr_b16 v[118:119], v126 offset:1664\n\t"
    "s_waitcnt lgkmcnt(6)\n\t"
    "v_cndmask_b32_e64 v120, 0, v120, s[44:45]\n\t"
    "v_cndmask_b32_e64 v121, 0, v121, s[44:45]\n\t"
    "v_cndmask_b32_e64 v122, 0, v122, s[44:45]\n\t"
    "v_cndmask_b32_e64 v123, 0, v123, s[44:45]\n\t"
    "s_waitcnt vmcnt(6)\n\t"
    "v_mfma_f32_32x32x16_bf16 v[0:15], v[60:63], v[36:39], v[0:15]\n\t"
    "v_mfma_f32_32x32x16_bf16 v[16:31], v[64:67], v[36:39], v[16:31]\n\t"
    "v_mfma_f32_16x16x32_bf16 v[32:35], v[120:123], v[36:39], v[32:35]\n\t"
    "ds_read_b64_tr_b16 v[60:61], v125 offset:14336\n\t"
    "ds_read_b64_tr_b16 v[62:63], v125 offset:14848\n\t"
    "ds_read_b64_tr_b16 v[64:65], v125 offset:15360\n\t"
    "ds_read_b64_tr_b16 v[66:67], v125 offset:15872\n\t"
    "ds_read_b64_tr_b16 v[120:121], v126 offset:1792\n\t"
    "ds_read_b64_tr_b16 v[122:123], v126 offset:1920\n\t"
    "s_waitcnt lgkmcnt(6)\n\t"
    "77:\n\t"
    "s_cbranch_execz 77f\n\t"
    "v_cndmask_b32_e64 v116, 0, v116, s[44:45]\n\t"
    "v_cndmask_b32_e64 v117, 0, v117, s[44:45]\n\t"
    "v_cndmask_b32_e64 v118, 0, v118, s[44:45]\n\t"
    "v_cndmask_b32_e64 v119, 0, v119, s[44:45]\n\t"
    "s_waitcnt vmcnt(5)\n\t"
    "v_mfma_f32_32x32x16_bf16 v[0:15], v[108:111], v[40:43], v[0:15]\n\t"
    "v_mfma_f32_32x32x16_bf16 v[16:31], v[112:115], v[40:43], v[16:31]\n\t"
    "v_mfma_f32_16x16x32_bf16 v[32:35], v[116:119], v[40:43], v[32:35]\n\t"
    "ds_read_b64_tr_b16 v[108:109], v125 offset:24576\n\t"
    "ds_read_b64_tr_b16 v[110:111], v125 offset:25088\n\t"
    "ds_read_b64_tr_b16 v[112:113], v125 offset:25600\n\t"
    "ds_read_b64_tr_b16 v[114:115], v125 offset:26112\n\t"
    "ds_read_b64_tr_b16 v[116:117], v126 offset:3072\n\t"
    "ds_read_b64_tr_b16 v[118:119], v126 offset:3200\n\t"
    "s_waitcnt lgkmcnt(6)\n\t"
    "v_cndmask_b32_e64 v120, 0, v120, s[44:45]\n\t"
    "v_cndmask_b32_e64 v121, 0, v121, s[44:45]\n\t"
    "v_cndmask_b32_e64 v122, 0, v122, s[44:45]\n\t"
    "v_cndmask_b32_e64 v123, 0, v123, s[44:45]\n\t"
    "s_waitcnt vmcnt(4)\n\t"
    "v_mfma_f32_32x32x16_bf16 v[0:15], v[60:63], v[44:47], v[0:15]\n\t"
    "v_mfma_f32_32x32x16_bf16 v[16:31], v[64:67], v[44:47], v[16:31]\n\t"
    "v_mfma_f32_16x16x32_bf16 v[32:35], v[120:123], v[44:47], v[32:35]\n\t"
    "ds_read_b64_tr_b16 v[60:61], v125 offset:26624\n\t"
    "ds_read_b64_tr_b16 v[62:63], v125 offset:27136\n\t"
    "ds_read_b64_tr_b16 v[64:65], v125 offset:27648\n\t"
    "ds_read_b64_tr_b16 v[66:67], v125 offset:28160\n\t"
    "ds_read_b64_tr_b16 v[120:121], v126 offset:3328\n\t"
    "ds_read_b64_tr_b16 v[122:123], v126 offset:3456\n\t"
    "s_waitcnt lgkmcnt(6)\n\t"
    "v_cndmask_b32_e64 v116, 0, v116, s[44:45]\n\t"
    "v_cndmask_b32_e64 v117, 0, v117, s[44:45]\n\t"
    "v_cndmask_b32_e64 v118, 0, v118, s[44:45]\n\t"
    "v_cndmask_b32_e64 v119, 0, v119, s[44:45]\n\t"
    "s_waitcnt vmcnt(3)\n\t"
    "v_mfma_f32_32x32x16_bf16 v[0:15], v[108:111], v[68:71], v[0:15]\n\t"
    "v_mfma_f32_32x32x16_bf16 v[16:31], v[112:115], v[68:71], v[16:31]\n\t"
    "v_mfma_f32_16x16x32_bf16 v[32:35], v[116:119], v[68:71], v[32:35]\n\t"
    "ds_read_b64_tr_b16 v[108:109], v125 offset:28672\n\t"
    "ds_read_b64_tr_b16 v[110:111], v125 offset:29184\n\t"
    "ds_read_b64_tr_b16 v[112:113], v125 offset:29696\n\t"
    "ds_read_b64_tr_b16 v[114:115], v125 offset:30208\n\t"
    "ds_read_b64_tr_b16 v[116:117], v126 offset:3584\n\t"
    "ds_read_b64_tr_b16 v[118:119], v126 offset:3712\n\t"
    "s_waitcnt lgkmcnt(6)\n\t"
    "v_cndmask_b32_e64 v120, 0, v120, s[44:45]\n\t"
    "v_cndmask_b32_e64 v121, 0, v121, s[44:45]\n\t"
    "v_cndmask_b32_e64 v122, 0, v122, s[44:45]\n\t"
    "v_cndmask_b32_e64 v123, 0, v123, s[44:45]\n\t"
    "s_waitcnt vmcnt(2)\n\t"
    "v_mfma_f32_32x32x16_bf16 v[0:15], v[60:63], v[72:75], v[0:15]\n\t"
    "v_mfma_f32_32x32x16_bf16 v[16:31], v[64:67], v[72:75], v[16:31]\n\t"
    "v_mfma_f32_16x16x32_bf16 v[32:35], v[120:123], v[72:75], v[32:35]\n\t"
    "ds_read_b64_tr_b16 v[60:61], v125 offset:30720\n\t"
    "ds_read_b64_tr_b16 v[62:63], v125 offset:31232\n\t"
    "ds_read_b64_tr_b16 v[64:65], v125 offset:31744\n\t"
    "ds_read_b64_tr_b16 v[66:67], v125 offset:32256\n\t"
    "ds_read_b64_tr_b16 v[120:121], v126 offset:3840\n\t"
    "ds_read_b64_tr_b16 v[122:123], v126 offset:3968\n\t"
    "s_waitcnt lgkmcnt(6)\n\t"
    "77:\n\t"
    "s_cbranch_execz 77f\n\t"
    "v_cndmask_b32_e64 v116, 0, v116, s[44:45]\n\t"
    "v_cndmask_b32_e64 v117, 0, v117, s[44:45]\n\t"
    "v_cndmask_b32_e64 v118, 0, v118, s[44:45]\n\t"
    "v_cndmask_b32_e64 v119, 0, v119, s[44:45]\n\t"
    "s_waitcnt vmcnt(1)\n\t"
    "v_mfma_f32_32x32x16_bf16 v[0:15], v[108:111], v[76:79], v[0:15]\n\t"
    "v_mfma_f32_32x32x16_bf16 v[16:31], v[112:115], v[76:79], v[16:31]\n\t"
    "v_mfma_f32_16x16x32_bf16 v[32:35], v[116:119], v[76:79], v[32:35]\n\t"
    "s_waitcnt lgkmcnt(0)\n\t"
    "v_cndmask_b32_e64 v120, 0, v120, s[44:45]\n\t"
    "v_cndmask_b32_e64 v121, 0, v121, s[44:45]\n\t"
    "v_cndmask_b32_e64 v122, 0, v122, s[44:45]\n\t"
    "v_cndmask_b32_e64 v123, 0, v123, s[44:45]\n\t"
    "s_waitcnt vmcnt(0)\n\t"
    "v_mfma_f32_32x32x16_bf16 v[0:15], v[60:63], v[80:83], v[0:15]\n\t"
    "v_mfma_f32_32x32x16_bf16 v[16:31], v[64:67], v[80:83], v[16:31]\n\t"
    "v_mfma_f32_16x16x32_bf16 v[32:35], v[120:123], v[80:83], v[32:35]\n\t"
    ".Ltz%=:\n\t"
    "s_nop 7\n\t"
    "s_nop 7\n\t"
      : "+{v[0:15]}"(a0), "+{v[16:31]}"(a1), "+{v[32:35]}"(a2), "+{v[36:39]}"(rw[0][0]), "+{v[40:43]}"(rw[0][1]),
        "+{v[44:47]}"(rw[0][2]), "+{v[48:51]}"(rw[1][0]), "+{v[52:55]}"(rw[1][1]), "+{v[56:59]}"(rw[1][2]), [md] "+s"(md)
      : "{s[40:41]}"(om + 4096), "{s[46:47]}"(om + 12288), "{s[42:43]}"(orr), "{v124}"(avo), "{v125}"(t0p), "{v126}"(t2p),
        "{v127}"((G >> 1) * 1024 + (ln & 31) * 4), "{s[44:45]}"(0xff0000ffff0000ffull), [stw] "s"(lds0 + TL_ST)
      : VCL10(6), VCL10(7), VCL10(8), VCL10(9), VCL10(10), VCL10(11), "v120", "v121", "v122", "v123", "scc", "memory");
  }
  if (__builtin_expect(md == 0u, 0)) {
    BAR();
    return Cand{16 * rb + 2 * nt, cmask};
  }
  TS(2, 5);
  PAD();
  asm volatile("s_waitcnt lgkmcnt(0)\n\ts_barrier" ::: "memory");
  {
    const lcp rp = (lcp)smem + wid * (9 * TL_US);
    const unsigned avo = lane_id() * 16;
#pragma unroll
    for (int q = 0; q < 4; ++q) {
      *(LAS f32x4*)(rp + q * TL_US + avo) = f32x4{a0[4 * q], a0[4 * q + 1], a0[4 * q + 2], a0[4 * q + 3]};
      *(LAS f32x4*)(rp + (4 + q) * TL_US + avo) = f32x4{a1[4 * q], a1[4 * q + 1], a1[4 * q + 2], a1[4 * q + 3]};
    }
    *(LAS f32x4*)(rp + 8 * TL_US + ((avo & 256) * 2 + (avo & 512) / 2 + (avo & 240))) = a2;
  }
  asm volatile("s_waitcnt lgkmcnt(0)\n\ts_barrier" ::: "memory");
  }
  const int tf = lane_id();
#pragma unroll
  for (int r = 0; r < 2; ++r) {
    PAD();
    const int it = wid * 64 + tf + r * 320;
    if (it < 576) {
      const int tok = it / 9, u = it - tok * 9;
      const lcp rp = (lcp)smem + (tok >> 5) * (9 * TL_US) + u * TL_US + (tok & 31) * 16;
      const float og = *(const LAS float*)((lcp)smem + TL_G + ((kq >> 1) * 64 + tok) * 4);
      const f32x4 lo = *(const LAS f32x4*)rp + *(const LAS f32x4*)(rp + 18 * TL_US) + og * *(const LAS f32x4*)((lcp)smem + TL_B2 + u * 32);
      const f32x4 hi = *(const LAS f32x4*)(rp + 512) + *(const LAS f32x4*)(rp + 18 * TL_US + 512) +
                       og * *(const LAS f32x4*)((lcp)smem + TL_B2 + u * 32 + 16);
      const u32x4 o4 = {cvtpk(lo[0], lo[1]), cvtpk(lo[2], lo[3]), cvtpk(hi[0], hi[1]), cvtpk(hi[2], hi[3])};
      *(u32x4*)(out + (size_t)(m64 * 64 + tok) * D + 72 * sl + 8 * u) = o4;
    }
  }
  TS(2, 6);
  return Cand{16 * rb + 2 * nt, cmask};
}

DI void tile_slow(const uint16_t* __restrict__ w2, const uint16_t* __restrict__ wo, const uint16_t* __restrict__ b2, char* __restrict__ ws,
                  uint16_t* __restrict__ out, int T) {
  const int lane = lane_id(), m64 = T >> 3, sl = T & 7;
  const int tok = m64 * 64 + lane, m32 = tok >> 5, l31 = lane & 31;
  const float og = 1.0f / (1.0f + __expf(((const float*)(ws + WS_G))[tok] + ((const float*)(ws + WS_G))[S + tok]));
#pragma unroll 1
  for (int u = 0; u < 9; ++u) {
    const int col = 72 * sl + 8 * u;
    float acc[8];
#pragma unroll
    for (int e = 0; e < 8; ++e) acc[e] = __uint_as_float((unsigned)b2[col + e] << 16);
    const int kq = (m64 & 1) * 2 + (sl & 1);
#pragma unroll 1
    for (int sh = 0; sh < 144; ++sh) {
      const int st = kq * 72 + (sh >> 1), h2 = sh & 1;
      const u32x4 hf = *(const u32x4*)(ws + WS_HT + (size_t)m32 * HT_ST + st * 1024 + h2 * 512 + l31 * 16);
#pragma unroll
      for (int j = 0; j < 8; ++j) {
        const u32x4 wv = *(const u32x4*)(w2 + (size_t)(16 * st + (j & 3) + 8 * (j >> 2) + 4 * h2) * D + col);
        const float hv = __uint_as_float((j & 1) ? (hf[j >> 1] & 0xffff0000u) : (hf[j >> 1] << 16));
#pragma unroll
        for (int e = 0; e < 8; ++e)
          acc[e] += hv * __uint_as_float((e & 1) ? (wv[e >> 1] & 0xffff0000u) : (wv[e >> 1] << 16));
      }
    }
#pragma unroll
    for (int e = 0; e < 8; ++e) acc[e] *= og;
#pragma unroll 1
    for (int q = 1; q < 4; ++q) {
      const char* pp = ws + WS_BP + (size_t)((((kq + q) & 3) * 64 + m32) * 8 + sl) * 5120 + (u >> 1) * 1024 + l31 * 16;
      u32x4 lo, hi;
      asm volatile("global_load_dwordx4 %0, %2, off sc1\n\tglobal_load_dwordx4 %1, %2, off offset:512 sc1\n\ts_waitcnt vmcnt(0)"
                   : "=&v"(lo), "=&v"(hi) : "v"(pp) : "memory");
      const unsigned w4[4] = {(u & 1) ? lo[2] : lo[0], (u & 1) ? lo[3] : lo[1], (u & 1) ? hi[2] : hi[0], (u & 1) ? hi[3] : hi[1]};
#pragma unroll
      for (int e = 0; e < 8; ++e) acc[e] += __uint_as_float((e & 1) ? (w4[e >> 1] & 0xffff0000u) : (w4[e >> 1] << 16));
    }
#pragma unroll 1
    for (int sh = 0; sh < 72; ++sh) {
      const int st = sh >> 1, h2 = sh & 1;
      const char* op = ws + WS_OT + ((size_t)m32 * 36 + st) * 1024 + h2 * 512 + l31 * (st < 32 ? 16 : 4);
      const int os = st < 32 ? 4 : 128;
      u32x4 of;
      asm volatile("global_load_dword %0, %4, off sc1\n\tglobal_load_dword %1, %5, off sc1\n\tglobal_load_dword %2, %6, off sc1\n\t"
                   "global_load_dword %3, %7, off sc1\n\ts_waitcnt vmcnt(0)" : "=&v"(of[0]), "=&v"(of[1]), "=&v"(of[2]), "=&v"(of[3])
                   : "v"(op), "v"(op + os), "v"(op + 2 * os), "v"(op + 3 * os) : "memory");
#pragma unroll
      for (int j = 0; j < 8; ++j) {
        const int row = st < 32 ? 18 * st + (j & 3) + 8 * (j >> 2) + 4 * h2 : 144 * (st - 32) + 18 * (4 * h2 + (j >> 1)) + 16 + (j & 1);
        const u32x4 wv = *(const u32x4*)(wo + (size_t)row * D + col);
        const float ov = __uint_as_float((j & 1) ? (of[j >> 1] & 0xffff0000u) : (of[j >> 1] << 16));
#pragma unroll
        for (int e = 0; e < 8; ++e)
          acc[e] += ov * __uint_as_float((e & 1) ? (wv[e >> 1] & 0xffff0000u) : (wv[e >> 1] << 16));
      }
    }
    const u32x4 o4 = {cvtpk(acc[0], acc[1]), cvtpk(acc[2], acc[3]), cvtpk(acc[4], acc[5]), cvtpk(acc[6], acc[7])};
    *(u32x4*)(out + (size_t)tok * D + col) = o4;
  }
}

__global__ __launch_bounds__(320, 4) void ag_kernel(const uint16_t* __restrict__ w2, const uint16_t* __restrict__ wo,
                                                    const uint16_t* __restrict__ b2, char* __restrict__ ws,
                                                    uint16_t* __restrict__ out) {
  extern __shared__ __attribute__((aligned(16))) char smem[];
  const int b = blockIdx.x;
  Cand cd;
  SEG(0);
  const int wid = __builtin_amdgcn_readfirstlane(threadIdx.x >> 6);
  if (b < 256) cd = attn_role(wo, ws, smem, b, wid);
  else cd = g2_role(w2, wo, b2, ws, out, smem, b - 256, wid);
  PAD();
  if (cd.mask != 0u) {
    const int lane = lane_id();
    unsigned fl = 0u;
    if (lane < 32 && ((cd.mask >> lane) & 1u)) fl = atom_add(ws + WS_SY + SY_F + (cd.base + lane) * 4, 0u);
    unsigned long long todo = __builtin_amdgcn_ballot_w64(fl != 0u);
#pragma unroll 1
    while (todo) {
      const int T = cd.base + __builtin_ctzll(todo);
      todo &= todo - 1;
      unsigned c = SY_ALL;
      if (lane < 2) c = atom_add(ws + WS_SY + ((T >> 3) * 2 + lane) * 128, 0u);
      else if (lane == 2) c = atom_add(ws + WS_SY + SY_B + (T >> 1) * 128, 0u) + (SY_ALL - 8u);
      if (__all(c == SY_ALL)) tile_slow(w2, wo, b2, ws, out, T);
    }
  }
}

__device__ __attribute__((used, noinline)) void chain_end() { asm volatile("s_branch 79f\n.Lpf_end:\n77:\n\ts_endpgm\n79:"); }

extern "C" void kernel_launch(void* const* d_in, const int* in_sizes, int n_in,
                              void* d_out, int out_size, void* d_ws, size_t ws_size,
                              hipStream_t stream) {
  const uint16_t* x = (const uint16_t*)d_in[0];
  const uint16_t* wqkv = (const uint16_t*)d_in[1];
  const uint16_t* wo = (const uint16_t*)d_in[2];
  const uint16_t* w1 = (const uint16_t*)d_in[4];
  const uint16_t* b1 = (const uint16_t*)d_in[5];
  const uint16_t* w2 = (const uint16_t*)d_in[6];
  const uint16_t* b2 = (const uint16_t*)d_in[7];
  const uint16_t* gw = (const uint16_t*)d_in[8];
  char* ws = (char*)d_ws;
  (void)hipFuncSetAttribute((const void*)k1_kernel, hipFuncAttributeMaxDynamicSharedMemorySize, K1_LDS);
  (void)hipFuncSetAttribute((const void*)ag_kernel, hipFuncAttributeMaxDynamicSharedMemorySize, AG_LDS);
  k1_kernel<<<256, 512, K1_LDS, stream>>>(x, wqkv, w1, b1, gw, ws);
  ag_kernel<<<512, 320, AG_LDS, stream>>>(w2, wo, b2, ws, (uint16_t*)d_out);
}
```
